```python
import torch

def _softmax(x, axis=-1):
    return torch.softmax(x, dim=axis)
def _gelu(x):
    return torch.nn.functional.gelu(x, approximate="tanh")
def _silu(x):
    return torch.nn.functional.silu(x)
def _sigmoid(x):
    return torch.sigmoid(x)
def _layernorm(x, axis=-1, eps=1e-5):
    m = x.mean(dim=axis, keepdim=True)
    v = x.var(dim=axis, keepdim=True, unbiased=False)
    return (x - m) / torch.sqrt(v + eps)
def _scatter_add(tgt, idx, src, axis=0):
    return tgt.clone().index_add_(axis, idx, src)
def _segment_sum(src, seg, n):
    out = torch.zeros((n,) + src.shape[1:], dtype=src.dtype)
    return out.index_add_(0, seg, src)
def _pad(x, pw):
    flat = tuple(v for pair in reversed(pw) for v in pair)
    return torch.nn.functional.pad(x, flat)

kernel_name = 'recurrent_scan__linear_attn_block__glu_gated_glu__prompt'

def setup_inputs():

    _w = (lambda a: (lambda t: t.to(torch.bfloat16) if t.is_floating_point() else t)(torch.as_tensor(a)))
    def _A_setup():
        B, S, D = 1, 2048, 640
        return {
            "x": _w((lambda *s: torch.randn(*s, dtype=torch.bfloat16) * 0.05)(B, S, D)),
            "wqkv": _w((lambda *s: torch.randn(*s, dtype=torch.bfloat16) * 0.05)(D, 3 * D)),
            "wo": _w((lambda *s: torch.randn(*s, dtype=torch.bfloat16) * 0.05)(D, D)),
        }
    _A_in = _A_setup()

    def _B_setup():
        B, S, D, F = 1, 2048, 640, 1728
        return {
            "x": _w((lambda *s: torch.randn(*s, dtype=torch.bfloat16) * 0.05)(B, S, D)),
            "w_gate": _w((lambda *s: torch.randn(*s, dtype=torch.bfloat16) * 0.05)(D, F)),
            "w_up": _w((lambda *s: torch.randn(*s, dtype=torch.bfloat16) * 0.05)(D, F)),
            "w_down": _w((lambda *s: torch.randn(*s, dtype=torch.bfloat16) * 0.05)(F, D)),
        }
    _B_in = _B_setup()

    return {
        "A_x": _A_in["x"],
        "A_wqkv": _A_in["wqkv"],
        "A_wo": _A_in["wo"],
        "B_x": _B_in["x"],
        "B_w_gate": _B_in["w_gate"],
        "B_w_up": _B_in["w_up"],
        "B_w_down": _B_in["w_down"],
    }

def reference(A_x, A_wqkv, A_wo, B_x, B_w_gate, B_w_up, B_w_down):
    def _A_ref(x, wqkv, wo):
        B, S, D = x.shape
        H = 32; Dh = D // H
        qkv = (lambda x, *d: x.permute(*d))((x @ wqkv).reshape(B, S, 3, H, Dh), 2, 0, 3, 1, 4)
        q, k, v = qkv[0], qkv[1], qkv[2]
        phi = lambda t: torch.where(t > 0, t + 1, torch.exp(t)) + 1e-6
        pq, pk = phi(q), phi(k)
        kv = torch.einsum("bhsd,bhse->bhde", pk, v)
        z = torch.einsum("bhsd->bhd", pk)
        num = torch.einsum("bhqd,bhde->bhqe", pq, kv)
        den = torch.einsum("bhqd,bhd->bhq", pq, z)[..., None] + 1e-6
        o = num / den
        return (lambda x, *d: x.permute(*d))(o, 0, 2, 1, 3).reshape(B, S, D) @ wo

    def _B_ref(x, w_gate, w_up, w_down):
        g = torch.einsum("bsd,df->bsf", x, w_gate)
        u = torch.einsum("bsd,df->bsf", x, w_up)
        h = _sigmoid(g) * u
        return torch.einsum("bsf,fd->bsd", h, w_down)

    x = A_x
    xn = _layernorm(x, axis=-1)
    s = _A_ref(xn, A_wqkv, A_wo)
    g = _silu(_B_ref(xn, B_w_gate, B_w_up, B_w_down))
    return x + s * g
```

```cpp
#include <hip/hip_runtime.h>
#include <hip/hip_bf16.h>
#include <stdint.h>

typedef unsigned short u16;
typedef __attribute__((ext_vector_type(8))) short s16x8;
typedef __attribute__((ext_vector_type(4))) short s16x4;
typedef __attribute__((ext_vector_type(4))) float f32x4;
typedef __attribute__((ext_vector_type(16))) float f32x16;
typedef __attribute__((ext_vector_type(2))) unsigned u32x2;
typedef __attribute__((ext_vector_type(4))) unsigned u32x4;
typedef __attribute__((address_space(3))) s16x4* lds_s16x4_ptr;
typedef __attribute__((address_space(1))) unsigned gu32;

#define RLX_AGENT __ATOMIC_RELAXED, __HIP_MEMORY_SCOPE_AGENT

constexpr int S_ = 2048, D_ = 640, F_ = 1728, NQKV_ = 1920;

constexpr size_t OFF_CNT   = 0;
constexpr size_t OFF_XN    = 4096;
constexpr size_t SZ_XN     = (size_t)S_ * D_ * 2;
constexpr size_t OFF_H     = OFF_XN + SZ_XN;
constexpr size_t SZ_H      = (size_t)S_ * F_ * 2;
constexpr size_t OFF_PQ    = OFF_H + SZ_H;
constexpr int    PQ_LD     = 648;
constexpr size_t SZ_PQ     = (size_t)32 * 64 * PQ_LD * 2;
constexpr size_t OFF_WC    = OFF_PQ + SZ_PQ;
constexpr size_t SZ_WC     = (size_t)8 * 74 * 5 * 1024;
constexpr size_t OFF_KVP   = OFF_WC + SZ_WC;
constexpr size_t SZ_KVP    = (size_t)16 * 32 * 504 * 4;
constexpr size_t OFF_KVF   = OFF_KVP + SZ_KVP;
constexpr size_t SZ_KVF    = 65536;

__device__ __forceinline__ float bf2f(u16 v) { return __uint_as_float(((unsigned)v) << 16); }
__device__ __forceinline__ u16 f2bf(float f) { return __builtin_bit_cast(u16, (__bf16)f); }
__device__ __forceinline__ float bfr(float f) { return bf2f(f2bf(f)); }
__device__ __forceinline__ unsigned pack2(float a, float b) { return (unsigned)f2bf(a) | ((unsigned)f2bf(b) << 16); }
__device__ __forceinline__ float phi_f(float t) {
  float p = t > 0.f ? bfr(t + 1.0f) : bfr(__expf(t));
  return bfr(p + 1e-6f);
}
__device__ __forceinline__ int aswz(int row) { int x = (row >> 2) & 3; return (((x ^ (x >> 1)) & 1) << 1) | (x >> 1); }

#define WAIT_V(n) asm volatile("s_waitcnt vmcnt(" #n ")" ::: "memory")
#define BAR __builtin_amdgcn_s_barrier()

__global__ __launch_bounds__(256) void k0_prep(const u16* __restrict__ x, const u16* __restrict__ wo,
                                               const u16* __restrict__ wdown, char* __restrict__ ws) {
  const int tid = threadIdx.x;
  if (blockIdx.x < 256) {
    if (blockIdx.x == 0 && tid < 4) ((unsigned*)(ws + OFF_CNT))[tid * 64] = 0u;
    const int row = blockIdx.x * 8 + (tid >> 5);
    const int l32 = tid & 31;
    const u16* xr = x + (size_t)row * D_;
    float v[20];
    float sum = 0.f;
#pragma unroll
    for (int i = 0; i < 5; ++i) {
      u32x2 w = *(const u32x2*)(xr + (l32 + 32 * i) * 4);
      v[4 * i + 0] = __uint_as_float(w[0] << 16);
      v[4 * i + 1] = __uint_as_float(w[0] & 0xffff0000u);
      v[4 * i + 2] = __uint_as_float(w[1] << 16);
      v[4 * i + 3] = __uint_as_float(w[1] & 0xffff0000u);
      sum += v[4 * i] + v[4 * i + 1] + v[4 * i + 2] + v[4 * i + 3];
    }
#pragma unroll
    for (int o = 16; o >= 1; o >>= 1) sum += __shfl_xor(sum, o);
    const float mean = sum * (1.0f / D_);
    float sq = 0.f;
#pragma unroll
    for (int i = 0; i < 20; ++i) { float d = v[i] - mean; sq += d * d; }
#pragma unroll
    for (int o = 16; o >= 1; o >>= 1) sq += __shfl_xor(sq, o);
    const float mb = bfr(mean);
    const float vb = bfr(sq * (1.0f / D_));
    const float sd = bfr(sqrtf(bfr(vb + 1e-5f)));
    u16* xn = (u16*)(ws + OFF_XN);
    const int mt = row >> 7, r = row & 127, sw = aswz(r);
#pragma unroll
    for (int i = 0; i < 5; ++i) {
      const int k = (l32 + 32 * i) * 4;
      float o0 = bfr(v[4 * i + 0] - mb) / sd, o1 = bfr(v[4 * i + 1] - mb) / sd;
      float o2 = bfr(v[4 * i + 2] - mb) / sd, o3 = bfr(v[4 * i + 3] - mb) / sd;
      u32x2 w; w[0] = pack2(o0, o1); w[1] = pack2(o2, o3);
      const int kt = k >> 5, ch = (k >> 3) & 3, hf = (k >> 2) & 1;
      size_t off = ((size_t)(mt * 20 + kt) * 128 + r) * 32 + ((ch ^ sw) << 3) + (hf << 2);
      *(u32x2*)(xn + off) = w;
    }
  } else {
    const int t = (blockIdx.x - 256) * 256 + tid;
    if (t < 296 * 640) {
      const int n = t % 640, koct = t / 640;
      const u16* src = (koct < 216) ? (wdown + (size_t)(koct * 8) * D_ + n) : (wo + (size_t)((koct - 216) * 8) * D_ + n);
      u16 e[8];
#pragma unroll
      for (int j = 0; j < 8; ++j) e[j] = src[(size_t)j * D_];
      u32x4 w;
      w[0] = e[0] | ((unsigned)e[1] << 16); w[1] = e[2] | ((unsigned)e[3] << 16);
      w[2] = e[4] | ((unsigned)e[5] << 16); w[3] = e[6] | ((unsigned)e[7] << 16);
      const int nt = n / 80, nn = n % 80, nf = nn >> 4, c = nn & 15, kt = koct >> 2, g = koct & 3;
      *(u32x4*)(ws + OFF_WC + ((size_t)((nt * 74 + kt) * 5 + nf) * 64 + g * 16 + c) * 16) = w;
    }
  }
}

constexpr int K1_STAGE = 32768;
constexpr int K1_NS = 4;

template <int TYPE>
__device__ __forceinline__ void k1_body(const u16* __restrict__ wA, const u16* __restrict__ wB, char* __restrict__ ws,
                                        int mt, int j, char* smem) {
  constexpr int NCB = TYPE == 0 ? 6 : 5;
  constexpr int NFR = TYPE == 0 ? 6 : 5;
  constexpr int LDW = TYPE == 0 ? F_ : NQKV_;
  const int tid = threadIdx.x, lane = tid & 63;
  const int wave = __builtin_amdgcn_readfirstlane(tid >> 6);
  const int wm = wave >> 2, wn = wave & 3;
  const int g = lane >> 4, c = lane & 15;

  const char* asrc = ws + OFF_XN + (size_t)mt * 20 * 8192 + wave * 1024 + lane * 16;
  const u16* bsrc[3];
#pragma unroll
  for (int t = 0; t < 3; ++t) {
    int ii = wave + 8 * t; if (ii >= NCB * 4) ii = wave;
    const int cb = ii >> 2, krow = (ii & 3) * 8 + (lane >> 3), u = lane & 7;
    const int sk = ((krow >> 1) & 1) | (((krow >> 3) & 1) << 1);
    const int ch = (u >> 1) ^ sk;
    const int lc = cb * 64 + ch * 16 + (u & 1) * 8;
    const u16* p;
    if (TYPE == 0) p = (lc < 192) ? (wA + j * 192 + lc) : (wB + j * 192 + (lc - 192));
    else if (TYPE == 1) p = wA + j * 320 + lc;
    else p = (lc < 160) ? (wA + 640 + j * 160 + lc) : (wA + 1280 + j * 160 + (lc - 160));
    bsrc[t] = p + (size_t)krow * LDW;
  }
  const bool has3 = (NCB == 6) || (wave < 4);

  auto issue = [&](int kt) {
    char* sb = smem + (kt & (K1_NS - 1)) * K1_STAGE;
    __builtin_amdgcn_global_load_lds((const unsigned*)(asrc + (size_t)kt * 8192), (unsigned*)(sb + wave * 1024), 16, 0, 0);
    __builtin_amdgcn_global_load_lds((const unsigned*)(bsrc[0] + (size_t)kt * 32 * LDW), (unsigned*)(sb + 8192 + wave * 1024), 16, 0, 0);
    __builtin_amdgcn_global_load_lds((const unsigned*)(bsrc[1] + (size_t)kt * 32 * LDW), (unsigned*)(sb + 8192 + (wave + 8) * 1024), 16, 0, 0);
    if (has3) __builtin_amdgcn_global_load_lds((const unsigned*)(bsrc[2] + (size_t)kt * 32 * LDW), (unsigned*)(sb + 8192 + (wave + 16) * 1024), 16, 0, 0);
  };

  const int xoff = (wm * 64 + c) * 64 + ((g ^ aswz(c)) << 4);
  int woff[NFR];
  {
    const int q = (lane & 15) >> 2, p = lane & 3;
    const int s = (q >> 1) | ((g & 1) << 1);
    const int lo = (8 * g + q) * 128 + (s << 5) + p * 8;
#pragma unroll
    for (int i = 0; i < NFR; ++i) {
      const int nf = TYPE == 0 ? (i < 3 ? 3 * wn + i : 12 + 3 * wn + (i - 3)) : 5 * wn + i;
      woff[i] = 8192 + (nf >> 2) * 4096 + (lo ^ ((nf & 3) << 5));
    }
  }

  f32x4 acc[4][NFR];
#pragma unroll
  for (int a = 0; a < 4; ++a)
#pragma unroll
    for (int b = 0; b < NFR; ++b) acc[a][b] = (f32x4){0.f, 0.f, 0.f, 0.f};

  issue(0); issue(1); issue(2);
#pragma unroll 4
  for (int kt = 0; kt < 20; ++kt) {
    if (kt < 18) { if (has3) WAIT_V(8); else WAIT_V(6); }
    else if (kt == 18) { if (has3) WAIT_V(4); else WAIT_V(3); }
    else WAIT_V(0);
    BAR;
    if (kt + 3 < 20) issue(kt + 3);
    const char* sb = smem + (kt & (K1_NS - 1)) * K1_STAGE;
    s16x8 xf[4], wf[NFR];
#pragma unroll
    for (int a = 0; a < 4; ++a) xf[a] = *(const s16x8*)(sb + xoff + a * 1024);
#pragma unroll
    for (int b = 0; b < NFR; ++b) {
      s16x4 lo = __builtin_amdgcn_ds_read_tr16_b64_v4i16((lds_s16x4_ptr)(sb + woff[b]));
      s16x4 hi = __builtin_amdgcn_ds_read_tr16_b64_v4i16((lds_s16x4_ptr)(sb + woff[b] + 512));
      wf[b] = __builtin_shufflevector(lo, hi, 0, 1, 2, 3, 4, 5, 6, 7);
    }
#pragma unroll
    for (int a = 0; a < 4; ++a)
#pragma unroll
      for (int b = 0; b < NFR; ++b) {
        if (TYPE == 2) acc[a][b] = __builtin_amdgcn_mfma_f32_16x16x32_bf16(xf[a], wf[b], acc[a][b], 0, 0, 0);
        else           acc[a][b] = __builtin_amdgcn_mfma_f32_16x16x32_bf16(wf[b], xf[a], acc[a][b], 0, 0, 0);
      }
  }

  if (TYPE == 0) {
    u16* H = (u16*)(ws + OFF_H);
#pragma unroll
    for (int a = 0; a < 4; ++a) {
      const int tok = mt * 128 + wm * 64 + a * 16 + c;
      const int mt64 = tok >> 6, r64 = tok & 63, sw = aswz(r64);
#pragma unroll
      for (int b = 0; b < 3; ++b) {
        const int f = j * 192 + wn * 48 + b * 16 + 4 * g;
        float hv[4];
#pragma unroll
        for (int r = 0; r < 4; ++r) {
          const float gv = bfr(acc[a][b][r]), uv = bfr(acc[a][b + 3][r]);
          const float sg = bfr(1.0f / (1.0f + __expf(-gv)));
          hv[r] = sg * uv;
        }
        u32x2 w; w[0] = pack2(hv[0], hv[1]); w[1] = pack2(hv[2], hv[3]);
        const int kt = f >> 5, ch = (f >> 3) & 3, hf = (f >> 2) & 1;
        *(u32x2*)(H + ((size_t)(mt64 * 54 + kt) * 64 + r64) * 32 + ((ch ^ sw) << 3) + (hf << 2)) = w;
      }
    }
  } else if (TYPE == 1) {
    u16* PQ = (u16*)(ws + OFF_PQ);
#pragma unroll
    for (int a = 0; a < 4; ++a) {
      const int tok = mt * 128 + wm * 64 + a * 16 + c;
#pragma unroll
      for (int b = 0; b < 5; ++b) {
        const int col = j * 320 + wn * 80 + b * 16 + 4 * g;
        float p0 = phi_f(bfr(acc[a][b][0])), p1 = phi_f(bfr(acc[a][b][1]));
        float p2 = phi_f(bfr(acc[a][b][2])), p3 = phi_f(bfr(acc[a][b][3]));
        u32x2 w; w[0] = pack2(p0, p1); w[1] = pack2(p2, p3);
        *(u32x2*)(PQ + (size_t)tok * PQ_LD + col) = w;
      }
    }
  } else {
    BAR;
#pragma unroll
    for (int a = 0; a < 4; ++a)
#pragma unroll
      for (int b = 0; b < 5; ++b) {
        const int col = wn * 80 + b * 16 + c;
        float v0 = bfr(acc[a][b][0]), v1 = bfr(acc[a][b][1]), v2 = bfr(acc[a][b][2]), v3 = bfr(acc[a][b][3]);
        if (wn < 2) { v0 = phi_f(v0); v1 = phi_f(v1); v2 = phi_f(v2); v3 = phi_f(v3); }
        u32x2 w; w[0] = pack2(v0, v1); w[1] = pack2(v2, v3);
        *(u32x2*)(smem + col * 272 + (wm * 64 + a * 16 + 4 * g) * 2) = w;
      }
    __syncthreads();
    const int r32 = lane & 31, hh = lane >> 5;
    f32x16 akv, az;
#pragma unroll
    for (int i = 0; i < 16; ++i) { akv[i] = 0.f; az[i] = 0.f; }
    s16x8 ones;
#pragma unroll
    for (int i = 0; i < 8; ++i) ones[i] = (short)0x3F80;
#pragma unroll
    for (int s = 0; s < 8; ++s) {
      s16x8 af = *(const s16x8*)(smem + (20 * wave + r32) * 272 + (16 * s + 8 * hh) * 2);
      s16x8 bfv = *(const s16x8*)(smem + (160 + 20 * wave + r32) * 272 + (16 * s + 8 * hh) * 2);
      akv = __builtin_amdgcn_mfma_f32_32x32x16_bf16(af, bfv, akv, 0, 0, 0);
      az = __builtin_amdgcn_mfma_f32_32x32x16_bf16(af, ones, az, 0, 0, 0);
    }
    const int head = j * 8 + wave;
    gu32* P = (gu32*)(ws + OFF_KVP) + (size_t)(mt * 32 + head) * 504;
    if (r32 <= 20) {
#pragma unroll
      for (int q = 0; q < 3; ++q) {
        const int d0 = 8 * q + 4 * hh;
        if (d0 < 20) {
#pragma unroll
          for (int r = 0; r < 4; ++r) {
            const float val = (r32 == 20) ? az[4 * q + r] : akv[4 * q + r];
            __hip_atomic_store(P + r32 * 24 + d0 + r, __float_as_uint(val), RLX_AGENT);
          }
        }
      }
    }
    asm volatile("s_waitcnt vmcnt(0)" ::: "memory");
    __syncthreads();
    volatile int* flag = (volatile int*)(smem + 98304);
    if (tid == 0) {
      unsigned tk = __hip_atomic_fetch_add((gu32*)(ws + OFF_CNT) + j * 64, 1u, RLX_AGENT);
      if (tk == 15u) {
        __builtin_amdgcn_fence(__ATOMIC_ACQUIRE, "agent");
        asm volatile("s_waitcnt vmcnt(0)" ::: "memory");
      }
      *flag = (int)tk;
    }
    __syncthreads();
    if (*flag == 15) {
      const float* Pf = (const float*)(ws + OFF_KVP);
#pragma unroll
      for (int u = 0; u < 2; ++u) {
        const int idx = tid + 512 * u;
        const int hl = idx >> 7, fr = (idx >> 6) & 1, ln = idx & 63, gg = ln >> 4, cc = ln & 15;
        const int e = 16 * fr + cc, d0 = 8 * gg;
        float s0[8];
#pragma unroll
        for (int i = 0; i < 8; ++i) s0[i] = 0.f;
        if (e <= 20 && d0 < 20) {
          for (int m = 0; m < 16; ++m) {
            const float* src = Pf + (size_t)(m * 32 + j * 8 + hl) * 504 + e * 24 + d0;
            f32x4 a0 = *(const f32x4*)src;
            s0[0] += a0[0]; s0[1] += a0[1]; s0[2] += a0[2]; s0[3] += a0[3];
            if (d0 < 16) {
              f32x4 a1 = *(const f32x4*)(src + 4);
              s0[4] += a1[0]; s0[5] += a1[1]; s0[6] += a1[2]; s0[7] += a1[3];
            }
          }
        }
        u32x4 w;
        w[0] = pack2(s0[0], s0[1]); w[1] = pack2(s0[2], s0[3]); w[2] = pack2(s0[4], s0[5]); w[3] = pack2(s0[6], s0[7]);
        *(u32x4*)(ws + OFF_KVF + ((size_t)((j * 8 + hl) * 2 + fr) * 64 + ln) * 16) = w;
      }
    }
  }
}

__global__ __launch_bounds__(512, 2) void k1_gemm(const u16* __restrict__ wqkv, const u16* __restrict__ wg,
                                                   const u16* __restrict__ wu, char* __restrict__ ws) {
  extern __shared__ __attribute__((aligned(16))) char smem[];
  const int bid = blockIdx.x;
  const int p = (bid & 7) * 30 + (bid >> 3);
  const int mh = p / 120, rem = p % 120, nt = rem >> 3, mt = mh * 8 + (rem & 7);
  if (nt < 4) k1_body<2>(wqkv, wqkv, ws, mt, nt, smem);
  else if (nt < 6) k1_body<1>(wqkv, wqkv, ws, mt, nt - 4, smem);
  else k1_body<0>(wg, wu, ws, mt, nt - 6, smem);
}

constexpr int K2_OT = 64 * PQ_LD * 2;
constexpr int K2_STAGE = 18432;
constexpr int K2_LDS = K2_OT + 4 * K2_STAGE;

__global__ __launch_bounds__(256, 1) void k2_gemm(const u16* __restrict__ x, const char* __restrict__ ws, u16* __restrict__ out) {
  extern __shared__ __attribute__((aligned(16))) char smem[];
  const int tid = threadIdx.x, lane = tid & 63;
  const int wave = __builtin_amdgcn_readfirstlane(tid >> 6);
  const int kg = wave >> 1, wm = wave & 1;
  const int g = lane >> 4, c = lane & 15;
  const int bid = blockIdx.x;
  const int mt = (bid & 7) * 4 + (bid >> 6), nt = (bid >> 3) & 7;

  char* ring = smem + K2_OT;
  const char* hsrc = ws + OFF_H + (size_t)mt * 54 * 4096 + lane * 16;
  const char* wsrc = ws + OFF_WC + (size_t)nt * 74 * 5120 + lane * 16;

  s16x8 kvf[8][2];
#pragma unroll
  for (int hh = 0; hh < 8; ++hh)
#pragma unroll
    for (int fr = 0; fr < 2; ++fr)
      kvf[hh][fr] = *(const s16x8*)(ws + OFF_KVF + ((size_t)((wave * 8 + hh) * 2 + fr) * 64 + lane) * 16);

  {
    const char* psrc = ws + OFF_PQ + (size_t)mt * K2_OT + lane * 16;
    for (int i = wave; i < 81; i += 4)
      __builtin_amdgcn_global_load_lds((const unsigned*)(psrc + i * 1024), (unsigned*)(smem + i * 1024), 16, 0, 0);
  }

  auto issue = [&](int it) {
    char* sb = ring + (it & 3) * K2_STAGE;
    if (it < 27) {
      __builtin_amdgcn_global_load_lds((const unsigned*)(hsrc + (size_t)it * 8192 + wave * 1024), (unsigned*)(sb + wave * 1024), 16, 0, 0);
      __builtin_amdgcn_global_load_lds((const unsigned*)(hsrc + (size_t)it * 8192 + (wave + 4) * 1024), (unsigned*)(sb + (wave + 4) * 1024), 16, 0, 0);
    }
    const char* w = wsrc + (size_t)it * 10240;
    __builtin_amdgcn_global_load_lds((const unsigned*)(w + wave * 1024), (unsigned*)(sb + 8192 + wave * 1024), 16, 0, 0);
    __builtin_amdgcn_global_load_lds((const unsigned*)(w + (wave + 4) * 1024), (unsigned*)(sb + 8192 + (wave + 4) * 1024), 16, 0, 0);
    if (wave < 2)
      __builtin_amdgcn_global_load_lds((const unsigned*)(w + (wave + 8) * 1024), (unsigned*)(sb + 8192 + (wave + 8) * 1024), 16, 0, 0);
  };

  f32x4 acc1[2][5], acc2[2][5];
#pragma unroll
  for (int a = 0; a < 2; ++a)
#pragma unroll
    for (int b = 0; b < 5; ++b) { acc1[a][b] = (f32x4){0.f, 0.f, 0.f, 0.f}; acc2[a][b] = (f32x4){0.f, 0.f, 0.f, 0.f}; }

  const int xoff = kg * 4096 + (wm * 32 + c) * 64 + ((g ^ aswz(c)) << 4);
  const int woff = 8192 + kg * 5120 + lane * 16;

  issue(0); issue(1); issue(2);
  for (int it = 0; it < 27; ++it) {
    if (it < 25) { if (wave < 2) WAIT_V(10); else WAIT_V(8); }
    else { if (wave < 2) WAIT_V(6); else WAIT_V(4); }
    BAR;
    issue(it + 3);
    const char* sb = ring + (it & 3) * K2_STAGE;
    s16x8 xf[2], wf[5];
#pragma unroll
    for (int a = 0; a < 2; ++a) xf[a] = *(const s16x8*)(sb + xoff + a * 1024);
#pragma unroll
    for (int b = 0; b < 5; ++b) wf[b] = *(const s16x8*)(sb + woff + b * 1024);
#pragma unroll
    for (int a = 0; a < 2; ++a)
#pragma unroll
      for (int b = 0; b < 5; ++b) acc1[a][b] = __builtin_amdgcn_mfma_f32_16x16x32_bf16(wf[b], xf[a], acc1[a][b], 0, 0, 0);
  }

#pragma unroll
  for (int hh = 0; hh < 8; ++hh) {
    const int head = wave * 8 + hh;
#pragma unroll
    for (int tg = 0; tg < 4; ++tg) {
      char* rowp = smem + (tg * 16 + c) * (PQ_LD * 2) + head * 40;
      u32x2 lo = *(const u32x2*)(rowp + 16 * g);
      u32x2 hi = *(const u32x2*)(rowp + 16 * g + 8);
      if (g >= 3) { lo[0] = 0u; lo[1] = 0u; }
      if (g >= 2) { hi[0] = 0u; hi[1] = 0u; }
      u32x4 pk4; pk4[0] = lo[0]; pk4[1] = lo[1]; pk4[2] = hi[0]; pk4[3] = hi[1];
      s16x8 pf = __builtin_bit_cast(s16x8, pk4);
      f32x4 z4 = (f32x4){0.f, 0.f, 0.f, 0.f};
      f32x4 d0 = __builtin_amdgcn_mfma_f32_16x16x32_bf16(kvf[hh][0], pf, z4, 0, 0, 0);
      f32x4 d1 = __builtin_amdgcn_mfma_f32_16x16x32_bf16(kvf[hh][1], pf, z4, 0, 0, 0);
      const float denr = __shfl(d1[0], 16 + c);
      const float den = bfr(bfr(denr) + 1e-6f);
      u32x2 w0, w1;
      w0[0] = pack2(bfr(d0[0]) / den, bfr(d0[1]) / den); w0[1] = pack2(bfr(d0[2]) / den, bfr(d0[3]) / den);
      w1[0] = pack2(bfr(d1[0]) / den, bfr(d1[1]) / den); w1[1] = pack2(bfr(d1[2]) / den, bfr(d1[3]) / den);
      *(u32x2*)(rowp + 8 * g) = w0;
      if (g == 0) *(u32x2*)(rowp + 32) = w1;
    }
  }
  asm volatile("s_waitcnt lgkmcnt(0)" ::: "memory");

  for (int it = 27; it < 37; ++it) {
    if (it < 35) { if (wave < 2) WAIT_V(6); else WAIT_V(4); }
    else if (it == 35) { if (wave < 2) WAIT_V(3); else WAIT_V(2); }
    else WAIT_V(0);
    BAR;
    if (it + 3 < 37) issue(it + 3);
    const char* sb = ring + (it & 3) * K2_STAGE;
    const int k0 = (it - 27) * 64 + kg * 32 + 8 * g;
    s16x8 xf[2], wf[5];
#pragma unroll
    for (int a = 0; a < 2; ++a) xf[a] = *(const s16x8*)(smem + (wm * 32 + a * 16 + c) * (PQ_LD * 2) + k0 * 2);
#pragma unroll
    for (int b = 0; b < 5; ++b) wf[b] = *(const s16x8*)(sb + woff + b * 1024);
#pragma unroll
    for (int a = 0; a < 2; ++a)
#pragma unroll
      for (int b = 0; b < 5; ++b) acc2[a][b] = __builtin_amdgcn_mfma_f32_16x16x32_bf16(wf[b], xf[a], acc2[a][b], 0, 0, 0);
  }

  __syncthreads();
  f32x4* red = (f32x4*)ring;
  if (kg == 1) {
#pragma unroll
    for (int a = 0; a < 2; ++a)
#pragma unroll
      for (int b = 0; b < 5; ++b) {
        red[(wm * 20 + a * 5 + b) * 64 + lane] = acc1[a][b];
        red[(wm * 20 + 10 + a * 5 + b) * 64 + lane] = acc2[a][b];
      }
  }
  __syncthreads();
  if (kg == 0) {
#pragma unroll
    for (int a = 0; a < 2; ++a) {
      const int tok = mt * 64 + wm * 32 + a * 16 + c;
#pragma unroll
      for (int b = 0; b < 5; ++b) {
        const f32x4 r1 = red[(wm * 20 + a * 5 + b) * 64 + lane];
        const f32x4 r2 = red[(wm * 20 + 10 + a * 5 + b) * 64 + lane];
        const int n = nt * 80 + b * 16 + 4 * g;
        const u32x2 xw = *(const u32x2*)(x + (size_t)tok * D_ + n);
        float xv[4];
        xv[0] = __uint_as_float(xw[0] << 16); xv[1] = __uint_as_float(xw[0] & 0xffff0000u);
        xv[2] = __uint_as_float(xw[1] << 16); xv[3] = __uint_as_float(xw[1] & 0xffff0000u);
        float o[4];
#pragma unroll
        for (int r = 0; r < 4; ++r) {
          const float y = bfr(acc1[a][b][r] + r1[r]);
          const float gs = bfr(y / (1.0f + __expf(-y)));
          const float s = bfr(acc2[a][b][r] + r2[r]);
          o[r] = xv[r] + bfr(s * gs);
        }
        u32x2 w; w[0] = pack2(o[0], o[1]); w[1] = pack2(o[2], o[3]);
        *(u32x2*)(out + (size_t)tok * D_ + n) = w;
      }
    }
  }
}

extern "C" void kernel_launch(void* const* d_in, const int* in_sizes, int n_in,
                              void* d_out, int out_size, void* d_ws, size_t ws_size,
                              hipStream_t stream) {
  const u16* x = (const u16*)d_in[0];
  const u16* wqkv = (const u16*)d_in[1];
  const u16* wo = (const u16*)d_in[2];
  const u16* wg = (const u16*)d_in[4];
  const u16* wu = (const u16*)d_in[5];
  const u16* wd = (const u16*)d_in[6];
  char* ws = (char*)d_ws;
  u16* out = (u16*)d_out;

  hipFuncSetAttribute((const void*)k1_gemm, hipFuncAttributeMaxDynamicSharedMemorySize, K1_NS * K1_STAGE);
  hipFuncSetAttribute((const void*)k2_gemm, hipFuncAttributeMaxDynamicSharedMemorySize, K2_LDS);

  k0_prep<<<256 + 740, 256, 0, stream>>>(x, wo, wd, ws);
  k1_gemm<<<240, 512, K1_NS * K1_STAGE, stream>>>(wqkv, wg, wu, ws);
  k2_gemm<<<256, 256, K2_LDS, stream>>>(x, ws, out);
}
```

```cpp
#include <hip/hip_runtime.h>
#include <hip/hip_bf16.h>
#include <stdint.h>

typedef unsigned short u16;
typedef __attribute__((ext_vector_type(8))) short s16x8;
typedef __attribute__((ext_vector_type(4))) short s16x4;
typedef __attribute__((ext_vector_type(4))) float f32x4;
typedef __attribute__((ext_vector_type(16))) float f32x16;
typedef __attribute__((ext_vector_type(2))) unsigned u32x2;
typedef __attribute__((ext_vector_type(4))) unsigned u32x4;
typedef __attribute__((address_space(3))) s16x4* lds_s16x4_ptr;
typedef __attribute__((address_space(1))) unsigned gu32;

#define RLX_AGENT __ATOMIC_RELAXED, __HIP_MEMORY_SCOPE_AGENT

constexpr int S_ = 2048, D_ = 640, F_ = 1728, NQKV_ = 1920;

constexpr size_t OFF_CNT   = 0;
constexpr size_t OFF_XN    = 4096;
constexpr size_t SZ_XN     = (size_t)S_ * D_ * 2;
constexpr size_t OFF_H     = OFF_XN + SZ_XN;
constexpr size_t SZ_H      = (size_t)S_ * F_ * 2;
constexpr size_t OFF_PQ    = OFF_H + SZ_H;
constexpr int    PQ_LD     = 648;
constexpr size_t SZ_PQ     = (size_t)32 * 64 * PQ_LD * 2;
constexpr size_t OFF_WC    = OFF_PQ + SZ_PQ;
constexpr size_t SZ_WC     = (size_t)8 * 74 * 5 * 1024;
constexpr size_t OFF_KVP   = OFF_WC + SZ_WC;
constexpr size_t SZ_KVP    = (size_t)16 * 32 * 504 * 4;
constexpr size_t OFF_KVF   = OFF_KVP + SZ_KVP;
constexpr size_t SZ_KVF    = 65536;

__device__ __forceinline__ float bf2f(u16 v) { return __uint_as_float(((unsigned)v) << 16); }
__device__ __forceinline__ u16 f2bf(float f) { return __builtin_bit_cast(u16, (__bf16)f); }
__device__ __forceinline__ float bfr(float f) { return bf2f(f2bf(f)); }
__device__ __forceinline__ unsigned pack2(float a, float b) { return (unsigned)f2bf(a) | ((unsigned)f2bf(b) << 16); }
__device__ __forceinline__ float phi_f(float t) {
  float p = t > 0.f ? bfr(t + 1.0f) : bfr(__expf(t));
  return bfr(p + 1e-6f);
}
__device__ __forceinline__ int aswz(int row) { int x = (row >> 2) & 3; return (((x ^ (x >> 1)) & 1) << 1) | (x >> 1); }

#define WAIT_V(n) asm volatile("s_waitcnt vmcnt(" #n ")" ::: "memory")
#define WAIT_L0 asm volatile("s_waitcnt lgkmcnt(0)" ::: "memory")
#define BAR __builtin_amdgcn_s_barrier()
#define SCHED __builtin_amdgcn_sched_barrier(0)
__device__ __forceinline__ float frcp(float v) { return __builtin_amdgcn_rcpf(v); }

__global__ __launch_bounds__(256) void k0_prep(const u16* __restrict__ x, char* __restrict__ ws) {
  const int tid = threadIdx.x;
  {
    if (blockIdx.x == 0 && tid < 4) ((unsigned*)(ws + OFF_CNT))[tid * 64] = 0u;
    const int row = blockIdx.x * 8 + (tid >> 5);
    const int l32 = tid & 31;
    const u16* xr = x + (size_t)row * D_;
    float v[20];
    float sum = 0.f;
#pragma unroll
    for (int i = 0; i < 5; ++i) {
      u32x2 w = *(const u32x2*)(xr + (l32 + 32 * i) * 4);
      v[4 * i + 0] = __uint_as_float(w[0] << 16);
      v[4 * i + 1] = __uint_as_float(w[0] & 0xffff0000u);
      v[4 * i + 2] = __uint_as_float(w[1] << 16);
      v[4 * i + 3] = __uint_as_float(w[1] & 0xffff0000u);
      sum += v[4 * i] + v[4 * i + 1] + v[4 * i + 2] + v[4 * i + 3];
    }
#pragma unroll
    for (int o = 16; o >= 1; o >>= 1) sum += __shfl_xor(sum, o);
    const float mean = sum * (1.0f / D_);
    float sq = 0.f;
#pragma unroll
    for (int i = 0; i < 20; ++i) { float d = v[i] - mean; sq += d * d; }
#pragma unroll
    for (int o = 16; o >= 1; o >>= 1) sq += __shfl_xor(sq, o);
    const float mb = bfr(mean);
    const float vb = bfr(sq * (1.0f / D_));
    const float sd = bfr(sqrtf(bfr(vb + 1e-5f)));
    u16* xn = (u16*)(ws + OFF_XN);
    const int mt = row >> 7, r = row & 127, sw = aswz(r);
#pragma unroll
    for (int i = 0; i < 5; ++i) {
      const int k = (l32 + 32 * i) * 4;
      float o0 = bfr(v[4 * i + 0] - mb) / sd, o1 = bfr(v[4 * i + 1] - mb) / sd;
      float o2 = bfr(v[4 * i + 2] - mb) / sd, o3 = bfr(v[4 * i + 3] - mb) / sd;
      u32x2 w; w[0] = pack2(o0, o1); w[1] = pack2(o2, o3);
      const int kt = k >> 5, ch = (k >> 3) & 3, hf = (k >> 2) & 1;
      size_t off = ((size_t)(mt * 20 + kt) * 128 + r) * 32 + ((ch ^ sw) << 3) + (hf << 2);
      *(u32x2*)(xn + off) = w;
    }
  }
}

constexpr int K1_STAGE = 32768;
constexpr int K1_NS = 4;

template <int TYPE>
__device__ __forceinline__ void k1_body(const u16* __restrict__ wA, const u16* __restrict__ wB, char* __restrict__ ws,
                                        int mt, int j, char* smem) {
  constexpr int NCB = TYPE == 0 ? 6 : 5;
  constexpr int NFR = TYPE == 0 ? 6 : 5;
  constexpr int LDW = TYPE == 0 ? F_ : NQKV_;
  const int tid = threadIdx.x, lane = tid & 63;
  const int wave = __builtin_amdgcn_readfirstlane(tid >> 6);
  const int wm = wave >> 2, wn = wave & 3;
  const int g = lane >> 4, c = lane & 15;

  const char* asrc = ws + OFF_XN + (size_t)mt * 20 * 8192 + wave * 1024 + lane * 16;
  const u16* bsrc[3];
#pragma unroll
  for (int t = 0; t < 3; ++t) {
    int ii = wave + 8 * t; if (ii >= NCB * 4) ii = wave;
    const int cb = ii >> 2, krow = (ii & 3) * 8 + (lane >> 3), u = lane & 7;
    const int sk = ((krow >> 1) & 1) | (((krow >> 3) & 1) << 1);
    const int ch = (u >> 1) ^ sk;
    const int lc = cb * 64 + ch * 16 + (u & 1) * 8;
    const u16* p;
    if (TYPE == 0) p = (lc < 192) ? (wA + j * 192 + lc) : (wB + j * 192 + (lc - 192));
    else if (TYPE == 1) p = wA + j * 320 + lc;
    else p = (lc < 160) ? (wA + 640 + j * 160 + lc) : (wA + 1280 + j * 160 + (lc - 160));
    bsrc[t] = p + (size_t)krow * LDW;
  }
  const bool has3 = (NCB == 6) || (wave < 4);

  auto issue = [&](int kt) {
    char* sb = smem + (kt & (K1_NS - 1)) * K1_STAGE;
    __builtin_amdgcn_global_load_lds((const unsigned*)(asrc + (size_t)kt * 8192), (unsigned*)(sb + wave * 1024), 16, 0, 0);
    __builtin_amdgcn_global_load_lds((const unsigned*)(bsrc[0] + (size_t)kt * 32 * LDW), (unsigned*)(sb + 8192 + wave * 1024), 16, 0, 0);
    __builtin_amdgcn_global_load_lds((const unsigned*)(bsrc[1] + (size_t)kt * 32 * LDW), (unsigned*)(sb + 8192 + (wave + 8) * 1024), 16, 0, 0);
    if (has3) __builtin_amdgcn_global_load_lds((const unsigned*)(bsrc[2] + (size_t)kt * 32 * LDW), (unsigned*)(sb + 8192 + (wave + 16) * 1024), 16, 0, 0);
  };

  const int xoff = (wm * 64 + c) * 64 + ((g ^ aswz(c)) << 4);
  int woff[NFR];
  {
    const int q = (lane & 15) >> 2, p = lane & 3;
    const int s = (q >> 1) | ((g & 1) << 1);
    const int lo = (8 * g + q) * 128 + (s << 5) + p * 8;
#pragma unroll
    for (int i = 0; i < NFR; ++i) {
      const int nf = TYPE == 0 ? (i < 3 ? 3 * wn + i : 12 + 3 * wn + (i - 3)) : 5 * wn + i;
      woff[i] = 8192 + (nf >> 2) * 4096 + (lo ^ ((nf & 3) << 5));
    }
  }

  f32x4 acc[4][NFR];
#pragma unroll
  for (int a = 0; a < 4; ++a)
#pragma unroll
    for (int b = 0; b < NFR; ++b) acc[a][b] = (f32x4){0.f, 0.f, 0.f, 0.f};

  auto readfr = [&](int kt, s16x8 (&xf)[4], s16x8 (&wf)[NFR]) {
    const char* sb = smem + (kt & (K1_NS - 1)) * K1_STAGE;
#pragma unroll
    for (int b = 0; b < NFR; ++b) {
      s16x4 lo = __builtin_amdgcn_ds_read_tr16_b64_v4i16((lds_s16x4_ptr)(sb + woff[b]));
      s16x4 hi = __builtin_amdgcn_ds_read_tr16_b64_v4i16((lds_s16x4_ptr)(sb + woff[b] + 512));
      wf[b] = __builtin_shufflevector(lo, hi, 0, 1, 2, 3, 4, 5, 6, 7);
    }
#pragma unroll
    for (int a = 0; a < 4; ++a) xf[a] = *(const s16x8*)(sb + xoff + a * 1024);
  };
  auto mma = [&](const s16x8 (&xf)[4], const s16x8 (&wf)[NFR]) {
#pragma unroll
    for (int a = 0; a < 4; ++a)
#pragma unroll
      for (int b = 0; b < NFR; ++b) {
        if (TYPE == 2) acc[a][b] = __builtin_amdgcn_mfma_f32_16x16x32_bf16(xf[a], wf[b], acc[a][b], 0, 0, 0);
        else           acc[a][b] = __builtin_amdgcn_mfma_f32_16x16x32_bf16(wf[b], xf[a], acc[a][b], 0, 0, 0);
      }
  };
#define K1_WAIT(n) do { if (has3) WAIT_V2(4, n); else WAIT_V2(3, n); } while (0)
#define WAIT_V2(per, n) do { if ((per) * (n) == 12) WAIT_V(12); else if ((per) * (n) == 9) WAIT_V(9); else if ((per) * (n) == 8) WAIT_V(8); \
                             else if ((per) * (n) == 6) WAIT_V(6); else if ((per) * (n) == 4) WAIT_V(4); else if ((per) * (n) == 3) WAIT_V(3); else WAIT_V(0); } while (0)

  s16x8 xA[4], wAf[NFR], xB[4], wBf[NFR];
  issue(0); issue(1); issue(2); issue(3);
  K1_WAIT(3);
  BAR;
  readfr(0, xA, wAf);
  for (int kt = 0; kt < 16; kt += 2) {
    K1_WAIT(2); WAIT_L0; BAR;
    issue(kt + 4);
    readfr(kt + 1, xB, wBf);
    mma(xA, wAf);
    K1_WAIT(2); WAIT_L0; BAR;
    issue(kt + 5);
    readfr(kt + 2, xA, wAf);
    mma(xB, wBf);
  }
  K1_WAIT(2); BAR;  readfr(17, xB, wBf); mma(xA, wAf);
  K1_WAIT(1); BAR;  readfr(18, xA, wAf); mma(xB, wBf);
  K1_WAIT(0); BAR;  readfr(19, xB, wBf); mma(xA, wAf);
  mma(xB, wBf);

  WAIT_L0; BAR;
  if (TYPE == 0) {
#pragma unroll
    for (int a = 0; a < 4; ++a) {
#pragma unroll
      for (int b = 0; b < 3; ++b) {
        float hv[4];
#pragma unroll
        for (int r = 0; r < 4; ++r) {
          const float gv = bfr(acc[a][b][r]), uv = bfr(acc[a][b + 3][r]);
          const float sg = bfr(frcp(1.0f + __expf(-gv)));
          hv[r] = sg * uv;
        }
        u32x2 w; w[0] = pack2(hv[0], hv[1]); w[1] = pack2(hv[2], hv[3]);
        const int f0 = wn * 48 + b * 16;
        const int ktl = f0 >> 5, gk = ((f0 >> 4) & 1) * 2 + (g >> 1);
        *(u32x2*)(smem + (((wm * 4 + a) * 6 + ktl) * 64 + gk * 16 + c) * 16 + (g & 1) * 8) = w;
      }
    }
    __syncthreads();
    char* H = ws + OFF_H + ((size_t)(mt * 8) * 54 + j * 6) * 1024;
#pragma unroll
    for (int i = 0; i < 6; ++i) {
      const int q = tid + 512 * i;
      const int m16 = q / 384, rest = q - m16 * 384;
      const u32x4 v = *(const u32x4*)(smem + q * 16);
      *(u32x4*)(H + (size_t)m16 * 54 * 1024 + rest * 16) = v;
    }
  } else if (TYPE == 1) {
#pragma unroll
    for (int a = 0; a < 4; ++a) {
#pragma unroll
      for (int b = 0; b < 5; ++b) {
        float p0 = phi_f(bfr(acc[a][b][0])), p1 = phi_f(bfr(acc[a][b][1]));
        float p2 = phi_f(bfr(acc[a][b][2])), p3 = phi_f(bfr(acc[a][b][3]));
        u32x2 w; w[0] = pack2(p0, p1); w[1] = pack2(p2, p3);
        *(u32x2*)(smem + (wm * 64 + a * 16 + c) * 656 + (wn * 80 + b * 16 + 4 * g) * 2) = w;
      }
    }
    __syncthreads();
    char* PQ = ws + OFF_PQ + (size_t)(mt * 128) * (PQ_LD * 2) + j * 640;
#pragma unroll
    for (int i = 0; i < 10; ++i) {
      const int q = tid + 512 * i;
      const int row = q / 40, cc = q - row * 40;
      const u32x4 v = *(const u32x4*)(smem + row * 656 + cc * 16);
      *(u32x4*)(PQ + (size_t)row * (PQ_LD * 2) + cc * 16) = v;
    }
  } else {
#pragma unroll
    for (int a = 0; a < 4; ++a)
#pragma unroll
      for (int b = 0; b < 5; ++b) {
        const int col = wn * 80 + b * 16 + c;
        float v0 = bfr(acc[a][b][0]), v1 = bfr(acc[a][b][1]), v2 = bfr(acc[a][b][2]), v3 = bfr(acc[a][b][3]);
        if (wn < 2) { v0 = phi_f(v0); v1 = phi_f(v1); v2 = phi_f(v2); v3 = phi_f(v3); }
        u32x2 w; w[0] = pack2(v0, v1); w[1] = pack2(v2, v3);
        *(u32x2*)(smem + col * 272 + (wm * 64 + a * 16 + 4 * g) * 2) = w;
      }
    __syncthreads();
    const int r32 = lane & 31, hh = lane >> 5;
    f32x16 akv, az;
#pragma unroll
    for (int i = 0; i < 16; ++i) { akv[i] = 0.f; az[i] = 0.f; }
    s16x8 ones;
#pragma unroll
    for (int i = 0; i < 8; ++i) ones[i] = (short)0x3F80;
#pragma unroll
    for (int s = 0; s < 8; ++s) {
      s16x8 af = *(const s16x8*)(smem + (20 * wave + r32) * 272 + (16 * s + 8 * hh) * 2);
      s16x8 bfv = *(const s16x8*)(smem + (160 + 20 * wave + r32) * 272 + (16 * s + 8 * hh) * 2);
      akv = __builtin_amdgcn_mfma_f32_32x32x16_bf16(af, bfv, akv, 0, 0, 0);
      az = __builtin_amdgcn_mfma_f32_32x32x16_bf16(af, ones, az, 0, 0, 0);
    }
    const int head = j * 8 + wave;
    gu32* P = (gu32*)(ws + OFF_KVP) + (size_t)(mt * 32 + head) * 504;
    if (r32 <= 20) {
#pragma unroll
      for (int q = 0; q < 3; ++q) {
        const int d0 = 8 * q + 4 * hh;
        if (d0 < 20) {
#pragma unroll
          for (int r = 0; r < 4; ++r) {
            const float val = (r32 == 20) ? az[4 * q + r] : akv[4 * q + r];
            __hip_atomic_store(P + r32 * 24 + d0 + r, __float_as_uint(val), RLX_AGENT);
          }
        }
      }
    }
    asm volatile("s_waitcnt vmcnt(0)" ::: "memory");
    __syncthreads();
    volatile int* flag = (volatile int*)(smem + 98304);
    if (tid == 0) {
      unsigned tk = __hip_atomic_fetch_add((gu32*)(ws + OFF_CNT) + j * 64, 1u, RLX_AGENT);
      if (tk == 15u) {
        __builtin_amdgcn_fence(__ATOMIC_ACQUIRE, "agent");
        asm volatile("s_waitcnt vmcnt(0)" ::: "memory");
      }
      *flag = (int)tk;
    }
    __syncthreads();
    if (*flag == 15) {
      const float* Pf = (const float*)(ws + OFF_KVP);
#pragma unroll
      for (int u = 0; u < 2; ++u) {
        const int idx = tid + 512 * u;
        const int hl = idx >> 7, fr = (idx >> 6) & 1, ln = idx & 63, gg = ln >> 4, cc = ln & 15;
        const int e = 16 * fr + cc, d0 = 8 * gg;
        const bool act = (e <= 20 && d0 < 20), act1 = act && (d0 < 16);
        const float* src = Pf + (size_t)(j * 8 + hl) * 504 + (act ? e * 24 + d0 : 0);
        f32x4 a0[16], a1[16];
#pragma unroll
        for (int m = 0; m < 16; ++m) {
          a0[m] = *(const f32x4*)(src + (size_t)m * 32 * 504);
          a1[m] = *(const f32x4*)(src + (size_t)m * 32 * 504 + (act1 ? 4 : 0));
        }
        float s0[8];
#pragma unroll
        for (int i = 0; i < 8; ++i) s0[i] = 0.f;
#pragma unroll
        for (int m = 0; m < 16; ++m) {
          s0[0] += a0[m][0]; s0[1] += a0[m][1]; s0[2] += a0[m][2]; s0[3] += a0[m][3];
          s0[4] += a1[m][0]; s0[5] += a1[m][1]; s0[6] += a1[m][2]; s0[7] += a1[m][3];
        }
        if (!act) { s0[0] = s0[1] = s0[2] = s0[3] = 0.f; }
        if (!act1) { s0[4] = s0[5] = s0[6] = s0[7] = 0.f; }
        u32x4 w;
        w[0] = pack2(s0[0], s0[1]); w[1] = pack2(s0[2], s0[3]); w[2] = pack2(s0[4], s0[5]); w[3] = pack2(s0[6], s0[7]);
        *(u32x4*)(ws + OFF_KVF + ((size_t)((j * 8 + hl) * 2 + fr) * 64 + ln) * 16) = w;
      }
    }
  }
}

__global__ __launch_bounds__(512, 2) void k1_gemm(const u16* __restrict__ wqkv, const u16* __restrict__ wg,
                                                   const u16* __restrict__ wu, const u16* __restrict__ wo,
                                                   const u16* __restrict__ wdown, char* __restrict__ ws) {
  extern __shared__ __attribute__((aligned(16))) char smem[];
  const int bid = blockIdx.x;
  if (bid >= 240) {
    for (int t = (bid - 240) * 512 + threadIdx.x; t < 296 * 80; t += 16 * 512) {
      const int koct = t / 80, noct = t - koct * 80;
      const u16* src = (koct < 216) ? (wdown + (size_t)(koct * 8) * D_ + noct * 8) : (wo + (size_t)((koct - 216) * 8) * D_ + noct * 8);
      u32x4 r[8];
#pragma unroll
      for (int jj = 0; jj < 8; ++jj) r[jj] = *(const u32x4*)(src + (size_t)jj * D_);
      const int n0 = noct * 8, nt = n0 / 80, nn = n0 - nt * 80, nf = nn >> 4, c0 = nn & 15, kt = koct >> 2, gg = koct & 3;
      char* dst = ws + OFF_WC + ((size_t)((nt * 74 + kt) * 5 + nf) * 64 + gg * 16 + c0) * 16;
#pragma unroll
      for (int i = 0; i < 4; ++i) {
        u32x4 lo, hi;
#pragma unroll
        for (int q = 0; q < 4; ++q) {
          lo[q] = (r[2 * q][i] & 0xffffu) | (r[2 * q + 1][i] << 16);
          hi[q] = (r[2 * q][i] >> 16) | (r[2 * q + 1][i] & 0xffff0000u);
        }
        *(u32x4*)(dst + (2 * i) * 16) = lo;
        *(u32x4*)(dst + (2 * i + 1) * 16) = hi;
      }
    }
    return;
  }
  const int p = (bid & 7) * 30 + (bid >> 3);
  const int mh = p / 120, rem = p % 120, nt = rem >> 3, mt = mh * 8 + (rem & 7);
  if (nt < 4) k1_body<2>(wqkv, wqkv, ws, mt, nt, smem);
  else if (nt < 6) k1_body<1>(wqkv, wqkv, ws, mt, nt - 4, smem);
  else k1_body<0>(wg, wu, ws, mt, nt - 6, smem);
}

constexpr int K2_OT = 64 * PQ_LD * 2;
constexpr int K2_LDS = 4 * 30 * 1024;
constexpr int K2_D = 6;

__global__ __launch_bounds__(256, 1) void k2_gemm(const u16* __restrict__ x, const char* __restrict__ ws, u16* __restrict__ out) {
  extern __shared__ __attribute__((aligned(16))) char smem[];
  const int tid = threadIdx.x, lane = tid & 63;
  const int wave = __builtin_amdgcn_readfirstlane(tid >> 6);
  const int g = lane >> 4, c = lane & 15;
  const int bid = blockIdx.x;
  const int mt = (bid & 7) * 4 + (bid >> 6), nt = (bid >> 3) & 7;

  const char* hsrc = ws + OFF_H + ((size_t)(mt * 4) * 54 + wave) * 1024 + lane * 16;
  const char* wsrc = ws + OFF_WC + ((size_t)nt * 74 + wave) * 5120 + lane * 16;

  {
    const char* psrc = ws + OFF_PQ + (size_t)mt * K2_OT + lane * 16;
    for (int i = wave; i < 81; i += 4)
      __builtin_amdgcn_global_load_lds((const unsigned*)(psrc + i * 1024), (unsigned*)(smem + i * 1024), 16, 0, 0);
  }

  f32x4 acc1[4][5], acc2[4][5];
#pragma unroll
  for (int a = 0; a < 4; ++a)
#pragma unroll
    for (int b = 0; b < 5; ++b) { acc1[a][b] = (f32x4){0.f, 0.f, 0.f, 0.f}; acc2[a][b] = (f32x4){0.f, 0.f, 0.f, 0.f}; }

  s16x8 xr[K2_D][4], wr[K2_D][5];
  auto ld = [&](int slot, int i) {
#pragma unroll
    for (int b = 0; b < 5; ++b) wr[slot][b] = *(const s16x8*)(wsrc + (size_t)(20 * i + b) * 1024);
#pragma unroll
    for (int a = 0; a < 4; ++a) xr[slot][a] = *(const s16x8*)(hsrc + (size_t)(a * 54 + 4 * i) * 1024);
  };
  auto mma1 = [&](int slot) {
#pragma unroll
    for (int a = 0; a < 4; ++a)
#pragma unroll
      for (int b = 0; b < 5; ++b) acc1[a][b] = __builtin_amdgcn_mfma_f32_16x16x32_bf16(wr[slot][b], xr[slot][a], acc1[a][b], 0, 0, 0);
  };

#pragma unroll
  for (int i = 0; i < K2_D; ++i) ld(i, i);
#pragma unroll
  for (int i = 0; i < 13; ++i) {
    mma1(i % K2_D);
    if (i + K2_D < 13) ld(i % K2_D, i + K2_D);
    else if (i + K2_D == 13) { if (wave < 2) ld(i % K2_D, 13); }
    SCHED;
  }

  s16x8 kvf[8][2];
#pragma unroll
  for (int hh = 0; hh < 8; ++hh)
#pragma unroll
    for (int fr = 0; fr < 2; ++fr)
      kvf[hh][fr] = *(const s16x8*)(ws + OFF_KVF + ((size_t)((wave * 8 + hh) * 2 + fr) * 64 + lane) * 16);
  s16x8 w2[5][5];
#pragma unroll
  for (int i = 0; i < 5; ++i)
#pragma unroll
    for (int b = 0; b < 5; ++b) w2[i][b] = *(const s16x8*)(wsrc + (size_t)(54 * 5 + 20 * i + b) * 1024);
  if (wave < 2) mma1(13 % K2_D);

  asm volatile("s_waitcnt vmcnt(0)" ::: "memory");
  BAR;
  asm volatile("" ::: "memory");
#pragma unroll
  for (int hh = 0; hh < 8; ++hh) {
    const int head = wave * 8 + hh;
#pragma unroll
    for (int tg = 0; tg < 4; ++tg) {
      char* rowp = smem + (tg * 16 + c) * (PQ_LD * 2) + head * 40;
      u32x2 lo = *(const u32x2*)(rowp + 16 * g);
      u32x2 hi = *(const u32x2*)(rowp + 16 * g + 8);
      if (g >= 3) { lo[0] = 0u; lo[1] = 0u; }
      if (g >= 2) { hi[0] = 0u; hi[1] = 0u; }
      u32x4 pk4; pk4[0] = lo[0]; pk4[1] = lo[1]; pk4[2] = hi[0]; pk4[3] = hi[1];
      s16x8 pf = __builtin_bit_cast(s16x8, pk4);
      f32x4 z4 = (f32x4){0.f, 0.f, 0.f, 0.f};
      f32x4 d0 = __builtin_amdgcn_mfma_f32_16x16x32_bf16(kvf[hh][0], pf, z4, 0, 0, 0);
      f32x4 d1 = __builtin_amdgcn_mfma_f32_16x16x32_bf16(kvf[hh][1], pf, z4, 0, 0, 0);
      const float denr = __shfl(d1[0], 16 + c);
      const float den = bfr(bfr(denr) + 1e-6f);
      u32x2 w0, w1;
      w0[0] = pack2(bfr(d0[0]) / den, bfr(d0[1]) / den); w0[1] = pack2(bfr(d0[2]) / den, bfr(d0[3]) / den);
      w1[0] = pack2(bfr(d1[0]) / den, bfr(d1[1]) / den); w1[1] = pack2(bfr(d1[2]) / den, bfr(d1[3]) / den);
      *(u32x2*)(rowp + 8 * g) = w0;
      if (g == 0) *(u32x2*)(rowp + 32) = w1;
    }
  }
  WAIT_L0;
  BAR;
  asm volatile("" ::: "memory");

#pragma unroll
  for (int i = 0; i < 5; ++i) {
    const int k0 = (wave + 4 * i) * 32 + 8 * g;
    s16x8 xf[4];
#pragma unroll
    for (int a = 0; a < 4; ++a) xf[a] = *(const s16x8*)(smem + (a * 16 + c) * (PQ_LD * 2) + k0 * 2);
#pragma unroll
    for (int a = 0; a < 4; ++a)
#pragma unroll
      for (int b = 0; b < 5; ++b) acc2[a][b] = __builtin_amdgcn_mfma_f32_16x16x32_bf16(w2[i][b], xf[a], acc2[a][b], 0, 0, 0);
  }

  __syncthreads();
  f32x4* red = (f32x4*)smem;
#pragma unroll
  for (int a = 0; a < 4; ++a) {
    if (a != wave) {
      const int sl = a - (a > wave ? 1 : 0);
#pragma unroll
      for (int b = 0; b < 5; ++b) {
        red[((wave * 3 + sl) * 10 + b) * 64 + lane] = acc1[a][b];
        red[((wave * 3 + sl) * 10 + 5 + b) * 64 + lane] = acc2[a][b];
      }
    }
  }
  __syncthreads();
  f32x4 y1[5], y2[5];
#pragma unroll
  for (int a = 0; a < 4; ++a)
    if (a == wave) {
#pragma unroll
      for (int b = 0; b < 5; ++b) { y1[b] = acc1[a][b]; y2[b] = acc2[a][b]; }
    }
#pragma unroll
  for (int sw = 0; sw < 4; ++sw) {
    if (sw != wave) {
      const int sl = wave - (wave > sw ? 1 : 0);
#pragma unroll
      for (int b = 0; b < 5; ++b) {
        y1[b] += red[((sw * 3 + sl) * 10 + b) * 64 + lane];
        y2[b] += red[((sw * 3 + sl) * 10 + 5 + b) * 64 + lane];
      }
    }
  }
  {
    const int tok = mt * 64 + wave * 16 + c;
#pragma unroll
    for (int b = 0; b < 5; ++b) {
      const int n = nt * 80 + b * 16 + 4 * g;
      const u32x2 xw = *(const u32x2*)(x + (size_t)tok * D_ + n);
      float xv[4];
      xv[0] = __uint_as_float(xw[0] << 16); xv[1] = __uint_as_float(xw[0] & 0xffff0000u);
      xv[2] = __uint_as_float(xw[1] << 16); xv[3] = __uint_as_float(xw[1] & 0xffff0000u);
      float o[4];
#pragma unroll
      for (int r = 0; r < 4; ++r) {
        const float y = bfr(y1[b][r]);
        const float gs = bfr(y / (1.0f + __expf(-y)));
        const float s = bfr(y2[b][r]);
        o[r] = xv[r] + bfr(s * gs);
      }
      u32x2 w; w[0] = pack2(o[0], o[1]); w[1] = pack2(o[2], o[3]);
      *(u32x2*)(out + (size_t)tok * D_ + n) = w;
    }
  }
}

extern "C" void kernel_launch(void* const* d_in, const int* in_sizes, int n_in,
                              void* d_out, int out_size, void* d_ws, size_t ws_size,
                              hipStream_t stream) {
  const u16* x = (const u16*)d_in[0];
  const u16* wqkv = (const u16*)d_in[1];
  const u16* wo = (const u16*)d_in[2];
  const u16* wg = (const u16*)d_in[4];
  const u16* wu = (const u16*)d_in[5];
  const u16* wd = (const u16*)d_in[6];
  char* ws = (char*)d_ws;
  u16* out = (u16*)d_out;

  hipFuncSetAttribute((const void*)k1_gemm, hipFuncAttributeMaxDynamicSharedMemorySize, K1_NS * K1_STAGE);
  hipFuncSetAttribute((const void*)k2_gemm, hipFuncAttributeMaxDynamicSharedMemorySize, K2_LDS);

  k0_prep<<<256, 256, 0, stream>>>(x, ws);
  k1_gemm<<<256, 512, K1_NS * K1_STAGE, stream>>>(wqkv, wg, wu, wo, wd, ws);
  k2_gemm<<<256, 256, K2_LDS, stream>>>(x, ws, out);
}
```

```cpp
#include <hip/hip_runtime.h>
#include <hip/hip_bf16.h>
#include <stdint.h>

typedef unsigned short u16;
typedef __attribute__((ext_vector_type(8))) short s16x8;
typedef __attribute__((ext_vector_type(4))) short s16x4;
typedef __attribute__((ext_vector_type(4))) float f32x4;
typedef __attribute__((ext_vector_type(16))) float f32x16;
typedef __attribute__((ext_vector_type(2))) unsigned u32x2;
typedef __attribute__((ext_vector_type(4))) unsigned u32x4;
typedef __attribute__((address_space(3))) s16x4* lds_s16x4_ptr;
typedef __attribute__((address_space(1))) unsigned gu32;
typedef __attribute__((address_space(1))) unsigned long long gu64;

#define RLX_AGENT __ATOMIC_RELAXED, __HIP_MEMORY_SCOPE_AGENT

constexpr int S_ = 2048, D_ = 640, F_ = 1728, NQKV_ = 1920;

constexpr size_t OFF_CNT   = 0;
constexpr size_t OFF_XN    = 8192;
constexpr size_t SZ_XN     = (size_t)S_ * D_ * 2;
constexpr size_t OFF_H     = OFF_XN + SZ_XN;
constexpr size_t SZ_H      = (size_t)S_ * F_ * 2;
constexpr size_t OFF_PQ    = OFF_H + SZ_H;
constexpr size_t SZ_PQ     = (size_t)S_ * D_ * 2;
constexpr size_t OFF_WC    = OFF_PQ + SZ_PQ;
constexpr size_t SZ_WC     = (size_t)8 * 74 * 5 * 1024;
constexpr size_t OFF_KVP   = OFF_WC + SZ_WC;
constexpr size_t SZ_KVP    = (size_t)16 * 32 * 504 * 4;
constexpr size_t OFF_O     = OFF_KVP + SZ_KVP;
constexpr size_t SZ_O      = (size_t)S_ * D_ * 2;

__device__ __forceinline__ float bf2f(u16 v) { return __uint_as_float(((unsigned)v) << 16); }
__device__ __forceinline__ u16 f2bf(float f) { return __builtin_bit_cast(u16, (__bf16)f); }
__device__ __forceinline__ float bfr(float f) { return bf2f(f2bf(f)); }
__device__ __forceinline__ unsigned pack2(float a, float b) { return (unsigned)f2bf(a) | ((unsigned)f2bf(b) << 16); }
__device__ __forceinline__ float phi_f(float t) {
  float p = t > 0.f ? bfr(t + 1.0f) : bfr(__expf(t));
  return bfr(p + 1e-6f);
}
__device__ __forceinline__ int aswz(int row) { int x = (row >> 2) & 3; return (((x ^ (x >> 1)) & 1) << 1) | (x >> 1); }

#define WAIT_V(n) asm volatile("s_waitcnt vmcnt(" #n ")" ::: "memory")
#define STAMP(kid, idx)
#define WAIT_L0 asm volatile("s_waitcnt lgkmcnt(0)" ::: "memory")
#define BAR __builtin_amdgcn_s_barrier()
#define SCHED __builtin_amdgcn_sched_barrier(0)
__device__ __forceinline__ float frcp(float v) { return __builtin_amdgcn_rcpf(v); }

__global__ __launch_bounds__(256) void k0_prep(const u16* __restrict__ x, char* __restrict__ ws) {
  const int tid = threadIdx.x;
  {
    if (blockIdx.x == 0 && tid < 32) ((unsigned*)(ws + OFF_CNT))[tid * 64] = 0u;
    const int row = blockIdx.x * 8 + (tid >> 5);
    const int l32 = tid & 31;
    const u16* xr = x + (size_t)row * D_;
    float v[20];
    float sum = 0.f;
#pragma unroll
    for (int i = 0; i < 5; ++i) {
      u32x2 w = *(const u32x2*)(xr + (l32 + 32 * i) * 4);
      v[4 * i + 0] = __uint_as_float(w[0] << 16);
      v[4 * i + 1] = __uint_as_float(w[0] & 0xffff0000u);
      v[4 * i + 2] = __uint_as_float(w[1] << 16);
      v[4 * i + 3] = __uint_as_float(w[1] & 0xffff0000u);
      sum += v[4 * i] + v[4 * i + 1] + v[4 * i + 2] + v[4 * i + 3];
    }
#pragma unroll
    for (int o = 16; o >= 1; o >>= 1) sum += __shfl_xor(sum, o);
    const float mean = sum * (1.0f / D_);
    float sq = 0.f;
#pragma unroll
    for (int i = 0; i < 20; ++i) { float d = v[i] - mean; sq += d * d; }
#pragma unroll
    for (int o = 16; o >= 1; o >>= 1) sq += __shfl_xor(sq, o);
    const float mb = bfr(mean);
    const float vb = bfr(sq * (1.0f / D_));
    const float sd = bfr(sqrtf(bfr(vb + 1e-5f)));
    u16* xn = (u16*)(ws + OFF_XN);
    const int mt = row >> 7, r = row & 127, sw = aswz(r);
#pragma unroll
    for (int i = 0; i < 5; ++i) {
      const int k = (l32 + 32 * i) * 4;
      float o0 = bfr(v[4 * i + 0] - mb) / sd, o1 = bfr(v[4 * i + 1] - mb) / sd;
      float o2 = bfr(v[4 * i + 2] - mb) / sd, o3 = bfr(v[4 * i + 3] - mb) / sd;
      u32x2 w; w[0] = pack2(o0, o1); w[1] = pack2(o2, o3);
      const int kt = k >> 5, ch = (k >> 3) & 3, hf = (k >> 2) & 1;
      size_t off = ((size_t)(mt * 20 + kt) * 128 + r) * 32 + ((ch ^ sw) << 3) + (hf << 2);
      *(u32x2*)(xn + off) = w;
    }
  }
}

constexpr int K1_STAGE = 32768;
constexpr int K1_NS = 4;

template <int TYPE>
__device__ __forceinline__ void k1_body(const u16* __restrict__ wA, const u16* __restrict__ wB, char* __restrict__ ws,
                                        int mt, int j, char* smem) {
  constexpr int NCB = TYPE == 0 ? 6 : 5;
  constexpr int NFR = TYPE == 0 ? 6 : 5;
  constexpr int LDW = TYPE == 0 ? F_ : NQKV_;
  const int tid = threadIdx.x, lane = tid & 63;
  const int wave = __builtin_amdgcn_readfirstlane(tid >> 6);
  const int wm = wave >> 2, wn = wave & 3;
  const int g = lane >> 4, c = lane & 15;

  const char* asrc = ws + OFF_XN + (size_t)mt * 20 * 8192 + wave * 1024 + lane * 16;
  const u16* bsrc[3];
#pragma unroll
  for (int t = 0; t < 3; ++t) {
    int ii = wave + 8 * t; if (ii >= NCB * 4) ii = wave;
    const int cb = ii >> 2, krow = (ii & 3) * 8 + (lane >> 3), u = lane & 7;
    const int sk = ((krow >> 1) & 1) | (((krow >> 3) & 1) << 1);
    const int ch = (u >> 1) ^ sk;
    const int lc = cb * 64 + ch * 16 + (u & 1) * 8;
    const u16* p;
    if (TYPE == 0) p = (lc < 192) ? (wA + j * 192 + lc) : (wB + j * 192 + (lc - 192));
    else if (TYPE == 1) p = wA + j * 320 + lc;
    else p = (lc < 160) ? (wA + 640 + j * 160 + lc) : (wA + 1280 + j * 160 + (lc - 160));
    bsrc[t] = p + (size_t)krow * LDW;
  }
  const bool has3 = (NCB == 6) || (wave < 4);

  auto issue = [&](int kt) {
    char* sb = smem + (kt & (K1_NS - 1)) * K1_STAGE;
    __builtin_amdgcn_global_load_lds((const unsigned*)(asrc + (size_t)kt * 8192), (unsigned*)(sb + wave * 1024), 16, 0, 0);
    __builtin_amdgcn_global_load_lds((const unsigned*)(bsrc[0] + (size_t)kt * 32 * LDW), (unsigned*)(sb + 8192 + wave * 1024), 16, 0, 0);
    __builtin_amdgcn_global_load_lds((const unsigned*)(bsrc[1] + (size_t)kt * 32 * LDW), (unsigned*)(sb + 8192 + (wave + 8) * 1024), 16, 0, 0);
    if (has3) __builtin_amdgcn_global_load_lds((const unsigned*)(bsrc[2] + (size_t)kt * 32 * LDW), (unsigned*)(sb + 8192 + (wave + 16) * 1024), 16, 0, 0);
  };

  const int xoff = (wm * 64 + c) * 64 + ((g ^ aswz(c)) << 4);
  int woff[NFR];
  {
    const int q = (lane & 15) >> 2, p = lane & 3;
    const int s = (q >> 1) | ((g & 1) << 1);
    const int lo = (8 * g + q) * 128 + (s << 5) + p * 8;
#pragma unroll
    for (int i = 0; i < NFR; ++i) {
      const int nf = TYPE == 0 ? (i < 3 ? 3 * wn + i : 12 + 3 * wn + (i - 3)) : 5 * wn + i;
      woff[i] = 8192 + (nf >> 2) * 4096 + (lo ^ ((nf & 3) << 5));
    }
  }

  f32x4 acc[4][NFR];
#pragma unroll
  for (int a = 0; a < 4; ++a)
#pragma unroll
    for (int b = 0; b < NFR; ++b) acc[a][b] = (f32x4){0.f, 0.f, 0.f, 0.f};

  auto readfr = [&](int kt, s16x8 (&xf)[4], s16x8 (&wf)[NFR]) {
    const char* sb = smem + (kt & (K1_NS - 1)) * K1_STAGE;
#pragma unroll
    for (int b = 0; b < NFR; ++b) {
      s16x4 lo = __builtin_amdgcn_ds_read_tr16_b64_v4i16((lds_s16x4_ptr)(sb + woff[b]));
      s16x4 hi = __builtin_amdgcn_ds_read_tr16_b64_v4i16((lds_s16x4_ptr)(sb + woff[b] + 512));
      wf[b] = __builtin_shufflevector(lo, hi, 0, 1, 2, 3, 4, 5, 6, 7);
    }
#pragma unroll
    for (int a = 0; a < 4; ++a) xf[a] = *(const s16x8*)(sb + xoff + a * 1024);
  };
  auto mma = [&](const s16x8 (&xf)[4], const s16x8 (&wf)[NFR]) {
#pragma unroll
    for (int a = 0; a < 4; ++a)
#pragma unroll
      for (int b = 0; b < NFR; ++b) {
        if (TYPE == 2) acc[a][b] = __builtin_amdgcn_mfma_f32_16x16x32_bf16(xf[a], wf[b], acc[a][b], 0, 0, 0);
        else           acc[a][b] = __builtin_amdgcn_mfma_f32_16x16x32_bf16(wf[b], xf[a], acc[a][b], 0, 0, 0);
      }
  };
#define K1_WAIT(n) do { if (has3) WAIT_V2(4, n); else WAIT_V2(3, n); } while (0)
#define WAIT_V2(per, n) do { if ((per) * (n) == 12) WAIT_V(12); else if ((per) * (n) == 9) WAIT_V(9); else if ((per) * (n) == 8) WAIT_V(8); \
                             else if ((per) * (n) == 6) WAIT_V(6); else if ((per) * (n) == 4) WAIT_V(4); else if ((per) * (n) == 3) WAIT_V(3); else WAIT_V(0); } while (0)

  s16x8 xA[4], wAf[NFR], xB[4], wBf[NFR];
#define SGB(m, n) __builtin_amdgcn_sched_group_barrier(m, n, 0)
  auto pattern_full = [&]() {
    SGB(0x10, 1); SGB(0x8, 1); SGB(0x10, 1); SGB(0x8, 1); SGB(0x10, 1); SGB(0x8, 1); SGB(0x10, 1); SGB(0x8, 1);
#pragma unroll
    for (int i = 0; i < 4 + 2 * NFR; ++i) { SGB(0x100, 1); SGB(0x8, 1); }
    SGB(0x8, 4);
  };
  auto pattern_tail = [&]() {
#pragma unroll
    for (int i = 0; i < 4 + 2 * NFR; ++i) { SGB(0x100, 1); SGB(0x8, 1); }
    SGB(0x8, 8);
  };
  STAMP(1, 0);
  issue(0); issue(1); issue(2); issue(3);
  K1_WAIT(3);
  BAR;
  STAMP(1, 1);
  readfr(0, xA, wAf);
  SCHED;
  for (int kt = 0; kt < 16; kt += 2) {
    K1_WAIT(2); WAIT_L0; BAR; SCHED;
    issue(kt + 4);
    readfr(kt + 1, xB, wBf);
    mma(xA, wAf);
    pattern_full();
    SCHED;
    K1_WAIT(2); WAIT_L0; BAR; SCHED;
    issue(kt + 5);
    readfr(kt + 2, xA, wAf);
    mma(xB, wBf);
    pattern_full();
    SCHED;
  }
  K1_WAIT(2); BAR; SCHED; readfr(17, xB, wBf); mma(xA, wAf); pattern_tail(); SCHED;
  K1_WAIT(1); BAR; SCHED; readfr(18, xA, wAf); mma(xB, wBf); pattern_tail(); SCHED;
  K1_WAIT(0); BAR; SCHED; readfr(19, xB, wBf); mma(xA, wAf); pattern_tail(); SCHED;
  mma(xB, wBf);

  WAIT_L0; BAR;
  STAMP(1, 2);
  if (TYPE == 0) {
#pragma unroll
    for (int a = 0; a < 4; ++a) {
#pragma unroll
      for (int b = 0; b < 3; ++b) {
        float hv[4];
#pragma unroll
        for (int r = 0; r < 4; ++r) {
          const float gv = bfr(acc[a][b][r]), uv = bfr(acc[a][b + 3][r]);
          const float sg = bfr(frcp(1.0f + __expf(-gv)));
          hv[r] = sg * uv;
        }
        u32x2 w; w[0] = pack2(hv[0], hv[1]); w[1] = pack2(hv[2], hv[3]);
        const int f0 = wn * 48 + b * 16;
        const int ktl = f0 >> 5, gk = ((f0 >> 4) & 1) * 2 + (g >> 1);
        *(u32x2*)(smem + (((wm * 4 + a) * 6 + ktl) * 64 + gk * 16 + c) * 16 + (g & 1) * 8) = w;
      }
    }
    __syncthreads();
    char* H = ws + OFF_H + ((size_t)(mt * 8) * 54 + j * 6) * 1024;
#pragma unroll
    for (int i = 0; i < 6; ++i) {
      const int q = tid + 512 * i;
      const int m16 = q / 384, rest = q - m16 * 384;
      const u32x4 v = *(const u32x4*)(smem + q * 16);
      *(u32x4*)(H + (size_t)m16 * 54 * 1024 + rest * 16) = v;
    }
  } else if (TYPE == 1) {
#pragma unroll
    for (int a = 0; a < 4; ++a) {
#pragma unroll
      for (int b = 0; b < 5; ++b) {
        float p0 = phi_f(bfr(acc[a][b][0])), p1 = phi_f(bfr(acc[a][b][1]));
        float p2 = phi_f(bfr(acc[a][b][2])), p3 = phi_f(bfr(acc[a][b][3]));
        u32x2 w; w[0] = pack2(p0, p1); w[1] = pack2(p2, p3);
        *(u32x2*)(smem + (wm * 64 + a * 16 + c) * 656 + (wn * 80 + b * 16 + 4 * g) * 2) = w;
      }
    }
    __syncthreads();
    char* PQ = ws + OFF_PQ + (size_t)(mt * 128) * 1280 + j * 640;
#pragma unroll
    for (int i = 0; i < 10; ++i) {
      const int q = tid + 512 * i;
      const int row = q / 40, cc = q - row * 40;
      const u32x4 v = *(const u32x4*)(smem + row * 656 + cc * 16);
      *(u32x4*)(PQ + (size_t)row * 1280 + cc * 16) = v;
    }
  } else {
#pragma unroll
    for (int a = 0; a < 4; ++a)
#pragma unroll
      for (int b = 0; b < 5; ++b) {
        const int col = wn * 80 + b * 16 + c;
        float v0 = bfr(acc[a][b][0]), v1 = bfr(acc[a][b][1]), v2 = bfr(acc[a][b][2]), v3 = bfr(acc[a][b][3]);
        if (wn < 2) { v0 = phi_f(v0); v1 = phi_f(v1); v2 = phi_f(v2); v3 = phi_f(v3); }
        u32x2 w; w[0] = pack2(v0, v1); w[1] = pack2(v2, v3);
        *(u32x2*)(smem + col * 272 + (wm * 64 + a * 16 + 4 * g) * 2) = w;
      }
    __syncthreads();
    const int r32 = lane & 31, hh = lane >> 5;
    f32x16 akv, az;
#pragma unroll
    for (int i = 0; i < 16; ++i) { akv[i] = 0.f; az[i] = 0.f; }
    s16x8 ones;
#pragma unroll
    for (int i = 0; i < 8; ++i) ones[i] = (short)0x3F80;
#pragma unroll
    for (int s = 0; s < 8; ++s) {
      s16x8 af = *(const s16x8*)(smem + (20 * wave + r32) * 272 + (16 * s + 8 * hh) * 2);
      s16x8 bfv = *(const s16x8*)(smem + (160 + 20 * wave + r32) * 272 + (16 * s + 8 * hh) * 2);
      akv = __builtin_amdgcn_mfma_f32_32x32x16_bf16(af, bfv, akv, 0, 0, 0);
      az = __builtin_amdgcn_mfma_f32_32x32x16_bf16(af, ones, az, 0, 0, 0);
    }
    const int head = j * 8 + wave;
    float* P = (float*)(ws + OFF_KVP) + (size_t)(mt * 32 + head) * 504;
    if (r32 <= 20) {
#pragma unroll
      for (int q = 0; q < 3; ++q) {
        const int d0 = 8 * q + 4 * hh;
        if (d0 < 20) {
          f32x4 val;
#pragma unroll
          for (int r = 0; r < 4; ++r) val[r] = (r32 == 20) ? az[4 * q + r] : akv[4 * q + r];
          *(f32x4*)(P + r32 * 24 + d0) = val;
        }
      }
    }
  }
}

__global__ __launch_bounds__(512, 2) void k1_gemm(const u16* __restrict__ wqkv, const u16* __restrict__ wg,
                                                   const u16* __restrict__ wu, const u16* __restrict__ wo,
                                                   const u16* __restrict__ wdown, char* __restrict__ ws) {
  extern __shared__ __attribute__((aligned(16))) char smem[];
  const int bid = blockIdx.x;
  if (bid >= 240) {
    for (int t = (bid - 240) * 512 + threadIdx.x; t < 296 * 80; t += 16 * 512) {
      const int koct = t / 80, noct = t - koct * 80;
      const u16* src = (koct < 216) ? (wdown + (size_t)(koct * 8) * D_ + noct * 8) : (wo + (size_t)((koct - 216) * 8) * D_ + noct * 8);
      u32x4 r[8];
#pragma unroll
      for (int jj = 0; jj < 8; ++jj) r[jj] = *(const u32x4*)(src + (size_t)jj * D_);
      const int n0 = noct * 8, nt = n0 / 80, nn = n0 - nt * 80, nf = nn >> 4, c0 = nn & 15, kt = koct >> 2, gg = koct & 3;
      char* dst = ws + OFF_WC + ((size_t)((nt * 74 + kt) * 5 + nf) * 64 + gg * 16 + c0) * 16;
#pragma unroll
      for (int i = 0; i < 4; ++i) {
        u32x4 lo, hi;
#pragma unroll
        for (int q = 0; q < 4; ++q) {
          lo[q] = (r[2 * q][i] & 0xffffu) | (r[2 * q + 1][i] << 16);
          hi[q] = (r[2 * q][i] >> 16) | (r[2 * q + 1][i] & 0xffff0000u);
        }
        *(u32x4*)(dst + (2 * i) * 16) = lo;
        *(u32x4*)(dst + (2 * i + 1) * 16) = hi;
      }
    }
    return;
  }
  const int p = (bid & 7) * 30 + (bid >> 3);
  const int mh = p / 120, rem = p % 120, nt = rem >> 3, mt = mh * 8 + (rem & 7);
  if (nt < 4) k1_body<2>(wqkv, wqkv, ws, mt, nt, smem);
  else if (nt < 6) k1_body<1>(wqkv, wqkv, ws, mt, nt - 4, smem);
  else k1_body<0>(wg, wu, ws, mt, nt - 6, smem);
  STAMP(1, 3);
}

constexpr int K2_LDS = 4 * 30 * 1024;
constexpr int K2_D = 6;

__global__ __launch_bounds__(256, 1) void k2_gemm(const u16* __restrict__ x, char* __restrict__ ws, u16* __restrict__ out) {
  extern __shared__ __attribute__((aligned(16))) char smem[];
  const int tid = threadIdx.x, lane = tid & 63;
  const int wave = __builtin_amdgcn_readfirstlane(tid >> 6);
  const int g = lane >> 4, c = lane & 15;
  const int bid = blockIdx.x;
  const int mt = (bid & 7) * 4 + (bid >> 6), nt = (bid >> 3) & 7;

  const char* hsrc = ws + OFF_H + ((size_t)(mt * 4) * 54 + wave) * 1024 + lane * 16;
  const char* wsrc = ws + OFF_WC + ((size_t)nt * 74 + wave) * 5120 + lane * 16;

  STAMP(2, 0);
  const int head = nt * 4 + wave;
  u32x2 plo[4], phi2[4];
  {
    const float* Pf = (const float*)(ws + OFF_KVP) + (size_t)head * 504;
    const int l0 = lane * 4, l1 = (lane < 62 ? lane + 64 : lane) * 4;
    f32x4 a0[16], a1[16];
#pragma unroll
    for (int m = 0; m < 16; ++m) {
      a0[m] = *(const f32x4*)(Pf + (size_t)m * 32 * 504 + l0);
      a1[m] = *(const f32x4*)(Pf + (size_t)m * 32 * 504 + l1);
    }
#pragma unroll
    for (int tg = 0; tg < 4; ++tg) {
      const char* rowp = ws + OFF_PQ + (size_t)(mt * 64 + tg * 16 + c) * 1280 + head * 40;
      plo[tg] = *(const u32x2*)(rowp + (g < 3 ? 16 * g : 0));
      phi2[tg] = *(const u32x2*)(rowp + (g < 2 ? 16 * g + 8 : 0));
    }
    f32x4 s0 = a0[0], s1 = a1[0];
#pragma unroll
    for (int m = 1; m < 16; ++m) { s0 += a0[m]; s1 += a1[m]; }
    float* kvs = (float*)(smem + wave * 2048);
    *(f32x4*)(kvs + l0) = s0;
    if (lane < 62) *(f32x4*)(kvs + l1) = s1;
  }
  SCHED;

  f32x4 acc1[4][5], acc2[4][5];
#pragma unroll
  for (int a = 0; a < 4; ++a)
#pragma unroll
    for (int b = 0; b < 5; ++b) { acc1[a][b] = (f32x4){0.f, 0.f, 0.f, 0.f}; acc2[a][b] = (f32x4){0.f, 0.f, 0.f, 0.f}; }

  s16x8 xr[K2_D][4], wr[K2_D][5];
  auto ld = [&](int slot, int i) {
#pragma unroll
    for (int b = 0; b < 5; ++b) wr[slot][b] = *(const s16x8*)(wsrc + (size_t)(20 * i + b) * 1024);
#pragma unroll
    for (int a = 0; a < 4; ++a) xr[slot][a] = *(const s16x8*)(hsrc + (size_t)(a * 54 + 4 * i) * 1024);
  };
  auto mma1 = [&](int slot) {
#pragma unroll
    for (int a = 0; a < 4; ++a)
#pragma unroll
      for (int b = 0; b < 5; ++b) acc1[a][b] = __builtin_amdgcn_mfma_f32_16x16x32_bf16(wr[slot][b], xr[slot][a], acc1[a][b], 0, 0, 0);
  };
#pragma unroll
  for (int i = 0; i < K2_D; ++i) ld(i, i);
  SCHED;

  {
    const float* kvs = (const float*)(smem + wave * 2048);
    s16x8 kvf[2];
#pragma unroll
    for (int fr = 0; fr < 2; ++fr) {
      const int e = 16 * fr + c, d0 = 8 * g;
      const bool act = (e <= 20 && d0 < 20), act1 = act && (d0 < 16);
      const float* src = kvs + (act ? e * 24 + d0 : 0);
      f32x4 v0 = *(const f32x4*)src, v1 = *(const f32x4*)(src + (act1 ? 4 : 0));
      u32x4 w;
      w[0] = act ? pack2(v0[0], v0[1]) : 0u; w[1] = act ? pack2(v0[2], v0[3]) : 0u;
      w[2] = act1 ? pack2(v1[0], v1[1]) : 0u; w[3] = act1 ? pack2(v1[2], v1[3]) : 0u;
      kvf[fr] = __builtin_bit_cast(s16x8, w);
    }
    char* O = ws + OFF_O + (size_t)(mt * 4) * 20 * 1024;
    const int k0 = head * 20 + 4 * g, k1 = head * 20 + 16;
    const int off0 = (k0 >> 5) * 1024 + (((k0 >> 3) & 3) * 16 + c) * 16 + (k0 & 7) * 2;
    const int off1 = (k1 >> 5) * 1024 + (((k1 >> 3) & 3) * 16 + c) * 16 + (k1 & 7) * 2;
#pragma unroll
    for (int tg = 0; tg < 4; ++tg) {
      u32x4 pk4;
      pk4[0] = g < 3 ? plo[tg][0] : 0u; pk4[1] = g < 3 ? plo[tg][1] : 0u;
      pk4[2] = g < 2 ? phi2[tg][0] : 0u; pk4[3] = g < 2 ? phi2[tg][1] : 0u;
      s16x8 pf = __builtin_bit_cast(s16x8, pk4);
      f32x4 z4 = (f32x4){0.f, 0.f, 0.f, 0.f};
      f32x4 d0 = __builtin_amdgcn_mfma_f32_16x16x32_bf16(kvf[0], pf, z4, 0, 0, 0);
      f32x4 d1 = __builtin_amdgcn_mfma_f32_16x16x32_bf16(kvf[1], pf, z4, 0, 0, 0);
      const float denr = __shfl(d1[0], 16 + c);
      const float den = bfr(bfr(denr) + 1e-6f);
      unsigned long long w0, w1;
      w0 = (unsigned long long)pack2(bfr(d0[0]) / den, bfr(d0[1]) / den) | ((unsigned long long)pack2(bfr(d0[2]) / den, bfr(d0[3]) / den) << 32);
      w1 = (unsigned long long)pack2(bfr(d1[0]) / den, bfr(d1[1]) / den) | ((unsigned long long)pack2(bfr(d1[2]) / den, bfr(d1[3]) / den) << 32);
      __hip_atomic_store((gu64*)(O + tg * 20 * 1024 + off0), w0, RLX_AGENT);
      if (g == 0) __hip_atomic_store((gu64*)(O + tg * 20 * 1024 + off1), w1, RLX_AGENT);
    }
  }
  asm volatile("s_waitcnt vmcnt(0)" ::: "memory");
  BAR;
  if (tid == 0) __hip_atomic_fetch_add((gu32*)(ws + OFF_CNT) + mt * 64, 1u, RLX_AGENT);
  STAMP(2, 1);

#pragma unroll
  for (int i = 0; i < 13; ++i) {
    mma1(i % K2_D);
    if (i + K2_D < 13) ld(i % K2_D, i + K2_D);
    else if (i + K2_D == 13) { if (wave < 2) ld(i % K2_D, 13); }
    SCHED;
  }
  s16x8 w2[5][5];
#pragma unroll
  for (int i = 0; i < 5; ++i)
#pragma unroll
    for (int b = 0; b < 5; ++b) w2[i][b] = *(const s16x8*)(wsrc + (size_t)(54 * 5 + 20 * i + b) * 1024);
  if (wave < 2) mma1(13 % K2_D);
  STAMP(2, 2);

  if (lane == 0) {
    while (__hip_atomic_load((gu32*)(ws + OFF_CNT) + mt * 64, RLX_AGENT) < 8u) __builtin_amdgcn_s_sleep(2);
  }
  __builtin_amdgcn_fence(__ATOMIC_ACQUIRE, "agent");
  asm volatile("s_waitcnt vmcnt(0)" ::: "memory");
  STAMP(2, 3);

  {
    const char* osrc = ws + OFF_O + ((size_t)(mt * 4) * 20 + wave) * 1024 + lane * 16;
    s16x8 xf[5][4];
#pragma unroll
    for (int i = 0; i < 5; ++i)
#pragma unroll
      for (int a = 0; a < 4; ++a) xf[i][a] = *(const s16x8*)(osrc + (size_t)(a * 20 + 4 * i) * 1024);
#pragma unroll
    for (int i = 0; i < 5; ++i)
#pragma unroll
      for (int a = 0; a < 4; ++a)
#pragma unroll
        for (int b = 0; b < 5; ++b) acc2[a][b] = __builtin_amdgcn_mfma_f32_16x16x32_bf16(w2[i][b], xf[i][a], acc2[a][b], 0, 0, 0);
  }
  STAMP(2, 4);

  __syncthreads();
  f32x4* red = (f32x4*)smem;
#pragma unroll
  for (int a = 0; a < 4; ++a) {
    if (a != wave) {
      const int sl = a - (a > wave ? 1 : 0);
#pragma unroll
      for (int b = 0; b < 5; ++b) {
        red[((wave * 3 + sl) * 10 + b) * 64 + lane] = acc1[a][b];
        red[((wave * 3 + sl) * 10 + 5 + b) * 64 + lane] = acc2[a][b];
      }
    }
  }
  __syncthreads();
  f32x4 y1[5], y2[5];
#pragma unroll
  for (int a = 0; a < 4; ++a)
    if (a == wave) {
#pragma unroll
      for (int b = 0; b < 5; ++b) { y1[b] = acc1[a][b]; y2[b] = acc2[a][b]; }
    }
#pragma unroll
  for (int sw = 0; sw < 4; ++sw) {
    if (sw != wave) {
      const int sl = wave - (wave > sw ? 1 : 0);
#pragma unroll
      for (int b = 0; b < 5; ++b) {
        y1[b] += red[((sw * 3 + sl) * 10 + b) * 64 + lane];
        y2[b] += red[((sw * 3 + sl) * 10 + 5 + b) * 64 + lane];
      }
    }
  }
  {
    const int tok = mt * 64 + wave * 16 + c;
#pragma unroll
    for (int b = 0; b < 5; ++b) {
      const int n = nt * 80 + b * 16 + 4 * g;
      const u32x2 xw = *(const u32x2*)(x + (size_t)tok * D_ + n);
      float xv[4];
      xv[0] = __uint_as_float(xw[0] << 16); xv[1] = __uint_as_float(xw[0] & 0xffff0000u);
      xv[2] = __uint_as_float(xw[1] << 16); xv[3] = __uint_as_float(xw[1] & 0xffff0000u);
      float o[4];
#pragma unroll
      for (int r = 0; r < 4; ++r) {
        const float y = bfr(y1[b][r]);
        const float gs = bfr(y / (1.0f + __expf(-y)));
        const float s = bfr(y2[b][r]);
        o[r] = xv[r] + bfr(s * gs);
      }
      u32x2 w; w[0] = pack2(o[0], o[1]); w[1] = pack2(o[2], o[3]);
      *(u32x2*)(out + (size_t)tok * D_ + n) = w;
    }
  }
  STAMP(2, 5);
}

extern "C" void kernel_launch(void* const* d_in, const int* in_sizes, int n_in,
                              void* d_out, int out_size, void* d_ws, size_t ws_size,
                              hipStream_t stream) {
  const u16* x = (const u16*)d_in[0];
  const u16* wqkv = (const u16*)d_in[1];
  const u16* wo = (const u16*)d_in[2];
  const u16* wg = (const u16*)d_in[4];
  const u16* wu = (const u16*)d_in[5];
  const u16* wd = (const u16*)d_in[6];
  char* ws = (char*)d_ws;
  u16* out = (u16*)d_out;

  hipFuncSetAttribute((const void*)k1_gemm, hipFuncAttributeMaxDynamicSharedMemorySize, K1_NS * K1_STAGE);
  hipFuncSetAttribute((const void*)k2_gemm, hipFuncAttributeMaxDynamicSharedMemorySize, K2_LDS);

  k0_prep<<<256, 256, 0, stream>>>(x, ws);
  k1_gemm<<<256, 512, K1_NS * K1_STAGE, stream>>>(wqkv, wg, wu, wo, wd, ws);
  k2_gemm<<<256, 256, K2_LDS, stream>>>(x, ws, out);
}
```

```cpp
#include <hip/hip_runtime.h>
#include <hip/hip_bf16.h>
#include <stdint.h>

typedef unsigned short u16;
typedef __attribute__((ext_vector_type(8))) short s16x8;
typedef __attribute__((ext_vector_type(4))) short s16x4;
typedef __attribute__((ext_vector_type(4))) float f32x4;
typedef __attribute__((ext_vector_type(16))) float f32x16;
typedef __attribute__((ext_vector_type(2))) unsigned u32x2;
typedef __attribute__((ext_vector_type(4))) unsigned u32x4;
typedef __attribute__((address_space(3))) s16x4* lds_s16x4_ptr;
typedef __attribute__((address_space(1))) unsigned gu32;
typedef __attribute__((address_space(1))) unsigned long long gu64;

#define RLX_AGENT __ATOMIC_RELAXED, __HIP_MEMORY_SCOPE_AGENT

constexpr int S_ = 2048, D_ = 640, F_ = 1728, NQKV_ = 1920;

constexpr size_t OFF_CNT   = 0;
constexpr size_t OFF_XN    = 8192;
constexpr size_t SZ_XN     = (size_t)S_ * D_ * 2;
constexpr size_t OFF_H     = OFF_XN + SZ_XN;
constexpr size_t SZ_H      = (size_t)S_ * F_ * 2;
constexpr size_t OFF_PQ    = OFF_H + SZ_H;
constexpr size_t SZ_PQ     = (size_t)S_ * D_ * 2;
constexpr size_t OFF_WC    = OFF_PQ + SZ_PQ;
constexpr size_t SZ_WC     = (size_t)8 * 54 * 5 * 1024;
constexpr size_t OFF_WO    = OFF_WC + SZ_WC;
constexpr size_t SZ_WO     = (size_t)8 * 32 * 5 * 1024;
constexpr size_t OFF_KVP   = OFF_WO + SZ_WO;
constexpr size_t SZ_KVP    = (size_t)16 * 32 * 504 * 4;
constexpr size_t OFF_KVF   = OFF_KVP + SZ_KVP;
constexpr size_t SZ_KVF    = 65536;

__device__ __forceinline__ float bf2f(u16 v) { return __uint_as_float(((unsigned)v) << 16); }
__device__ __forceinline__ u16 f2bf(float f) { return __builtin_bit_cast(u16, (__bf16)f); }
__device__ __forceinline__ float bfr(float f) { return bf2f(f2bf(f)); }
__device__ __forceinline__ unsigned pack2(float a, float b) { return (unsigned)f2bf(a) | ((unsigned)f2bf(b) << 16); }
__device__ __forceinline__ float phi_f(float t) {
  float p = t > 0.f ? bfr(t + 1.0f) : bfr(__expf(t));
  return bfr(p + 1e-6f);
}
__device__ __forceinline__ int aswz(int row) { int x = (row >> 2) & 3; return (((x ^ (x >> 1)) & 1) << 1) | (x >> 1); }

#define WAIT_V(n) asm volatile("s_waitcnt vmcnt(" #n ")" ::: "memory")
#define STAMP(kid, idx)
#define WAIT_L0 asm volatile("s_waitcnt lgkmcnt(0)" ::: "memory")
#define BAR __builtin_amdgcn_s_barrier()
#define SCHED __builtin_amdgcn_sched_barrier(0)
__device__ __forceinline__ float frcp(float v) { return __builtin_amdgcn_rcpf(v); }

__global__ __launch_bounds__(256) void k0_prep(const u16* __restrict__ x, char* __restrict__ ws) {
  const int tid = threadIdx.x;
  {
    if (blockIdx.x == 0 && tid < 4) ((unsigned*)(ws + OFF_CNT))[tid * 64] = 0u;
    const int row = blockIdx.x * 8 + (tid >> 5);
    const int l32 = tid & 31;
    const u16* xr = x + (size_t)row * D_;
    float v[20];
    float sum = 0.f;
#pragma unroll
    for (int i = 0; i < 5; ++i) {
      u32x2 w = *(const u32x2*)(xr + (l32 + 32 * i) * 4);
      v[4 * i + 0] = __uint_as_float(w[0] << 16);
      v[4 * i + 1] = __uint_as_float(w[0] & 0xffff0000u);
      v[4 * i + 2] = __uint_as_float(w[1] << 16);
      v[4 * i + 3] = __uint_as_float(w[1] & 0xffff0000u);
      sum += v[4 * i] + v[4 * i + 1] + v[4 * i + 2] + v[4 * i + 3];
    }
#pragma unroll
    for (int o = 16; o >= 1; o >>= 1) sum += __shfl_xor(sum, o);
    const float mean = sum * (1.0f / D_);
    float sq = 0.f;
#pragma unroll
    for (int i = 0; i < 20; ++i) { float d = v[i] - mean; sq += d * d; }
#pragma unroll
    for (int o = 16; o >= 1; o >>= 1) sq += __shfl_xor(sq, o);
    const float mb = bfr(mean);
    const float vb = bfr(sq * (1.0f / D_));
    const float sd = bfr(sqrtf(bfr(vb + 1e-5f)));
    u16* xn = (u16*)(ws + OFF_XN);
    const int mt = row >> 7, r = row & 127, sw = aswz(r);
#pragma unroll
    for (int i = 0; i < 5; ++i) {
      const int k = (l32 + 32 * i) * 4;
      float o0 = bfr(v[4 * i + 0] - mb) / sd, o1 = bfr(v[4 * i + 1] - mb) / sd;
      float o2 = bfr(v[4 * i + 2] - mb) / sd, o3 = bfr(v[4 * i + 3] - mb) / sd;
      u32x2 w; w[0] = pack2(o0, o1); w[1] = pack2(o2, o3);
      const int kt = k >> 5, ch = (k >> 3) & 3, hf = (k >> 2) & 1;
      size_t off = ((size_t)(mt * 20 + kt) * 128 + r) * 32 + ((ch ^ sw) << 3) + (hf << 2);
      *(u32x2*)(xn + off) = w;
    }
  }
}

constexpr int K1_STAGE = 32768;
constexpr int K1_NS = 4;

template <int TYPE>
__device__ __forceinline__ void k1_body(const u16* __restrict__ wA, const u16* __restrict__ wB, char* __restrict__ ws,
                                        int mt, int j, char* smem) {
  constexpr int NCB = TYPE == 0 ? 6 : 5;
  constexpr int NFR = TYPE == 0 ? 6 : 5;
  constexpr int LDW = TYPE == 0 ? F_ : NQKV_;
  const int tid = threadIdx.x, lane = tid & 63;
  const int wave = __builtin_amdgcn_readfirstlane(tid >> 6);
  const int wm = wave >> 2, wn = wave & 3;
  const int g = lane >> 4, c = lane & 15;

  const char* asrc = ws + OFF_XN + (size_t)mt * 20 * 8192 + wave * 1024 + lane * 16;
  const u16* bsrc[3];
#pragma unroll
  for (int t = 0; t < 3; ++t) {
    int ii = wave + 8 * t; if (ii >= NCB * 4) ii = wave;
    const int cb = ii >> 2, krow = (ii & 3) * 8 + (lane >> 3), u = lane & 7;
    const int sk = ((krow >> 1) & 1) | (((krow >> 3) & 1) << 1);
    const int ch = (u >> 1) ^ sk;
    const int lc = cb * 64 + ch * 16 + (u & 1) * 8;
    const u16* p;
    if (TYPE == 0) p = (lc < 192) ? (wA + j * 192 + lc) : (wB + j * 192 + (lc - 192));
    else if (TYPE == 1) p = wA + j * 320 + lc;
    else p = (lc < 160) ? (wA + 640 + j * 160 + lc) : (wA + 1280 + j * 160 + (lc - 160));
    bsrc[t] = p + (size_t)krow * LDW;
  }
  const bool has3 = (NCB == 6) || (wave < 4);

  auto issue = [&](int kt) {
    char* sb = smem + (kt & (K1_NS - 1)) * K1_STAGE;
    __builtin_amdgcn_global_load_lds((const unsigned*)(asrc + (size_t)kt * 8192), (unsigned*)(sb + wave * 1024), 16, 0, 0);
    __builtin_amdgcn_global_load_lds((const unsigned*)(bsrc[0] + (size_t)kt * 32 * LDW), (unsigned*)(sb + 8192 + wave * 1024), 16, 0, 0);
    __builtin_amdgcn_global_load_lds((const unsigned*)(bsrc[1] + (size_t)kt * 32 * LDW), (unsigned*)(sb + 8192 + (wave + 8) * 1024), 16, 0, 0);
    if (has3) __builtin_amdgcn_global_load_lds((const unsigned*)(bsrc[2] + (size_t)kt * 32 * LDW), (unsigned*)(sb + 8192 + (wave + 16) * 1024), 16, 0, 0);
  };

  const int xoff = (wm * 64 + c) * 64 + ((g ^ aswz(c)) << 4);
  int woff[NFR];
  {
    const int q = (lane & 15) >> 2, p = lane & 3;
    const int s = (q >> 1) | ((g & 1) << 1);
    const int lo = (8 * g + q) * 128 + (s << 5) + p * 8;
#pragma unroll
    for (int i = 0; i < NFR; ++i) {
      const int nf = TYPE == 0 ? (i < 3 ? 3 * wn + i : 12 + 3 * wn + (i - 3)) : 5 * wn + i;
      woff[i] = 8192 + (nf >> 2) * 4096 + (lo ^ ((nf & 3) << 5));
    }
  }

  f32x4 acc[4][NFR];
#pragma unroll
  for (int a = 0; a < 4; ++a)
#pragma unroll
    for (int b = 0; b < NFR; ++b) acc[a][b] = (f32x4){0.f, 0.f, 0.f, 0.f};

  auto readfr = [&](int kt, s16x8 (&xf)[4], s16x8 (&wf)[NFR]) {
    const char* sb = smem + (kt & (K1_NS - 1)) * K1_STAGE;
#pragma unroll
    for (int b = 0; b < NFR; ++b) {
      s16x4 lo = __builtin_amdgcn_ds_read_tr16_b64_v4i16((lds_s16x4_ptr)(sb + woff[b]));
      s16x4 hi = __builtin_amdgcn_ds_read_tr16_b64_v4i16((lds_s16x4_ptr)(sb + woff[b] + 512));
      wf[b] = __builtin_shufflevector(lo, hi, 0, 1, 2, 3, 4, 5, 6, 7);
    }
#pragma unroll
    for (int a = 0; a < 4; ++a) xf[a] = *(const s16x8*)(sb + xoff + a * 1024);
  };
  auto mma = [&](const s16x8 (&xf)[4], const s16x8 (&wf)[NFR]) {
#pragma unroll
    for (int a = 0; a < 4; ++a)
#pragma unroll
      for (int b = 0; b < NFR; ++b) {
        if (TYPE == 2) acc[a][b] = __builtin_amdgcn_mfma_f32_16x16x32_bf16(xf[a], wf[b], acc[a][b], 0, 0, 0);
        else           acc[a][b] = __builtin_amdgcn_mfma_f32_16x16x32_bf16(wf[b], xf[a], acc[a][b], 0, 0, 0);
      }
  };
#define K1_WAIT(n) do { if (has3) WAIT_V2(4, n); else WAIT_V2(3, n); } while (0)
#define WAIT_V2(per, n) do { if ((per) * (n) == 12) WAIT_V(12); else if ((per) * (n) == 9) WAIT_V(9); else if ((per) * (n) == 8) WAIT_V(8); \
                             else if ((per) * (n) == 6) WAIT_V(6); else if ((per) * (n) == 4) WAIT_V(4); else if ((per) * (n) == 3) WAIT_V(3); else WAIT_V(0); } while (0)

  s16x8 xA[4], wAf[NFR], xB[4], wBf[NFR];
#define SGB(m, n) __builtin_amdgcn_sched_group_barrier(m, n, 0)
  auto pattern_full = [&]() {
    SGB(0x10, 1); SGB(0x8, 1); SGB(0x10, 1); SGB(0x8, 1); SGB(0x10, 1); SGB(0x8, 1); SGB(0x10, 1); SGB(0x8, 1);
#pragma unroll
    for (int i = 0; i < 4 + 2 * NFR; ++i) { SGB(0x100, 1); SGB(0x8, 1); }
    SGB(0x8, 4);
  };
  auto pattern_tail = [&]() {
#pragma unroll
    for (int i = 0; i < 4 + 2 * NFR; ++i) { SGB(0x100, 1); SGB(0x8, 1); }
    SGB(0x8, 8);
  };
  STAMP(1, 0);
  issue(0); issue(1); issue(2); issue(3);
  K1_WAIT(3);
  BAR;
  STAMP(1, 1);
  readfr(0, xA, wAf);
  SCHED;
  for (int kt = 0; kt < 16; kt += 2) {
    K1_WAIT(2); WAIT_L0; BAR; SCHED;
    issue(kt + 4);
    readfr(kt + 1, xB, wBf);
    mma(xA, wAf);
    pattern_full();
    SCHED;
    K1_WAIT(2); WAIT_L0; BAR; SCHED;
    issue(kt + 5);
    readfr(kt + 2, xA, wAf);
    mma(xB, wBf);
    pattern_full();
    SCHED;
  }
  K1_WAIT(2); BAR; SCHED; readfr(17, xB, wBf); mma(xA, wAf); pattern_tail(); SCHED;
  K1_WAIT(1); BAR; SCHED; readfr(18, xA, wAf); mma(xB, wBf); pattern_tail(); SCHED;
  K1_WAIT(0); BAR; SCHED; readfr(19, xB, wBf); mma(xA, wAf); pattern_tail(); SCHED;
  mma(xB, wBf);

  WAIT_L0; BAR;
  STAMP(1, 2);
  if (TYPE == 0) {
#pragma unroll
    for (int a = 0; a < 4; ++a) {
#pragma unroll
      for (int b = 0; b < 3; ++b) {
        float hv[4];
#pragma unroll
        for (int r = 0; r < 4; ++r) {
          const float gv = bfr(acc[a][b][r]), uv = bfr(acc[a][b + 3][r]);
          const float sg = bfr(frcp(1.0f + __expf(-gv)));
          hv[r] = sg * uv;
        }
        u32x2 w; w[0] = pack2(hv[0], hv[1]); w[1] = pack2(hv[2], hv[3]);
        const int f0 = wn * 48 + b * 16;
        const int ktl = f0 >> 5, gk = ((f0 >> 4) & 1) * 2 + (g >> 1);
        *(u32x2*)(smem + (((wm * 4 + a) * 6 + ktl) * 64 + gk * 16 + c) * 16 + (g & 1) * 8) = w;
      }
    }
    __syncthreads();
    char* H = ws + OFF_H + ((size_t)(mt * 8) * 54 + j * 6) * 1024;
#pragma unroll
    for (int i = 0; i < 6; ++i) {
      const int q = tid + 512 * i;
      const int m16 = q / 384, rest = q - m16 * 384;
      const u32x4 v = *(const u32x4*)(smem + q * 16);
      *(u32x4*)(H + (size_t)m16 * 54 * 1024 + rest * 16) = v;
    }
  } else if (TYPE == 1) {
#pragma unroll
    for (int a = 0; a < 4; ++a) {
#pragma unroll
      for (int b = 0; b < 5; ++b) {
        float p0 = phi_f(bfr(acc[a][b][0])), p1 = phi_f(bfr(acc[a][b][1]));
        float p2 = phi_f(bfr(acc[a][b][2])), p3 = phi_f(bfr(acc[a][b][3]));
        u32x2 w; w[0] = pack2(p0, p1); w[1] = pack2(p2, p3);
        *(u32x2*)(smem + (wm * 64 + a * 16 + c) * 656 + (wn * 80 + b * 16 + 4 * g) * 2) = w;
      }
    }
    __syncthreads();
    char* PQ = ws + OFF_PQ + (size_t)(mt * 128) * 1280 + j * 640;
#pragma unroll
    for (int i = 0; i < 10; ++i) {
      const int q = tid + 512 * i;
      const int row = q / 40, cc = q - row * 40;
      const u32x4 v = *(const u32x4*)(smem + row * 656 + cc * 16);
      *(u32x4*)(PQ + (size_t)row * 1280 + cc * 16) = v;
    }
  } else {
#pragma unroll
    for (int a = 0; a < 4; ++a)
#pragma unroll
      for (int b = 0; b < 5; ++b) {
        const int col = wn * 80 + b * 16 + c;
        float v0 = bfr(acc[a][b][0]), v1 = bfr(acc[a][b][1]), v2 = bfr(acc[a][b][2]), v3 = bfr(acc[a][b][3]);
        if (wn < 2) { v0 = phi_f(v0); v1 = phi_f(v1); v2 = phi_f(v2); v3 = phi_f(v3); }
        u32x2 w; w[0] = pack2(v0, v1); w[1] = pack2(v2, v3);
        *(u32x2*)(smem + col * 272 + (wm * 64 + a * 16 + 4 * g) * 2) = w;
      }
    __syncthreads();
    const int r32 = lane & 31, hh = lane >> 5;
    f32x16 akv, az;
#pragma unroll
    for (int i = 0; i < 16; ++i) { akv[i] = 0.f; az[i] = 0.f; }
    s16x8 ones;
#pragma unroll
    for (int i = 0; i < 8; ++i) ones[i] = (short)0x3F80;
#pragma unroll
    for (int s = 0; s < 8; ++s) {
      s16x8 af = *(const s16x8*)(smem + (20 * wave + r32) * 272 + (16 * s + 8 * hh) * 2);
      s16x8 bfv = *(const s16x8*)(smem + (160 + 20 * wave + r32) * 272 + (16 * s + 8 * hh) * 2);
      akv = __builtin_amdgcn_mfma_f32_32x32x16_bf16(af, bfv, akv, 0, 0, 0);
      az = __builtin_amdgcn_mfma_f32_32x32x16_bf16(af, ones, az, 0, 0, 0);
    }
    const int head = j * 8 + wave;
    float* P = (float*)(ws + OFF_KVP) + (size_t)(mt * 32 + head) * 504;
    if (r32 <= 20) {
#pragma unroll
      for (int q = 0; q < 3; ++q) {
        const int d0 = 8 * q + 4 * hh;
        if (d0 < 20) {
          f32x4 val;
#pragma unroll
          for (int r = 0; r < 4; ++r) val[r] = (r32 == 20) ? az[4 * q + r] : akv[4 * q + r];
          asm volatile("global_store_dwordx4 %0, %1, off sc1" :: "v"(P + r32 * 24 + d0), "v"(val) : "memory");
        }
      }
    }
    asm volatile("s_waitcnt vmcnt(0)" ::: "memory");
    __syncthreads();
    volatile int* flag = (volatile int*)(smem + 98304);
    if (tid == 0) {
      unsigned tk = __hip_atomic_fetch_add((gu32*)(ws + OFF_CNT) + j * 64, 1u, RLX_AGENT);
      if (tk == 15u) {
        __builtin_amdgcn_fence(__ATOMIC_ACQUIRE, "agent");
        asm volatile("s_waitcnt vmcnt(0)" ::: "memory");
      }
      *flag = (int)tk;
    }
    __syncthreads();
    if (*flag == 15) {
      const float* Pf = (const float*)(ws + OFF_KVP) + (size_t)head * 504;
      const int l0 = lane * 4, l1 = (lane < 62 ? lane + 64 : lane) * 4;
      f32x4 a0[16], a1[16];
#pragma unroll
      for (int m = 0; m < 16; ++m) {
        a0[m] = *(const f32x4*)(Pf + (size_t)m * 32 * 504 + l0);
        a1[m] = *(const f32x4*)(Pf + (size_t)m * 32 * 504 + l1);
      }
      f32x4 s0 = a0[0], s1 = a1[0];
#pragma unroll
      for (int m = 1; m < 16; ++m) { s0 += a0[m]; s1 += a1[m]; }
      float* kvs = (float*)(smem + 100352 + wave * 2048);
      *(f32x4*)(kvs + l0) = s0;
      if (lane < 62) *(f32x4*)(kvs + l1) = s1;
      WAIT_L0;
#pragma unroll
      for (int fr = 0; fr < 2; ++fr) {
        const int e = 16 * fr + c, d0 = 8 * g;
        const bool act = (e <= 20 && d0 < 20), act1 = act && (d0 < 16);
        const float* src = kvs + (act ? e * 24 + d0 : 0);
        f32x4 v0 = *(const f32x4*)src, v1 = *(const f32x4*)(src + (act1 ? 4 : 0));
        u32x4 w;
        w[0] = act ? pack2(v0[0], v0[1]) : 0u; w[1] = act ? pack2(v0[2], v0[3]) : 0u;
        w[2] = act1 ? pack2(v1[0], v1[1]) : 0u; w[3] = act1 ? pack2(v1[2], v1[3]) : 0u;
        *(u32x4*)(ws + OFF_KVF + ((size_t)(head * 2 + fr) * 64 + lane) * 16) = w;
      }
    }
  }
}

__global__ __launch_bounds__(512, 2) void k1_gemm(const u16* __restrict__ wqkv, const u16* __restrict__ wg,
                                                   const u16* __restrict__ wu, const u16* __restrict__ wo,
                                                   const u16* __restrict__ wdown, char* __restrict__ ws) {
  extern __shared__ __attribute__((aligned(16))) char smem[];
  const int bid = blockIdx.x;
  if (bid >= 240) {
    for (int t = (bid - 240) * 512 + threadIdx.x; t < 216 * 80 + 32 * 80; t += 16 * 512) {
      if (t < 216 * 80) {
        const int koct = t / 80, noct = t - koct * 80;
        const u16* src = wdown + (size_t)(koct * 8) * D_ + noct * 8;
        u32x4 r[8];
#pragma unroll
        for (int jj = 0; jj < 8; ++jj) r[jj] = *(const u32x4*)(src + (size_t)jj * D_);
        const int n0 = noct * 8, nt = n0 / 80, nn = n0 - nt * 80, nf = nn >> 4, c0 = nn & 15, kt = koct >> 2, gg = koct & 3;
        char* dst = ws + OFF_WC + ((size_t)((nt * 54 + kt) * 5 + nf) * 64 + gg * 16 + c0) * 16;
#pragma unroll
        for (int i = 0; i < 4; ++i) {
          u32x4 lo, hi;
#pragma unroll
          for (int q = 0; q < 4; ++q) {
            lo[q] = (r[2 * q][i] & 0xffffu) | (r[2 * q + 1][i] << 16);
            hi[q] = (r[2 * q][i] >> 16) | (r[2 * q + 1][i] & 0xffff0000u);
          }
          *(u32x4*)(dst + (2 * i) * 16) = lo;
          *(u32x4*)(dst + (2 * i + 1) * 16) = hi;
        }
      } else {
        const int t2 = t - 216 * 80, hd = t2 / 80, noct = t2 - hd * 80;
        const u16* src = wo + (size_t)(hd * 20) * D_ + noct * 8;
        u32x4 r[20];
#pragma unroll
        for (int jj = 0; jj < 20; ++jj) r[jj] = *(const u32x4*)(src + (size_t)jj * D_);
        const int n0 = noct * 8, nt = n0 / 80, nn = n0 - nt * 80, nf = nn >> 4, c0 = nn & 15;
        char* dst = ws + OFF_WO + ((size_t)((nt * 32 + hd) * 5 + nf) * 64 + c0) * 16;
#pragma unroll
        for (int gg = 0; gg < 4; ++gg) {
#pragma unroll
          for (int i = 0; i < 4; ++i) {
            u32x4 lo, hi;
            lo[0] = (r[4 * gg][i] & 0xffffu) | (r[4 * gg + 1][i] << 16);
            lo[1] = (r[4 * gg + 2][i] & 0xffffu) | (r[4 * gg + 3][i] << 16);
            hi[0] = (r[4 * gg][i] >> 16) | (r[4 * gg + 1][i] & 0xffff0000u);
            hi[1] = (r[4 * gg + 2][i] >> 16) | (r[4 * gg + 3][i] & 0xffff0000u);
            if (gg == 0) {
              lo[2] = (r[16][i] & 0xffffu) | (r[17][i] << 16); lo[3] = (r[18][i] & 0xffffu) | (r[19][i] << 16);
              hi[2] = (r[16][i] >> 16) | (r[17][i] & 0xffff0000u); hi[3] = (r[18][i] >> 16) | (r[19][i] & 0xffff0000u);
            } else { lo[2] = 0u; lo[3] = 0u; hi[2] = 0u; hi[3] = 0u; }
            *(u32x4*)(dst + (gg * 16 + 2 * i) * 16) = lo;
            *(u32x4*)(dst + (gg * 16 + 2 * i + 1) * 16) = hi;
          }
        }
      }
    }
    return;
  }
  const int p = (bid & 7) * 30 + (bid >> 3);
  const int mh = p / 120, rem = p % 120, nt = rem >> 3, mt = mh * 8 + (rem & 7);
  if (nt < 4) k1_body<2>(wqkv, wqkv, ws, mt, nt, smem);
  else if (nt < 6) k1_body<1>(wqkv, wqkv, ws, mt, nt - 4, smem);
  else k1_body<0>(wg, wu, ws, mt, nt - 6, smem);
  STAMP(1, 3);
}

constexpr int K2_LDS = 4 * 30 * 1024;
constexpr int K2_D = 5;
constexpr int K2_HD = 3;

__global__ __launch_bounds__(256, 1) void k2_gemm(const u16* __restrict__ x, const char* __restrict__ ws, u16* __restrict__ out) {
  extern __shared__ __attribute__((aligned(16))) char smem[];
  const int tid = threadIdx.x, lane = tid & 63;
  const int wave = __builtin_amdgcn_readfirstlane(tid >> 6);
  const int g = lane >> 4, c = lane & 15;
  const int bid = blockIdx.x;
  const int mt = (bid & 7) * 4 + (bid >> 6), nt = (bid >> 3) & 7;

  const char* hsrc = ws + OFF_H + ((size_t)(mt * 4) * 54 + wave) * 1024 + lane * 16;
  const char* wsrc = ws + OFF_WC + ((size_t)nt * 54 + wave) * 5120 + lane * 16;

  STAMP(2, 0);
  f32x4 acc1[4][5], acc2[4][5];
#pragma unroll
  for (int a = 0; a < 4; ++a)
#pragma unroll
    for (int b = 0; b < 5; ++b) { acc1[a][b] = (f32x4){0.f, 0.f, 0.f, 0.f}; acc2[a][b] = (f32x4){0.f, 0.f, 0.f, 0.f}; }

  s16x8 xr[K2_D][4], wr[K2_D][5];
  auto ld = [&](int slot, int i) {
#pragma unroll
    for (int b = 0; b < 5; ++b) wr[slot][b] = *(const s16x8*)(wsrc + (size_t)(20 * i + b) * 1024);
#pragma unroll
    for (int a = 0; a < 4; ++a) xr[slot][a] = *(const s16x8*)(hsrc + (size_t)(a * 54 + 4 * i) * 1024);
  };
  auto mma1 = [&](int slot) {
#pragma unroll
    for (int a = 0; a < 4; ++a)
#pragma unroll
      for (int b = 0; b < 5; ++b) acc1[a][b] = __builtin_amdgcn_mfma_f32_16x16x32_bf16(wr[slot][b], xr[slot][a], acc1[a][b], 0, 0, 0);
  };
  s16x8 kvf[K2_HD][2], w2[K2_HD][5];
  u32x2 plo[K2_HD][4], phi2[K2_HD][4];
  const char* kvsrc = ws + OFF_KVF + (size_t)(wave * 8) * 2048 + lane * 16;
  const char* wosrc = ws + OFF_WO + ((size_t)nt * 32 + wave * 8) * 5120 + lane * 16;
  const char* pqsrc0 = ws + OFF_PQ + (size_t)(mt * 64 + c) * 1280 + wave * 320 + (g < 3 ? 16 * g : 0);
  const char* pqsrc1 = ws + OFF_PQ + (size_t)(mt * 64 + c) * 1280 + wave * 320 + (g < 2 ? 16 * g + 8 : 0);
  auto ld2 = [&](int slot, int hh) {
#pragma unroll
    for (int tg = 0; tg < 4; ++tg) {
      plo[slot][tg] = *(const u32x2*)(pqsrc0 + tg * 16 * 1280 + hh * 40);
      phi2[slot][tg] = *(const u32x2*)(pqsrc1 + tg * 16 * 1280 + hh * 40);
    }
#pragma unroll
    for (int fr = 0; fr < 2; ++fr) kvf[slot][fr] = *(const s16x8*)(kvsrc + (hh * 2 + fr) * 1024);
#pragma unroll
    for (int b = 0; b < 5; ++b) w2[slot][b] = *(const s16x8*)(wosrc + (hh * 5 + b) * 1024);
  };

#pragma unroll
  for (int i = 0; i < K2_D; ++i) ld(i, i);
  SCHED;
#pragma unroll
  for (int i = 0; i < 13; ++i) {
    mma1(i % K2_D);
    if (i + K2_D < 13) ld(i % K2_D, i + K2_D);
    else if (i + K2_D == 13) { if (wave < 2) ld(i % K2_D, 13); }
    else if (i + K2_D - 14 < K2_HD) ld2(i + K2_D - 14, i + K2_D - 14);
    SCHED;
  }
  if (wave < 2) mma1(13 % K2_D);
  STAMP(2, 1);
  u32x2 xw[5];
  const int tok = mt * 64 + wave * 16 + c;
#pragma unroll
  for (int b = 0; b < 5; ++b) xw[b] = *(const u32x2*)(x + (size_t)tok * D_ + nt * 80 + b * 16 + 4 * g);
  SCHED;

#pragma unroll
  for (int hh = 0; hh < 8; ++hh) {
    const int sl = hh % K2_HD;
    s16x8 of[4];
#pragma unroll
    for (int tg = 0; tg < 4; ++tg) {
      u32x4 pk4;
      pk4[0] = g < 3 ? plo[sl][tg][0] : 0u; pk4[1] = g < 3 ? plo[sl][tg][1] : 0u;
      pk4[2] = g < 2 ? phi2[sl][tg][0] : 0u; pk4[3] = g < 2 ? phi2[sl][tg][1] : 0u;
      s16x8 pf = __builtin_bit_cast(s16x8, pk4);
      f32x4 z4 = (f32x4){0.f, 0.f, 0.f, 0.f};
      f32x4 d0 = __builtin_amdgcn_mfma_f32_16x16x32_bf16(kvf[sl][0], pf, z4, 0, 0, 0);
      f32x4 d1 = __builtin_amdgcn_mfma_f32_16x16x32_bf16(kvf[sl][1], pf, z4, 0, 0, 0);
      const float denr = __shfl(d1[0], 16 + c);
      const float rden = frcp(bfr(bfr(denr) + 1e-6f));
      u32x4 ow;
      ow[0] = pack2(bfr(d0[0]) * rden, bfr(d0[1]) * rden); ow[1] = pack2(bfr(d0[2]) * rden, bfr(d0[3]) * rden);
      ow[2] = g == 0 ? pack2(bfr(d1[0]) * rden, bfr(d1[1]) * rden) : 0u; ow[3] = g == 0 ? pack2(bfr(d1[2]) * rden, bfr(d1[3]) * rden) : 0u;
      of[tg] = __builtin_bit_cast(s16x8, ow);
    }
#pragma unroll
    for (int a = 0; a < 4; ++a)
#pragma unroll
      for (int b = 0; b < 5; ++b) acc2[a][b] = __builtin_amdgcn_mfma_f32_16x16x32_bf16(w2[sl][b], of[a], acc2[a][b], 0, 0, 0);
    if (hh + K2_HD < 8) ld2(sl, hh + K2_HD);
    SCHED;
  }
  STAMP(2, 2);

  f32x4* red = (f32x4*)smem;
#pragma unroll
  for (int a = 0; a < 4; ++a) {
    if (a != wave) {
      const int sl = a - (a > wave ? 1 : 0);
#pragma unroll
      for (int b = 0; b < 5; ++b) {
        red[((wave * 3 + sl) * 10 + b) * 64 + lane] = acc1[a][b];
        red[((wave * 3 + sl) * 10 + 5 + b) * 64 + lane] = acc2[a][b];
      }
    }
  }
  __syncthreads();
  f32x4 y1[5], y2[5];
#pragma unroll
  for (int a = 0; a < 4; ++a)
    if (a == wave) {
#pragma unroll
      for (int b = 0; b < 5; ++b) { y1[b] = acc1[a][b]; y2[b] = acc2[a][b]; }
    }
#pragma unroll
  for (int sw = 0; sw < 4; ++sw) {
    if (sw != wave) {
      const int sl = wave - (wave > sw ? 1 : 0);
#pragma unroll
      for (int b = 0; b < 5; ++b) {
        y1[b] += red[((sw * 3 + sl) * 10 + b) * 64 + lane];
        y2[b] += red[((sw * 3 + sl) * 10 + 5 + b) * 64 + lane];
      }
    }
  }
#pragma unroll
  for (int b = 0; b < 5; ++b) {
    const int n = nt * 80 + b * 16 + 4 * g;
    float xv[4];
    xv[0] = __uint_as_float(xw[b][0] << 16); xv[1] = __uint_as_float(xw[b][0] & 0xffff0000u);
    xv[2] = __uint_as_float(xw[b][1] << 16); xv[3] = __uint_as_float(xw[b][1] & 0xffff0000u);
    float o[4];
#pragma unroll
    for (int r = 0; r < 4; ++r) {
      const float y = bfr(y1[b][r]);
      const float gs = bfr(y * frcp(1.0f + __expf(-y)));
      const float s = bfr(y2[b][r]);
      o[r] = xv[r] + bfr(s * gs);
    }
    u32x2 w; w[0] = pack2(o[0], o[1]); w[1] = pack2(o[2], o[3]);
    *(u32x2*)(out + (size_t)tok * D_ + n) = w;
  }
  STAMP(2, 3);
}

extern "C" void kernel_launch(void* const* d_in, const int* in_sizes, int n_in,
                              void* d_out, int out_size, void* d_ws, size_t ws_size,
                              hipStream_t stream) {
  const u16* x = (const u16*)d_in[0];
  const u16* wqkv = (const u16*)d_in[1];
  const u16* wo = (const u16*)d_in[2];
  const u16* wg = (const u16*)d_in[4];
  const u16* wu = (const u16*)d_in[5];
  const u16* wd = (const u16*)d_in[6];
  char* ws = (char*)d_ws;
  u16* out = (u16*)d_out;

  hipFuncSetAttribute((const void*)k1_gemm, hipFuncAttributeMaxDynamicSharedMemorySize, K1_NS * K1_STAGE);
  hipFuncSetAttribute((const void*)k2_gemm, hipFuncAttributeMaxDynamicSharedMemorySize, K2_LDS);

  k0_prep<<<256, 256, 0, stream>>>(x, ws);
  k1_gemm<<<256, 512, K1_NS * K1_STAGE, stream>>>(wqkv, wg, wu, wo, wd, ws);
  k2_gemm<<<256, 256, K2_LDS, stream>>>(x, ws, out);
}
```

```cpp
#include <hip/hip_runtime.h>
#include <hip/hip_bf16.h>
#include <stdint.h>

typedef unsigned short u16;
typedef __attribute__((ext_vector_type(8))) short s16x8;
typedef __attribute__((ext_vector_type(4))) short s16x4;
typedef __attribute__((ext_vector_type(4))) float f32x4;
typedef __attribute__((ext_vector_type(16))) float f32x16;
typedef __attribute__((ext_vector_type(2))) unsigned u32x2;
typedef __attribute__((ext_vector_type(4))) unsigned u32x4;
typedef __attribute__((address_space(3))) s16x4* lds_s16x4_ptr;
typedef __attribute__((address_space(1))) unsigned gu32;
typedef __attribute__((address_space(1))) unsigned long long gu64;

#define RLX_AGENT __ATOMIC_RELAXED, __HIP_MEMORY_SCOPE_AGENT

constexpr int S_ = 2048, D_ = 640, F_ = 1728, NQKV_ = 1920;

constexpr size_t OFF_CNT   = 0;
constexpr size_t OFF_XN    = 8192;
constexpr size_t SZ_XN     = (size_t)S_ * D_ * 2;
constexpr size_t OFF_H     = OFF_XN + SZ_XN;
constexpr size_t SZ_H      = (size_t)S_ * F_ * 2;
constexpr size_t OFF_PQ    = OFF_H + SZ_H;
constexpr size_t SZ_PQ     = (size_t)S_ * D_ * 2;
constexpr size_t OFF_WC    = OFF_PQ + SZ_PQ;
constexpr size_t SZ_WC     = (size_t)8 * 54 * 5 * 1024;
constexpr size_t OFF_WO    = OFF_WC + SZ_WC;
constexpr size_t SZ_WO     = (size_t)8 * 32 * 5 * 1024;
constexpr size_t OFF_KVP   = OFF_WO + SZ_WO;
constexpr size_t SZ_KVP    = (size_t)16 * 32 * 504 * 4;
constexpr size_t OFF_KVF   = OFF_KVP + SZ_KVP;
constexpr size_t SZ_KVF    = 65536;

__device__ __forceinline__ float bf2f(u16 v) { return __uint_as_float(((unsigned)v) << 16); }
__device__ __forceinline__ u16 f2bf(float f) { return __builtin_bit_cast(u16, (__bf16)f); }
__device__ __forceinline__ float bfr(float f) { return bf2f(f2bf(f)); }
__device__ __forceinline__ unsigned pack2(float a, float b) { return (unsigned)f2bf(a) | ((unsigned)f2bf(b) << 16); }
__device__ __forceinline__ float phi_f(float t) {
  float p = t > 0.f ? bfr(t + 1.0f) : bfr(__expf(t));
  return bfr(p + 1e-6f);
}
__device__ __forceinline__ int aswz(int row) { int x = (row >> 2) & 3; return (((x ^ (x >> 1)) & 1) << 1) | (x >> 1); }

#define WAIT_V(n) asm volatile("s_waitcnt vmcnt(" #n ")" ::: "memory")
#define STAMP(kid, idx)
#define WAIT_L0 asm volatile("s_waitcnt lgkmcnt(0)" ::: "memory")
#define BAR __builtin_amdgcn_s_barrier()
#define SCHED __builtin_amdgcn_sched_barrier(0)
__device__ __forceinline__ float frcp(float v) { return __builtin_amdgcn_rcpf(v); }

__global__ __launch_bounds__(256) void k0_prep(const u16* __restrict__ x, const u16* __restrict__ wo,
                                               const u16* __restrict__ wdown, char* __restrict__ ws) {
  const int tid = threadIdx.x;
  if (blockIdx.x >= 256) {
    const int t = (blockIdx.x - 256) * 256 + tid;
    if (t < 216 * 80 + 32 * 80) {
    if (t < 216 * 80) {
      const int koct = t / 80, noct = t - koct * 80;
      const u16* src = wdown + (size_t)(koct * 8) * D_ + noct * 8;
      u32x4 r[8];
#pragma unroll
      for (int jj = 0; jj < 8; ++jj) r[jj] = *(const u32x4*)(src + (size_t)jj * D_);
      const int n0 = noct * 8, nt = n0 / 80, nn = n0 - nt * 80, nf = nn >> 4, c0 = nn & 15, kt = koct >> 2, gg = koct & 3;
      char* dst = ws + OFF_WC + ((size_t)((nt * 54 + kt) * 5 + nf) * 64 + gg * 16 + c0) * 16;
#pragma unroll
      for (int i = 0; i < 4; ++i) {
        u32x4 lo, hi;
#pragma unroll
        for (int q = 0; q < 4; ++q) {
          lo[q] = (r[2 * q][i] & 0xffffu) | (r[2 * q + 1][i] << 16);
          hi[q] = (r[2 * q][i] >> 16) | (r[2 * q + 1][i] & 0xffff0000u);
        }
        *(u32x4*)(dst + (2 * i) * 16) = lo;
        *(u32x4*)(dst + (2 * i + 1) * 16) = hi;
      }
    } else {
      const int t2 = t - 216 * 80, hd = t2 / 80, noct = t2 - hd * 80;
      const u16* src = wo + (size_t)(hd * 20) * D_ + noct * 8;
      u32x4 r[20];
#pragma unroll
      for (int jj = 0; jj < 20; ++jj) r[jj] = *(const u32x4*)(src + (size_t)jj * D_);
      const int n0 = noct * 8, nt = n0 / 80, nn = n0 - nt * 80, nf = nn >> 4, c0 = nn & 15;
      char* dst = ws + OFF_WO + ((size_t)((nt * 32 + hd) * 5 + nf) * 64 + c0) * 16;
#pragma unroll
      for (int gg = 0; gg < 4; ++gg) {
#pragma unroll
        for (int i = 0; i < 4; ++i) {
          u32x4 lo, hi;
          lo[0] = (r[4 * gg][i] & 0xffffu) | (r[4 * gg + 1][i] << 16);
          lo[1] = (r[4 * gg + 2][i] & 0xffffu) | (r[4 * gg + 3][i] << 16);
          hi[0] = (r[4 * gg][i] >> 16) | (r[4 * gg + 1][i] & 0xffff0000u);
          hi[1] = (r[4 * gg + 2][i] >> 16) | (r[4 * gg + 3][i] & 0xffff0000u);
          if (gg == 0) {
            lo[2] = (r[16][i] & 0xffffu) | (r[17][i] << 16); lo[3] = (r[18][i] & 0xffffu) | (r[19][i] << 16);
            hi[2] = (r[16][i] >> 16) | (r[17][i] & 0xffff0000u); hi[3] = (r[18][i] >> 16) | (r[19][i] & 0xffff0000u);
          } else { lo[2] = 0u; lo[3] = 0u; hi[2] = 0u; hi[3] = 0u; }
          *(u32x4*)(dst + (gg * 16 + 2 * i) * 16) = lo;
          *(u32x4*)(dst + (gg * 16 + 2 * i + 1) * 16) = hi;
        }
      }
    }
    }
  } else {
    if (blockIdx.x == 0 && tid < 4) ((unsigned*)(ws + OFF_CNT))[tid * 64] = 0u;
    const int row = blockIdx.x * 8 + (tid >> 5);
    const int l32 = tid & 31;
    const u16* xr = x + (size_t)row * D_;
    float v[20];
    float sum = 0.f;
#pragma unroll
    for (int i = 0; i < 5; ++i) {
      u32x2 w = *(const u32x2*)(xr + (l32 + 32 * i) * 4);
      v[4 * i + 0] = __uint_as_float(w[0] << 16);
      v[4 * i + 1] = __uint_as_float(w[0] & 0xffff0000u);
      v[4 * i + 2] = __uint_as_float(w[1] << 16);
      v[4 * i + 3] = __uint_as_float(w[1] & 0xffff0000u);
      sum += v[4 * i] + v[4 * i + 1] + v[4 * i + 2] + v[4 * i + 3];
    }
#pragma unroll
    for (int o = 16; o >= 1; o >>= 1) sum += __shfl_xor(sum, o);
    const float mean = sum * (1.0f / D_);
    float sq = 0.f;
#pragma unroll
    for (int i = 0; i < 20; ++i) { float d = v[i] - mean; sq += d * d; }
#pragma unroll
    for (int o = 16; o >= 1; o >>= 1) sq += __shfl_xor(sq, o);
    const float mb = bfr(mean);
    const float vb = bfr(sq * (1.0f / D_));
    const float sd = bfr(sqrtf(bfr(vb + 1e-5f)));
    u16* xn = (u16*)(ws + OFF_XN);
    const int mt = row >> 7, r = row & 127, sw = aswz(r);
#pragma unroll
    for (int i = 0; i < 5; ++i) {
      const int k = (l32 + 32 * i) * 4;
      float o0 = bfr(v[4 * i + 0] - mb) / sd, o1 = bfr(v[4 * i + 1] - mb) / sd;
      float o2 = bfr(v[4 * i + 2] - mb) / sd, o3 = bfr(v[4 * i + 3] - mb) / sd;
      u32x2 w; w[0] = pack2(o0, o1); w[1] = pack2(o2, o3);
      const int kt = k >> 5, ch = (k >> 3) & 3, hf = (k >> 2) & 1;
      size_t off = ((size_t)(mt * 20 + kt) * 128 + r) * 32 + ((ch ^ sw) << 3) + (hf << 2);
      *(u32x2*)(xn + off) = w;
    }
  }
}

constexpr int K1_STAGE = 32768;
constexpr int K1_NS = 4;

template <int TYPE>
__device__ __forceinline__ void k1_body(const u16* __restrict__ wA, const u16* __restrict__ wB, char* __restrict__ ws,
                                        int mt, int j, char* smem) {
  constexpr int NCB = TYPE == 0 ? 6 : 5;
  constexpr int NFR = TYPE == 0 ? 6 : 5;
  constexpr int LDW = TYPE == 0 ? F_ : NQKV_;
  const int tid = threadIdx.x, lane = tid & 63;
  const int wave = __builtin_amdgcn_readfirstlane(tid >> 6);
  const int wm = wave >> 2, wn = wave & 3;
  const int g = lane >> 4, c = lane & 15;

  const char* asrc = ws + OFF_XN + (size_t)mt * 20 * 8192 + wave * 1024 + lane * 16;
  const u16* bsrc[3];
#pragma unroll
  for (int t = 0; t < 3; ++t) {
    int ii = wave + 8 * t; if (ii >= NCB * 4) ii = wave;
    const int cb = ii >> 2, krow = (ii & 3) * 8 + (lane >> 3), u = lane & 7;
    const int sk = ((krow >> 1) & 1) | (((krow >> 3) & 1) << 1);
    const int ch = (u >> 1) ^ sk;
    const int lc = cb * 64 + ch * 16 + (u & 1) * 8;
    const u16* p;
    if (TYPE == 0) p = (lc < 192) ? (wA + j * 192 + lc) : (wB + j * 192 + (lc - 192));
    else if (TYPE == 1) p = wA + j * 320 + lc;
    else p = (lc < 160) ? (wA + 640 + j * 160 + lc) : (wA + 1280 + j * 160 + (lc - 160));
    bsrc[t] = p + (size_t)krow * LDW;
  }
  const bool has3 = (NCB == 6) || (wave < 4);

  auto issue = [&](int kt) {
    char* sb = smem + (kt & (K1_NS - 1)) * K1_STAGE;
    __builtin_amdgcn_global_load_lds((const unsigned*)(asrc + (size_t)kt * 8192), (unsigned*)(sb + wave * 1024), 16, 0, 0);
    __builtin_amdgcn_global_load_lds((const unsigned*)(bsrc[0] + (size_t)kt * 32 * LDW), (unsigned*)(sb + 8192 + wave * 1024), 16, 0, 0);
    __builtin_amdgcn_global_load_lds((const unsigned*)(bsrc[1] + (size_t)kt * 32 * LDW), (unsigned*)(sb + 8192 + (wave + 8) * 1024), 16, 0, 0);
    if (has3) __builtin_amdgcn_global_load_lds((const unsigned*)(bsrc[2] + (size_t)kt * 32 * LDW), (unsigned*)(sb + 8192 + (wave + 16) * 1024), 16, 0, 0);
  };

  const int xoff = (wm * 64 + c) * 64 + ((g ^ aswz(c)) << 4);
  int woff[NFR];
  {
    const int q = (lane & 15) >> 2, p = lane & 3;
    const int s = (q >> 1) | ((g & 1) << 1);
    const int lo = (8 * g + q) * 128 + (s << 5) + p * 8;
#pragma unroll
    for (int i = 0; i < NFR; ++i) {
      const int nf = TYPE == 0 ? (i < 3 ? 3 * wn + i : 12 + 3 * wn + (i - 3)) : 5 * wn + i;
      woff[i] = 8192 + (nf >> 2) * 4096 + (lo ^ ((nf & 3) << 5));
    }
  }

  f32x4 acc[4][NFR];
#pragma unroll
  for (int a = 0; a < 4; ++a)
#pragma unroll
    for (int b = 0; b < NFR; ++b) acc[a][b] = (f32x4){0.f, 0.f, 0.f, 0.f};

  auto readfr = [&](int kt, s16x8 (&xf)[4], s16x8 (&wf)[NFR]) {
    const char* sb = smem + (kt & (K1_NS - 1)) * K1_STAGE;
#pragma unroll
    for (int b = 0; b < NFR; ++b) {
      s16x4 lo = __builtin_amdgcn_ds_read_tr16_b64_v4i16((lds_s16x4_ptr)(sb + woff[b]));
      s16x4 hi = __builtin_amdgcn_ds_read_tr16_b64_v4i16((lds_s16x4_ptr)(sb + woff[b] + 512));
      wf[b] = __builtin_shufflevector(lo, hi, 0, 1, 2, 3, 4, 5, 6, 7);
    }
#pragma unroll
    for (int a = 0; a < 4; ++a) xf[a] = *(const s16x8*)(sb + xoff + a * 1024);
  };
  auto mma = [&](const s16x8 (&xf)[4], const s16x8 (&wf)[NFR]) {
#pragma unroll
    for (int a = 0; a < 4; ++a)
#pragma unroll
      for (int b = 0; b < NFR; ++b) {
        if (TYPE == 2) acc[a][b] = __builtin_amdgcn_mfma_f32_16x16x32_bf16(xf[a], wf[b], acc[a][b], 0, 0, 0);
        else           acc[a][b] = __builtin_amdgcn_mfma_f32_16x16x32_bf16(wf[b], xf[a], acc[a][b], 0, 0, 0);
      }
  };
#define K1_WAIT(n) do { if (has3) WAIT_V2(4, n); else WAIT_V2(3, n); } while (0)
#define WAIT_V2(per, n) do { if ((per) * (n) == 12) WAIT_V(12); else if ((per) * (n) == 9) WAIT_V(9); else if ((per) * (n) == 8) WAIT_V(8); \
                             else if ((per) * (n) == 6) WAIT_V(6); else if ((per) * (n) == 4) WAIT_V(4); else if ((per) * (n) == 3) WAIT_V(3); else WAIT_V(0); } while (0)

  s16x8 xA[4], wAf[NFR], xB[4], wBf[NFR];
#define SGB(m, n) __builtin_amdgcn_sched_group_barrier(m, n, 0)
  auto pattern_full = [&]() {
    SGB(0x10, 1); SGB(0x8, 1); SGB(0x10, 1); SGB(0x8, 1); SGB(0x10, 1); SGB(0x8, 1); SGB(0x10, 1); SGB(0x8, 1);
#pragma unroll
    for (int i = 0; i < 4 + 2 * NFR; ++i) { SGB(0x100, 1); SGB(0x8, 1); }
    SGB(0x8, 4);
  };
  auto pattern_tail = [&]() {
#pragma unroll
    for (int i = 0; i < 4 + 2 * NFR; ++i) { SGB(0x100, 1); SGB(0x8, 1); }
    SGB(0x8, 8);
  };
  STAMP(1, 0);
  issue(0); issue(1); issue(2); issue(3);
  K1_WAIT(3);
  BAR;
  STAMP(1, 1);
  readfr(0, xA, wAf);
  SCHED;
  for (int kt = 0; kt < 16; kt += 2) {
    K1_WAIT(2); WAIT_L0; BAR; SCHED;
    issue(kt + 4);
    readfr(kt + 1, xB, wBf);
    mma(xA, wAf);
    pattern_full();
    SCHED;
    K1_WAIT(2); WAIT_L0; BAR; SCHED;
    issue(kt + 5);
    readfr(kt + 2, xA, wAf);
    mma(xB, wBf);
    pattern_full();
    SCHED;
  }
  K1_WAIT(2); BAR; SCHED; readfr(17, xB, wBf); mma(xA, wAf); pattern_tail(); SCHED;
  K1_WAIT(1); BAR; SCHED; readfr(18, xA, wAf); mma(xB, wBf); pattern_tail(); SCHED;
  K1_WAIT(0); BAR; SCHED; readfr(19, xB, wBf); mma(xA, wAf); pattern_tail(); SCHED;
  mma(xB, wBf);

  WAIT_L0; BAR;
  STAMP(1, 2);
  if (TYPE == 0) {
#pragma unroll
    for (int a = 0; a < 4; ++a) {
#pragma unroll
      for (int b = 0; b < 3; ++b) {
        float hv[4];
#pragma unroll
        for (int r = 0; r < 4; ++r) {
          const float gv = bfr(acc[a][b][r]), uv = bfr(acc[a][b + 3][r]);
          const float sg = bfr(frcp(1.0f + __expf(-gv)));
          hv[r] = sg * uv;
        }
        u32x2 w; w[0] = pack2(hv[0], hv[1]); w[1] = pack2(hv[2], hv[3]);
        const int f0 = wn * 48 + b * 16;
        const int ktl = f0 >> 5, gk = ((f0 >> 4) & 1) * 2 + (g >> 1);
        *(u32x2*)(smem + (((wm * 4 + a) * 6 + ktl) * 64 + gk * 16 + c) * 16 + (g & 1) * 8) = w;
      }
    }
    __syncthreads();
    char* H = ws + OFF_H + ((size_t)(mt * 8) * 54 + j * 6) * 1024;
#pragma unroll
    for (int i = 0; i < 6; ++i) {
      const int q = tid + 512 * i;
      const int m16 = q / 384, rest = q - m16 * 384;
      const u32x4 v = *(const u32x4*)(smem + q * 16);
      *(u32x4*)(H + (size_t)m16 * 54 * 1024 + rest * 16) = v;
    }
  } else if (TYPE == 1) {
#pragma unroll
    for (int a = 0; a < 4; ++a) {
#pragma unroll
      for (int b = 0; b < 5; ++b) {
        float p0 = phi_f(bfr(acc[a][b][0])), p1 = phi_f(bfr(acc[a][b][1]));
        float p2 = phi_f(bfr(acc[a][b][2])), p3 = phi_f(bfr(acc[a][b][3]));
        u32x2 w; w[0] = pack2(p0, p1); w[1] = pack2(p2, p3);
        *(u32x2*)(smem + (wm * 64 + a * 16 + c) * 656 + (wn * 80 + b * 16 + 4 * g) * 2) = w;
      }
    }
    __syncthreads();
    char* PQ = ws + OFF_PQ + (size_t)(mt * 128) * 1280 + j * 640;
#pragma unroll
    for (int i = 0; i < 10; ++i) {
      const int q = tid + 512 * i;
      const int row = q / 40, cc = q - row * 40;
      const u32x4 v = *(const u32x4*)(smem + row * 656 + cc * 16);
      *(u32x4*)(PQ + (size_t)row * 1280 + cc * 16) = v;
    }
  } else {
#pragma unroll
    for (int a = 0; a < 4; ++a)
#pragma unroll
      for (int b = 0; b < 5; ++b) {
        const int col = wn * 80 + b * 16 + c;
        float v0 = bfr(acc[a][b][0]), v1 = bfr(acc[a][b][1]), v2 = bfr(acc[a][b][2]), v3 = bfr(acc[a][b][3]);
        if (wn < 2) { v0 = phi_f(v0); v1 = phi_f(v1); v2 = phi_f(v2); v3 = phi_f(v3); }
        u32x2 w; w[0] = pack2(v0, v1); w[1] = pack2(v2, v3);
        *(u32x2*)(smem + col * 272 + (wm * 64 + a * 16 + 4 * g) * 2) = w;
      }
    __syncthreads();
    const int r32 = lane & 31, hh = lane >> 5;
    f32x16 akv, az;
#pragma unroll
    for (int i = 0; i < 16; ++i) { akv[i] = 0.f; az[i] = 0.f; }
    s16x8 ones;
#pragma unroll
    for (int i = 0; i < 8; ++i) ones[i] = (short)0x3F80;
#pragma unroll
    for (int s = 0; s < 8; ++s) {
      s16x8 af = *(const s16x8*)(smem + (20 * wave + r32) * 272 + (16 * s + 8 * hh) * 2);
      s16x8 bfv = *(const s16x8*)(smem + (160 + 20 * wave + r32) * 272 + (16 * s + 8 * hh) * 2);
      akv = __builtin_amdgcn_mfma_f32_32x32x16_bf16(af, bfv, akv, 0, 0, 0);
      az = __builtin_amdgcn_mfma_f32_32x32x16_bf16(af, ones, az, 0, 0, 0);
    }
    const int head = j * 8 + wave;
    float* P = (float*)(ws + OFF_KVP) + (size_t)(mt * 32 + head) * 504;
    if (r32 <= 20) {
#pragma unroll
      for (int q = 0; q < 3; ++q) {
        const int d0 = 8 * q + 4 * hh;
        if (d0 < 20) {
          f32x4 val;
#pragma unroll
          for (int r = 0; r < 4; ++r) val[r] = (r32 == 20) ? az[4 * q + r] : akv[4 * q + r];
          asm volatile("global_store_dwordx4 %0, %1, off sc1" :: "v"(P + r32 * 24 + d0), "v"(val) : "memory");
        }
      }
    }
    asm volatile("s_waitcnt vmcnt(0)" ::: "memory");
    __syncthreads();
    volatile int* flag = (volatile int*)(smem + 98304);
    if (tid == 0) {
      unsigned tk = __hip_atomic_fetch_add((gu32*)(ws + OFF_CNT) + j * 64, 1u, RLX_AGENT);
      if (tk == 15u) {
        __builtin_amdgcn_fence(__ATOMIC_ACQUIRE, "agent");
        asm volatile("s_waitcnt vmcnt(0)" ::: "memory");
      }
      *flag = (int)tk;
    }
    __syncthreads();
    if (*flag == 15) {
      const float* Pf = (const float*)(ws + OFF_KVP) + (size_t)head * 504;
      const int l0 = lane * 4, l1 = (lane < 62 ? lane + 64 : lane) * 4;
      f32x4 a0[16], a1[16];
#pragma unroll
      for (int m = 0; m < 16; ++m) {
        a0[m] = *(const f32x4*)(Pf + (size_t)m * 32 * 504 + l0);
        a1[m] = *(const f32x4*)(Pf + (size_t)m * 32 * 504 + l1);
      }
      f32x4 s0 = a0[0], s1 = a1[0];
#pragma unroll
      for (int m = 1; m < 16; ++m) { s0 += a0[m]; s1 += a1[m]; }
      float* kvs = (float*)(smem + 100352 + wave * 2048);
      *(f32x4*)(kvs + l0) = s0;
      if (lane < 62) *(f32x4*)(kvs + l1) = s1;
      WAIT_L0;
#pragma unroll
      for (int fr = 0; fr < 2; ++fr) {
        const int e = 16 * fr + c, d0 = 8 * g;
        const bool act = (e <= 20 && d0 < 20), act1 = act && (d0 < 16);
        const float* src = kvs + (act ? e * 24 + d0 : 0);
        f32x4 v0 = *(const f32x4*)src, v1 = *(const f32x4*)(src + (act1 ? 4 : 0));
        u32x4 w;
        w[0] = act ? pack2(v0[0], v0[1]) : 0u; w[1] = act ? pack2(v0[2], v0[3]) : 0u;
        w[2] = act1 ? pack2(v1[0], v1[1]) : 0u; w[3] = act1 ? pack2(v1[2], v1[3]) : 0u;
        *(u32x4*)(ws + OFF_KVF + ((size_t)(head * 2 + fr) * 64 + lane) * 16) = w;
      }
    }
  }
}

__global__ __launch_bounds__(512, 2) void k1_gemm(const u16* __restrict__ wqkv, const u16* __restrict__ wg,
                                                   const u16* __restrict__ wu, char* __restrict__ ws) {
  extern __shared__ __attribute__((aligned(16))) char smem[];
  const int bid = blockIdx.x;
  const int p = (bid & 7) * 30 + (bid >> 3);
  const int mh = p / 120, rem = p % 120, nt = rem >> 3, mt = mh * 8 + (rem & 7);
  if (nt < 4) k1_body<2>(wqkv, wqkv, ws, mt, nt, smem);
  else if (nt < 6) k1_body<1>(wqkv, wqkv, ws, mt, nt - 4, smem);
  else k1_body<0>(wg, wu, ws, mt, nt - 6, smem);
  STAMP(1, 3);
}

constexpr int K2_LDS = 81920 + 65536;
#define K2DD 5
constexpr int K2_D = K2DD;
constexpr int K2_HD = 5;

__global__ __launch_bounds__(256, 1) void k2_gemm(const u16* __restrict__ x, const char* __restrict__ ws, u16* __restrict__ out) {
  extern __shared__ __attribute__((aligned(16))) char smem[];
  const int tid = threadIdx.x, lane = tid & 63;
  const int wave = __builtin_amdgcn_readfirstlane(tid >> 6);
  const int g = lane >> 4, c = lane & 15;
  const int bid = blockIdx.x;
  const int mt = (bid & 7) * 4 + (bid >> 6), nt = (bid >> 3) & 7;

  const char* hsrc = ws + OFF_H + ((size_t)(mt * 4) * 54 + wave) * 1024 + lane * 16;
  const char* wsrc = ws + OFF_WC + ((size_t)nt * 54 + wave) * 5120 + lane * 16;

  STAMP(2, 0);
  f32x4 acc1[4][5], acc2[4][5];
#pragma unroll
  for (int a = 0; a < 4; ++a)
#pragma unroll
    for (int b = 0; b < 5; ++b) { acc1[a][b] = (f32x4){0.f, 0.f, 0.f, 0.f}; acc2[a][b] = (f32x4){0.f, 0.f, 0.f, 0.f}; }

  s16x8 xr[K2_D][4], wr[K2_D][5];
  auto ld = [&](int slot, int i) {
#pragma unroll
    for (int b = 0; b < 5; ++b) wr[slot][b] = *(const s16x8*)(wsrc + (size_t)(20 * i + b) * 1024);
#pragma unroll
    for (int a = 0; a < 4; ++a) xr[slot][a] = *(const s16x8*)(hsrc + (size_t)(a * 54 + 4 * i) * 1024);
  };
  auto mma1 = [&](int slot) {
#pragma unroll
    for (int a = 0; a < 4; ++a)
#pragma unroll
      for (int b = 0; b < 5; ++b) acc1[a][b] = __builtin_amdgcn_mfma_f32_16x16x32_bf16(wr[slot][b], xr[slot][a], acc1[a][b], 0, 0, 0);
  };
  {
    const char* psrc = ws + OFF_PQ + (size_t)mt * 81920 + lane * 16;
    for (int i = wave; i < 80; i += 4)
      __builtin_amdgcn_global_load_lds((const unsigned*)(psrc + i * 1024), (unsigned*)(smem + i * 1024), 16, 0, 0);
    const char* ksrc = ws + OFF_KVF + lane * 16;
    for (int i = wave; i < 64; i += 4)
      __builtin_amdgcn_global_load_lds((const unsigned*)(ksrc + i * 1024), (unsigned*)(smem + 81920 + i * 1024), 16, 0, 0);
  }
  s16x8 w2[K2_HD][5];
  const char* wosrc = ws + OFF_WO + ((size_t)nt * 32 + wave * 8) * 5120 + lane * 16;
  auto ld2 = [&](int slot, int hh) {
#pragma unroll
    for (int b = 0; b < 5; ++b) w2[slot][b] = *(const s16x8*)(wosrc + (hh * 5 + b) * 1024);
  };

#pragma unroll
  for (int i = 0; i < K2_D; ++i) ld(i, i);
  SCHED;
#pragma unroll
  for (int i = 0; i < 13; ++i) {
    mma1(i % K2_D);
    if (i + K2_D < 13) ld(i % K2_D, i + K2_D);
    else if (i + K2_D == 13) { if (wave < 2) ld(i % K2_D, 13); }
    else if (i + K2_D - 14 < K2_HD) ld2(i + K2_D - 14, i + K2_D - 14);
    SCHED;
  }
  if (wave < 2) mma1(13 % K2_D);
#pragma unroll
  for (int q = (K2_D - 1 > 0 ? K2_D - 1 : 0); q < K2_HD; ++q) ld2(q, q);
  STAMP(2, 1);
  u32x2 xw[5];
  const int tok = mt * 64 + wave * 16 + c;
#pragma unroll
  for (int b = 0; b < 5; ++b) xw[b] = *(const u32x2*)(x + (size_t)tok * D_ + nt * 80 + b * 16 + 4 * g);
  asm volatile("s_waitcnt vmcnt(30)" ::: "memory");
  BAR;
  SCHED;

#pragma unroll
  for (int hh = 0; hh < 8; ++hh) {
    const int sl = hh % K2_HD;
    s16x8 of[4];
    f32x4 d0[4], d1[4];
    const f32x4 z4 = (f32x4){0.f, 0.f, 0.f, 0.f};
    s16x8 pf[4], kvf[2];
#pragma unroll
    for (int fr = 0; fr < 2; ++fr) kvf[fr] = *(const s16x8*)(smem + 81920 + ((wave * 8 + hh) * 2 + fr) * 1024 + lane * 16);
#pragma unroll
    for (int tg = 0; tg < 4; ++tg) {
      const char* rowp = smem + (tg * 16 + c) * 1280 + (wave * 8 + hh) * 40;
      u32x2 lo = *(const u32x2*)(rowp + (g < 3 ? 16 * g : 0));
      u32x2 hi = *(const u32x2*)(rowp + (g < 2 ? 16 * g + 8 : 0));
      u32x4 pk4;
      pk4[0] = g < 3 ? lo[0] : 0u; pk4[1] = g < 3 ? lo[1] : 0u;
      pk4[2] = g < 2 ? hi[0] : 0u; pk4[3] = g < 2 ? hi[1] : 0u;
      pf[tg] = __builtin_bit_cast(s16x8, pk4);
    }
#pragma unroll
    for (int tg = 0; tg < 4; ++tg) d1[tg] = __builtin_amdgcn_mfma_f32_16x16x32_bf16(kvf[1], pf[tg], z4, 0, 0, 0);
#pragma unroll
    for (int tg = 0; tg < 4; ++tg) d0[tg] = __builtin_amdgcn_mfma_f32_16x16x32_bf16(kvf[0], pf[tg], z4, 0, 0, 0);
    float denr[4];
#pragma unroll
    for (int tg = 0; tg < 4; ++tg) denr[tg] = __shfl(d1[tg][0], 16 + c);
#pragma unroll
    for (int tg = 0; tg < 4; ++tg) {
      const float rden = frcp(bfr(bfr(denr[tg]) + 1e-6f));
      const float rden1 = g == 0 ? rden : 0.f;
      u32x4 ow;
      ow[0] = pack2(bfr(d0[tg][0]) * rden, bfr(d0[tg][1]) * rden); ow[1] = pack2(bfr(d0[tg][2]) * rden, bfr(d0[tg][3]) * rden);
      ow[2] = pack2(bfr(d1[tg][0]) * rden1, bfr(d1[tg][1]) * rden1); ow[3] = pack2(bfr(d1[tg][2]) * rden1, bfr(d1[tg][3]) * rden1);
      of[tg] = __builtin_bit_cast(s16x8, ow);
    }
#pragma unroll
    for (int a = 0; a < 4; ++a)
#pragma unroll
      for (int b = 0; b < 5; ++b) acc2[a][b] = __builtin_amdgcn_mfma_f32_16x16x32_bf16(w2[sl][b], of[a], acc2[a][b], 0, 0, 0);
    if (hh + K2_HD < 8) ld2(sl, hh + K2_HD);
    SCHED;
  }
  STAMP(2, 2);

  WAIT_L0; BAR;
  f32x4* red = (f32x4*)smem;
#pragma unroll
  for (int a = 0; a < 4; ++a) {
    if (a != wave) {
      const int sl = a - (a > wave ? 1 : 0);
#pragma unroll
      for (int b = 0; b < 5; ++b) {
        red[((wave * 3 + sl) * 10 + b) * 64 + lane] = acc1[a][b];
        red[((wave * 3 + sl) * 10 + 5 + b) * 64 + lane] = acc2[a][b];
      }
    }
  }
  __syncthreads();
  f32x4 y1[5], y2[5];
#pragma unroll
  for (int a = 0; a < 4; ++a)
    if (a == wave) {
#pragma unroll
      for (int b = 0; b < 5; ++b) { y1[b] = acc1[a][b]; y2[b] = acc2[a][b]; }
    }
#pragma unroll
  for (int sw = 0; sw < 4; ++sw) {
    if (sw != wave) {
      const int sl = wave - (wave > sw ? 1 : 0);
#pragma unroll
      for (int b = 0; b < 5; ++b) {
        y1[b] += red[((sw * 3 + sl) * 10 + b) * 64 + lane];
        y2[b] += red[((sw * 3 + sl) * 10 + 5 + b) * 64 + lane];
      }
    }
  }
#pragma unroll
  for (int b = 0; b < 5; ++b) {
    const int n = nt * 80 + b * 16 + 4 * g;
    float xv[4];
    xv[0] = __uint_as_float(xw[b][0] << 16); xv[1] = __uint_as_float(xw[b][0] & 0xffff0000u);
    xv[2] = __uint_as_float(xw[b][1] << 16); xv[3] = __uint_as_float(xw[b][1] & 0xffff0000u);
    float o[4];
#pragma unroll
    for (int r = 0; r < 4; ++r) {
      const float y = bfr(y1[b][r]);
      const float gs = bfr(y * frcp(1.0f + __expf(-y)));
      const float s = bfr(y2[b][r]);
      o[r] = xv[r] + bfr(s * gs);
    }
    u32x2 w; w[0] = pack2(o[0], o[1]); w[1] = pack2(o[2], o[3]);
    *(u32x2*)(out + (size_t)tok * D_ + n) = w;
  }
  STAMP(2, 3);
}

extern "C" void kernel_launch(void* const* d_in, const int* in_sizes, int n_in,
                              void* d_out, int out_size, void* d_ws, size_t ws_size,
                              hipStream_t stream) {
  const u16* x = (const u16*)d_in[0];
  const u16* wqkv = (const u16*)d_in[1];
  const u16* wo = (const u16*)d_in[2];
  const u16* wg = (const u16*)d_in[4];
  const u16* wu = (const u16*)d_in[5];
  const u16* wd = (const u16*)d_in[6];
  char* ws = (char*)d_ws;
  u16* out = (u16*)d_out;

  hipFuncSetAttribute((const void*)k1_gemm, hipFuncAttributeMaxDynamicSharedMemorySize, K1_NS * K1_STAGE);
  hipFuncSetAttribute((const void*)k2_gemm, hipFuncAttributeMaxDynamicSharedMemorySize, K2_LDS);

  k0_prep<<<256 + 78, 256, 0, stream>>>(x, wo, wd, ws);
  k1_gemm<<<240, 512, K1_NS * K1_STAGE, stream>>>(wqkv, wg, wu, ws);
  k2_gemm<<<256, 256, K2_LDS, stream>>>(x, ws, out);
}
```

```cpp
#include <hip/hip_runtime.h>
#include <hip/hip_bf16.h>
#include <stdint.h>

typedef unsigned short u16;
typedef __attribute__((ext_vector_type(8))) short s16x8;
typedef __attribute__((ext_vector_type(4))) short s16x4;
typedef __attribute__((ext_vector_type(4))) float f32x4;
typedef __attribute__((ext_vector_type(16))) float f32x16;
typedef __attribute__((ext_vector_type(2))) unsigned u32x2;
typedef __attribute__((ext_vector_type(4))) unsigned u32x4;
typedef __attribute__((address_space(3))) s16x4* lds_s16x4_ptr;
typedef __attribute__((address_space(1))) unsigned gu32;
typedef __attribute__((address_space(1))) unsigned long long gu64;

#define RLX_AGENT __ATOMIC_RELAXED, __HIP_MEMORY_SCOPE_AGENT

constexpr int S_ = 2048, D_ = 640, F_ = 1728, NQKV_ = 1920;

constexpr size_t OFF_CNT   = 0;
constexpr size_t OFF_XN    = 8192;
constexpr size_t SZ_XN     = (size_t)S_ * D_ * 2;
constexpr size_t OFF_H     = OFF_XN + SZ_XN;
constexpr size_t SZ_H      = (size_t)S_ * F_ * 2;
constexpr size_t OFF_PQ    = OFF_H + SZ_H;
constexpr size_t SZ_PQ     = (size_t)S_ * D_ * 2;
constexpr size_t OFF_WC    = OFF_PQ + SZ_PQ;
constexpr size_t SZ_WC     = (size_t)8 * 54 * 5 * 1024;
constexpr size_t OFF_WO    = OFF_WC + SZ_WC;
constexpr size_t SZ_WO     = (size_t)8 * 32 * 5 * 1024;
constexpr size_t OFF_KVP   = OFF_WO + SZ_WO;
constexpr size_t SZ_KVP    = (size_t)16 * 32 * 504 * 4;
constexpr size_t OFF_KVF   = OFF_KVP + SZ_KVP;
constexpr size_t SZ_KVF    = 65536;

__device__ __forceinline__ float bf2f(u16 v) { return __uint_as_float(((unsigned)v) << 16); }
__device__ __forceinline__ u16 f2bf(float f) { return __builtin_bit_cast(u16, (__bf16)f); }
__device__ __forceinline__ float bfr(float f) { return bf2f(f2bf(f)); }
__device__ __forceinline__ unsigned pack2(float a, float b) { return (unsigned)f2bf(a) | ((unsigned)f2bf(b) << 16); }
__device__ __forceinline__ float phi_f(float t) {
  float p = t > 0.f ? bfr(t + 1.0f) : bfr(__expf(t));
  return bfr(p + 1e-6f);
}
__device__ __forceinline__ int aswz(int row) { int x = (row >> 2) & 3; return (((x ^ (x >> 1)) & 1) << 1) | (x >> 1); }

#define WAIT_V(n) asm volatile("s_waitcnt vmcnt(" #n ")" ::: "memory")
#define STAMP(kid, idx)
#define WAIT_L0 asm volatile("s_waitcnt lgkmcnt(0)" ::: "memory")
#define BAR __builtin_amdgcn_s_barrier()
#define SCHED __builtin_amdgcn_sched_barrier(0)
__device__ __forceinline__ float frcp(float v) { return __builtin_amdgcn_rcpf(v); }

__global__ __launch_bounds__(256) void k0_prep(const u16* __restrict__ x, const u16* __restrict__ wo,
                                               const u16* __restrict__ wdown, char* __restrict__ ws) {
  const int tid = threadIdx.x;
  if (blockIdx.x >= 256) {
    const int t = (blockIdx.x - 256) * 256 + tid;
    if (t < 216 * 80 + 128 * 80) {
    if (t < 216 * 80) {
      const int koct = t / 80, noct = t - koct * 80;
      const u16* src = wdown + (size_t)(koct * 8) * D_ + noct * 8;
      u32x4 r[8];
#pragma unroll
      for (int jj = 0; jj < 8; ++jj) r[jj] = *(const u32x4*)(src + (size_t)jj * D_);
      const int n0 = noct * 8, nt = n0 / 80, nn = n0 - nt * 80, nf = nn >> 4, c0 = nn & 15, kt = koct >> 2, gg = koct & 3;
      char* dst = ws + OFF_WC + ((size_t)((nt * 54 + kt) * 5 + nf) * 64 + gg * 16 + c0) * 16;
#pragma unroll
      for (int i = 0; i < 4; ++i) {
        u32x4 lo, hi;
#pragma unroll
        for (int q = 0; q < 4; ++q) {
          lo[q] = (r[2 * q][i] & 0xffffu) | (r[2 * q + 1][i] << 16);
          hi[q] = (r[2 * q][i] >> 16) | (r[2 * q + 1][i] & 0xffff0000u);
        }
        *(u32x4*)(dst + (2 * i) * 16) = lo;
        *(u32x4*)(dst + (2 * i + 1) * 16) = hi;
      }
    } else {
      const int t2 = t - 216 * 80, hg = t2 / 80, noct = t2 - hg * 80, hd = hg >> 2, gg = hg & 3;
      const u16* src = wo + (size_t)(hd * 20) * D_ + noct * 8;
      u32x4 r[4], e[4];
#pragma unroll
      for (int jj = 0; jj < 4; ++jj) r[jj] = *(const u32x4*)(src + (size_t)(4 * gg + jj) * D_);
#pragma unroll
      for (int jj = 0; jj < 4; ++jj) e[jj] = *(const u32x4*)(src + (size_t)(gg == 0 ? 16 + jj : 0) * D_);
      const int n0 = noct * 8, nt = n0 / 80, nn = n0 - nt * 80, nf = nn >> 4, c0 = nn & 15;
      char* dst = ws + OFF_WO + ((size_t)((nt * 32 + hd) * 5 + nf) * 64 + c0) * 16;
#pragma unroll
      for (int i = 0; i < 4; ++i) {
        u32x4 lo, hi;
        lo[0] = (r[0][i] & 0xffffu) | (r[1][i] << 16);
        lo[1] = (r[2][i] & 0xffffu) | (r[3][i] << 16);
        hi[0] = (r[0][i] >> 16) | (r[1][i] & 0xffff0000u);
        hi[1] = (r[2][i] >> 16) | (r[3][i] & 0xffff0000u);
        lo[2] = gg == 0 ? ((e[0][i] & 0xffffu) | (e[1][i] << 16)) : 0u; lo[3] = gg == 0 ? ((e[2][i] & 0xffffu) | (e[3][i] << 16)) : 0u;
        hi[2] = gg == 0 ? ((e[0][i] >> 16) | (e[1][i] & 0xffff0000u)) : 0u; hi[3] = gg == 0 ? ((e[2][i] >> 16) | (e[3][i] & 0xffff0000u)) : 0u;
        *(u32x4*)(dst + (gg * 16 + 2 * i) * 16) = lo;
        *(u32x4*)(dst + (gg * 16 + 2 * i + 1) * 16) = hi;
      }
    }
    }
  } else {
    if (blockIdx.x == 0 && tid < 4) ((unsigned*)(ws + OFF_CNT))[tid * 64] = 0u;
    const int row = blockIdx.x * 8 + (tid >> 5);
    const int l32 = tid & 31;
    const u16* xr = x + (size_t)row * D_;
    float v[20];
    float sum = 0.f;
#pragma unroll
    for (int i = 0; i < 5; ++i) {
      u32x2 w = *(const u32x2*)(xr + (l32 + 32 * i) * 4);
      v[4 * i + 0] = __uint_as_float(w[0] << 16);
      v[4 * i + 1] = __uint_as_float(w[0] & 0xffff0000u);
      v[4 * i + 2] = __uint_as_float(w[1] << 16);
      v[4 * i + 3] = __uint_as_float(w[1] & 0xffff0000u);
      sum += v[4 * i] + v[4 * i + 1] + v[4 * i + 2] + v[4 * i + 3];
    }
#pragma unroll
    for (int o = 16; o >= 1; o >>= 1) sum += __shfl_xor(sum, o);
    const float mean = sum * (1.0f / D_);
    float sq = 0.f;
#pragma unroll
    for (int i = 0; i < 20; ++i) { float d = v[i] - mean; sq += d * d; }
#pragma unroll
    for (int o = 16; o >= 1; o >>= 1) sq += __shfl_xor(sq, o);
    const float mb = bfr(mean);
    const float vb = bfr(sq * (1.0f / D_));
    const float sd = bfr(sqrtf(bfr(vb + 1e-5f)));
    u16* xn = (u16*)(ws + OFF_XN);
    const int mt = row >> 7, r = row & 127, sw = aswz(r);
#pragma unroll
    for (int i = 0; i < 5; ++i) {
      const int k = (l32 + 32 * i) * 4;
      float o0 = bfr(v[4 * i + 0] - mb) / sd, o1 = bfr(v[4 * i + 1] - mb) / sd;
      float o2 = bfr(v[4 * i + 2] - mb) / sd, o3 = bfr(v[4 * i + 3] - mb) / sd;
      u32x2 w; w[0] = pack2(o0, o1); w[1] = pack2(o2, o3);
      const int kt = k >> 5, ch = (k >> 3) & 3, hf = (k >> 2) & 1;
      size_t off = ((size_t)(mt * 20 + kt) * 128 + r) * 32 + ((ch ^ sw) << 3) + (hf << 2);
      *(u32x2*)(xn + off) = w;
    }
  }
}

constexpr int K1_STAGE = 32768;
constexpr int K1_NS = 4;

template <int TYPE>
__device__ __forceinline__ void k1_body(const u16* __restrict__ wA, const u16* __restrict__ wB, char* __restrict__ ws,
                                        int mt, int j, char* smem) {
  constexpr int NCB = TYPE == 0 ? 6 : 5;
  constexpr int NFR = TYPE == 0 ? 6 : 5;
  constexpr int LDW = TYPE == 0 ? F_ : NQKV_;
  const int tid = threadIdx.x, lane = tid & 63;
  const int wave = __builtin_amdgcn_readfirstlane(tid >> 6);
  const int wm = wave >> 2, wn = wave & 3;
  const int g = lane >> 4, c = lane & 15;

  const char* asrc = ws + OFF_XN + (size_t)mt * 20 * 8192 + wave * 1024 + lane * 16;
  const u16* bsrc[3];
#pragma unroll
  for (int t = 0; t < 3; ++t) {
    int ii = wave + 8 * t; if (ii >= NCB * 4) ii = wave;
    const int cb = ii >> 2, krow = (ii & 3) * 8 + (lane >> 3), u = lane & 7;
    const int sk = ((krow >> 1) & 1) | (((krow >> 3) & 1) << 1);
    const int ch = (u >> 1) ^ sk;
    const int lc = cb * 64 + ch * 16 + (u & 1) * 8;
    const u16* p;
    if (TYPE == 0) p = (lc < 192) ? (wA + j * 192 + lc) : (wB + j * 192 + (lc - 192));
    else if (TYPE == 1) p = wA + j * 320 + lc;
    else p = (lc < 160) ? (wA + 640 + j * 160 + lc) : (wA + 1280 + j * 160 + (lc - 160));
    bsrc[t] = p + (size_t)krow * LDW;
  }
  const bool has3 = (NCB == 6) || (wave < 4);

  auto issue = [&](int kt) {
    char* sb = smem + (kt & (K1_NS - 1)) * K1_STAGE;
    __builtin_amdgcn_global_load_lds((const unsigned*)(asrc + (size_t)kt * 8192), (unsigned*)(sb + wave * 1024), 16, 0, 0);
    __builtin_amdgcn_global_load_lds((const unsigned*)(bsrc[0] + (size_t)kt * 32 * LDW), (unsigned*)(sb + 8192 + wave * 1024), 16, 0, 0);
    __builtin_amdgcn_global_load_lds((const unsigned*)(bsrc[1] + (size_t)kt * 32 * LDW), (unsigned*)(sb + 8192 + (wave + 8) * 1024), 16, 0, 0);
    if (has3) __builtin_amdgcn_global_load_lds((const unsigned*)(bsrc[2] + (size_t)kt * 32 * LDW), (unsigned*)(sb + 8192 + (wave + 16) * 1024), 16, 0, 0);
  };

  const int xoff = (wm * 64 + c) * 64 + ((g ^ aswz(c)) << 4);
  int woff[NFR];
  {
    const int q = (lane & 15) >> 2, p = lane & 3;
    const int s = (q >> 1) | ((g & 1) << 1);
    const int lo = (8 * g + q) * 128 + (s << 5) + p * 8;
#pragma unroll
    for (int i = 0; i < NFR; ++i) {
      const int nf = TYPE == 0 ? (i < 3 ? 3 * wn + i : 12 + 3 * wn + (i - 3)) : 5 * wn + i;
      woff[i] = 8192 + (nf >> 2) * 4096 + (lo ^ ((nf & 3) << 5));
    }
  }

  f32x4 acc[4][NFR];
#pragma unroll
  for (int a = 0; a < 4; ++a)
#pragma unroll
    for (int b = 0; b < NFR; ++b) acc[a][b] = (f32x4){0.f, 0.f, 0.f, 0.f};

  auto readfr = [&](int kt, s16x8 (&xf)[4], s16x8 (&wf)[NFR]) {
    const char* sb = smem + (kt & (K1_NS - 1)) * K1_STAGE;
#pragma unroll
    for (int b = 0; b < NFR; ++b) {
      s16x4 lo = __builtin_amdgcn_ds_read_tr16_b64_v4i16((lds_s16x4_ptr)(sb + woff[b]));
      s16x4 hi = __builtin_amdgcn_ds_read_tr16_b64_v4i16((lds_s16x4_ptr)(sb + woff[b] + 512));
      wf[b] = __builtin_shufflevector(lo, hi, 0, 1, 2, 3, 4, 5, 6, 7);
    }
#pragma unroll
    for (int a = 0; a < 4; ++a) xf[a] = *(const s16x8*)(sb + xoff + a * 1024);
  };
  auto mma = [&](const s16x8 (&xf)[4], const s16x8 (&wf)[NFR]) {
#pragma unroll
    for (int a = 0; a < 4; ++a)
#pragma unroll
      for (int b = 0; b < NFR; ++b) {
        if (TYPE == 2) acc[a][b] = __builtin_amdgcn_mfma_f32_16x16x32_bf16(xf[a], wf[b], acc[a][b], 0, 0, 0);
        else           acc[a][b] = __builtin_amdgcn_mfma_f32_16x16x32_bf16(wf[b], xf[a], acc[a][b], 0, 0, 0);
      }
  };
#define K1_WAIT(n) do { if (has3) WAIT_V2(4, n); else WAIT_V2(3, n); } while (0)
#define WAIT_V2(per, n) do { if ((per) * (n) == 12) WAIT_V(12); else if ((per) * (n) == 9) WAIT_V(9); else if ((per) * (n) == 8) WAIT_V(8); \
                             else if ((per) * (n) == 6) WAIT_V(6); else if ((per) * (n) == 4) WAIT_V(4); else if ((per) * (n) == 3) WAIT_V(3); else WAIT_V(0); } while (0)

  s16x8 xA[4], wAf[NFR], xB[4], wBf[NFR];
#define SGB(m, n) __builtin_amdgcn_sched_group_barrier(m, n, 0)
  auto pattern_full = [&]() {
    SGB(0x10, 1); SGB(0x8, 1); SGB(0x10, 1); SGB(0x8, 1); SGB(0x10, 1); SGB(0x8, 1); SGB(0x10, 1); SGB(0x8, 1);
#pragma unroll
    for (int i = 0; i < 4 + 2 * NFR; ++i) { SGB(0x100, 1); SGB(0x8, 1); }
    SGB(0x8, 4);
  };
  auto pattern_tail = [&]() {
#pragma unroll
    for (int i = 0; i < 4 + 2 * NFR; ++i) { SGB(0x100, 1); SGB(0x8, 1); }
    SGB(0x8, 8);
  };
  STAMP(1, 0);
  issue(0); issue(1); issue(2); issue(3);
  K1_WAIT(3);
  BAR;
  STAMP(1, 1);
  readfr(0, xA, wAf);
  SCHED;
  for (int kt = 0; kt < 16; kt += 2) {
    K1_WAIT(2); WAIT_L0; BAR; SCHED;
    issue(kt + 4);
    readfr(kt + 1, xB, wBf);
    mma(xA, wAf);
    pattern_full();
    SCHED;
    K1_WAIT(2); WAIT_L0; BAR; SCHED;
    issue(kt + 5);
    readfr(kt + 2, xA, wAf);
    mma(xB, wBf);
    pattern_full();
    SCHED;
  }
  K1_WAIT(2); BAR; SCHED; readfr(17, xB, wBf); mma(xA, wAf); pattern_tail(); SCHED;
  K1_WAIT(1); BAR; SCHED; readfr(18, xA, wAf); mma(xB, wBf); pattern_tail(); SCHED;
  K1_WAIT(0); BAR; SCHED; readfr(19, xB, wBf); mma(xA, wAf); pattern_tail(); SCHED;
  mma(xB, wBf);

  WAIT_L0; BAR;
  STAMP(1, 2);
  if (TYPE == 0) {
#pragma unroll
    for (int a = 0; a < 4; ++a) {
#pragma unroll
      for (int b = 0; b < 3; ++b) {
        float hv[4];
#pragma unroll
        for (int r = 0; r < 4; ++r) {
          const float gv = bfr(acc[a][b][r]), uv = bfr(acc[a][b + 3][r]);
          const float sg = bfr(frcp(1.0f + __expf(-gv)));
          hv[r] = sg * uv;
        }
        u32x2 w; w[0] = pack2(hv[0], hv[1]); w[1] = pack2(hv[2], hv[3]);
        const int f0 = wn * 48 + b * 16;
        const int ktl = f0 >> 5, gk = ((f0 >> 4) & 1) * 2 + (g >> 1);
        *(u32x2*)(smem + (((wm * 4 + a) * 6 + ktl) * 64 + gk * 16 + c) * 16 + (g & 1) * 8) = w;
      }
    }
    __syncthreads();
    char* H = ws + OFF_H + ((size_t)(mt * 8) * 54 + j * 6) * 1024;
#pragma unroll
    for (int i = 0; i < 6; ++i) {
      const int q = tid + 512 * i;
      const int m16 = q / 384, rest = q - m16 * 384;
      const u32x4 v = *(const u32x4*)(smem + q * 16);
      *(u32x4*)(H + (size_t)m16 * 54 * 1024 + rest * 16) = v;
    }
  } else if (TYPE == 1) {
#pragma unroll
    for (int a = 0; a < 4; ++a) {
#pragma unroll
      for (int b = 0; b < 5; ++b) {
        float p0 = phi_f(bfr(acc[a][b][0])), p1 = phi_f(bfr(acc[a][b][1]));
        float p2 = phi_f(bfr(acc[a][b][2])), p3 = phi_f(bfr(acc[a][b][3]));
        u32x2 w; w[0] = pack2(p0, p1); w[1] = pack2(p2, p3);
        *(u32x2*)(smem + (wm * 64 + a * 16 + c) * 656 + (wn * 80 + b * 16 + 4 * g) * 2) = w;
      }
    }
    __syncthreads();
    char* PQ = ws + OFF_PQ + (size_t)(mt * 128) * 1280 + j * 640;
#pragma unroll
    for (int i = 0; i < 10; ++i) {
      const int q = tid + 512 * i;
      const int row = q / 40, cc = q - row * 40;
      const u32x4 v = *(const u32x4*)(smem + row * 656 + cc * 16);
      *(u32x4*)(PQ + (size_t)row * 1280 + cc * 16) = v;
    }
  } else {
#pragma unroll
    for (int a = 0; a < 4; ++a)
#pragma unroll
      for (int b = 0; b < 5; ++b) {
        const int col = wn * 80 + b * 16 + c;
        float v0 = bfr(acc[a][b][0]), v1 = bfr(acc[a][b][1]), v2 = bfr(acc[a][b][2]), v3 = bfr(acc[a][b][3]);
        if (wn < 2) { v0 = phi_f(v0); v1 = phi_f(v1); v2 = phi_f(v2); v3 = phi_f(v3); }
        u32x2 w; w[0] = pack2(v0, v1); w[1] = pack2(v2, v3);
        *(u32x2*)(smem + col * 272 + (wm * 64 + a * 16 + 4 * g) * 2) = w;
      }
    __syncthreads();
    const int r32 = lane & 31, hh = lane >> 5;
    f32x16 akv, az;
#pragma unroll
    for (int i = 0; i < 16; ++i) { akv[i] = 0.f; az[i] = 0.f; }
    s16x8 ones;
#pragma unroll
    for (int i = 0; i < 8; ++i) ones[i] = (short)0x3F80;
#pragma unroll
    for (int s = 0; s < 8; ++s) {
      s16x8 af = *(const s16x8*)(smem + (20 * wave + r32) * 272 + (16 * s + 8 * hh) * 2);
      s16x8 bfv = *(const s16x8*)(smem + (160 + 20 * wave + r32) * 272 + (16 * s + 8 * hh) * 2);
      akv = __builtin_amdgcn_mfma_f32_32x32x16_bf16(af, bfv, akv, 0, 0, 0);
      az = __builtin_amdgcn_mfma_f32_32x32x16_bf16(af, ones, az, 0, 0, 0);
    }
    const int head = j * 8 + wave;
    float* P = (float*)(ws + OFF_KVP) + (size_t)(mt * 32 + head) * 504;
    if (r32 <= 20) {
#pragma unroll
      for (int q = 0; q < 3; ++q) {
        const int d0 = 8 * q + 4 * hh;
        if (d0 < 20) {
          f32x4 val;
#pragma unroll
          for (int r = 0; r < 4; ++r) val[r] = (r32 == 20) ? az[4 * q + r] : akv[4 * q + r];
          asm volatile("global_store_dwordx4 %0, %1, off sc1" :: "v"(P + r32 * 24 + d0), "v"(val) : "memory");
        }
      }
    }
    asm volatile("s_waitcnt vmcnt(0)" ::: "memory");
    __syncthreads();
    volatile int* flag = (volatile int*)(smem + 98304);
    if (tid == 0) {
      unsigned tk = __hip_atomic_fetch_add((gu32*)(ws + OFF_CNT) + j * 64, 1u, RLX_AGENT);
      if (tk == 15u) {
        __builtin_amdgcn_fence(__ATOMIC_ACQUIRE, "agent");
        asm volatile("s_waitcnt vmcnt(0)" ::: "memory");
      }
      *flag = (int)tk;
    }
    __syncthreads();
    if (*flag == 15) {
      const float* Pf = (const float*)(ws + OFF_KVP) + (size_t)head * 504;
      const int l0 = lane * 4, l1 = (lane < 62 ? lane + 64 : lane) * 4;
      f32x4 a0[16], a1[16];
#pragma unroll
      for (int m = 0; m < 16; ++m) {
        a0[m] = *(const f32x4*)(Pf + (size_t)m * 32 * 504 + l0);
        a1[m] = *(const f32x4*)(Pf + (size_t)m * 32 * 504 + l1);
      }
      f32x4 s0 = a0[0], s1 = a1[0];
#pragma unroll
      for (int m = 1; m < 16; ++m) { s0 += a0[m]; s1 += a1[m]; }
      float* kvs = (float*)(smem + 100352 + wave * 2048);
      *(f32x4*)(kvs + l0) = s0;
      if (lane < 62) *(f32x4*)(kvs + l1) = s1;
      WAIT_L0;
#pragma unroll
      for (int fr = 0; fr < 2; ++fr) {
        const int e = 16 * fr + c, d0 = 8 * g;
        const bool act = (e <= 20 && d0 < 20), act1 = act && (d0 < 16);
        const float* src = kvs + (act ? e * 24 + d0 : 0);
        f32x4 v0 = *(const f32x4*)src, v1 = *(const f32x4*)(src + (act1 ? 4 : 0));
        u32x4 w;
        w[0] = act ? pack2(v0[0], v0[1]) : 0u; w[1] = act ? pack2(v0[2], v0[3]) : 0u;
        w[2] = act1 ? pack2(v1[0], v1[1]) : 0u; w[3] = act1 ? pack2(v1[2], v1[3]) : 0u;
        *(u32x4*)(ws + OFF_KVF + ((size_t)(head * 2 + fr) * 64 + lane) * 16) = w;
      }
    }
  }
}

__global__ __launch_bounds__(512, 2) void k1_gemm(const u16* __restrict__ wqkv, const u16* __restrict__ wg,
                                                   const u16* __restrict__ wu, char* __restrict__ ws) {
  extern __shared__ __attribute__((aligned(16))) char smem[];
  const int bid = blockIdx.x;
  const int p = (bid & 7) * 30 + (bid >> 3);
  const int mh = p / 120, rem = p % 120, nt = rem >> 3, mt = mh * 8 + (rem & 7);
  if (nt < 4) k1_body<2>(wqkv, wqkv, ws, mt, nt, smem);
  else if (nt < 6) k1_body<1>(wqkv, wqkv, ws, mt, nt - 4, smem);
  else k1_body<0>(wg, wu, ws, mt, nt - 6, smem);
  STAMP(1, 3);
}

constexpr int K2_LDS = 81920 + 65536;
#define K2DD 5
constexpr int K2_D = K2DD;
constexpr int K2_HD = 5;

__global__ __launch_bounds__(256, 1) void k2_gemm(const u16* __restrict__ x, const char* __restrict__ ws, u16* __restrict__ out) {
  extern __shared__ __attribute__((aligned(16))) char smem[];
  const int tid = threadIdx.x, lane = tid & 63;
  const int wave = __builtin_amdgcn_readfirstlane(tid >> 6);
  const int g = lane >> 4, c = lane & 15;
  const int bid = blockIdx.x;
  const int mt = (bid & 7) * 4 + (bid >> 6), nt = (bid >> 3) & 7;

  const char* hsrc = ws + OFF_H + ((size_t)(mt * 4) * 54 + wave) * 1024 + lane * 16;
  const char* wsrc = ws + OFF_WC + ((size_t)nt * 54 + wave) * 5120 + lane * 16;

  STAMP(2, 0);
  f32x4 acc1[4][5], acc2[4][5];
#pragma unroll
  for (int a = 0; a < 4; ++a)
#pragma unroll
    for (int b = 0; b < 5; ++b) { acc1[a][b] = (f32x4){0.f, 0.f, 0.f, 0.f}; acc2[a][b] = (f32x4){0.f, 0.f, 0.f, 0.f}; }

  s16x8 xr[K2_D][4], wr[K2_D][5];
  auto ld = [&](int slot, int i) {
#pragma unroll
    for (int b = 0; b < 5; ++b) wr[slot][b] = *(const s16x8*)(wsrc + (size_t)(20 * i + b) * 1024);
#pragma unroll
    for (int a = 0; a < 4; ++a) xr[slot][a] = *(const s16x8*)(hsrc + (size_t)(a * 54 + 4 * i) * 1024);
  };
  auto mma1 = [&](int slot) {
#pragma unroll
    for (int a = 0; a < 4; ++a)
#pragma unroll
      for (int b = 0; b < 5; ++b) acc1[a][b] = __builtin_amdgcn_mfma_f32_16x16x32_bf16(wr[slot][b], xr[slot][a], acc1[a][b], 0, 0, 0);
  };
  {
    const char* psrc = ws + OFF_PQ + (size_t)mt * 81920 + lane * 16;
    for (int i = wave; i < 80; i += 4)
      __builtin_amdgcn_global_load_lds((const unsigned*)(psrc + i * 1024), (unsigned*)(smem + i * 1024), 16, 0, 0);
    const char* ksrc = ws + OFF_KVF + lane * 16;
    for (int i = wave; i < 64; i += 4)
      __builtin_amdgcn_global_load_lds((const unsigned*)(ksrc + i * 1024), (unsigned*)(smem + 81920 + i * 1024), 16, 0, 0);
  }
  s16x8 w2[K2_HD][5];
  const char* wosrc = ws + OFF_WO + ((size_t)nt * 32 + wave * 8) * 5120 + lane * 16;
  auto ld2 = [&](int slot, int hh) {
#pragma unroll
    for (int b = 0; b < 5; ++b) w2[slot][b] = *(const s16x8*)(wosrc + (hh * 5 + b) * 1024);
  };

#pragma unroll
  for (int i = 0; i < K2_D; ++i) ld(i, i);
  SCHED;
#pragma unroll
  for (int i = 0; i < 13; ++i) {
    mma1(i % K2_D);
    if (i + K2_D < 13) ld(i % K2_D, i + K2_D);
    else if (i + K2_D == 13) { if (wave < 2) ld(i % K2_D, 13); }
    else if (i + K2_D - 14 < K2_HD) ld2(i + K2_D - 14, i + K2_D - 14);
    SCHED;
  }
  if (wave < 2) mma1(13 % K2_D);
#pragma unroll
  for (int q = (K2_D - 1 > 0 ? K2_D - 1 : 0); q < K2_HD; ++q) ld2(q, q);
  STAMP(2, 1);
  u32x2 xw[5];
  const int tok = mt * 64 + wave * 16 + c;
#pragma unroll
  for (int b = 0; b < 5; ++b) xw[b] = *(const u32x2*)(x + (size_t)tok * D_ + nt * 80 + b * 16 + 4 * g);
  asm volatile("s_waitcnt vmcnt(30)" ::: "memory");
  BAR;
  SCHED;

#pragma unroll
  for (int hh = 0; hh < 8; ++hh) {
    const int sl = hh % K2_HD;
    s16x8 of[4];
    f32x4 d0[4], d1[4];
    const f32x4 z4 = (f32x4){0.f, 0.f, 0.f, 0.f};
    s16x8 pf[4], kvf[2];
#pragma unroll
    for (int fr = 0; fr < 2; ++fr) kvf[fr] = *(const s16x8*)(smem + 81920 + ((wave * 8 + hh) * 2 + fr) * 1024 + lane * 16);
#pragma unroll
    for (int tg = 0; tg < 4; ++tg) {
      const char* rowp = smem + (tg * 16 + c) * 1280 + (wave * 8 + hh) * 40;
      u32x2 lo = *(const u32x2*)(rowp + (g < 3 ? 16 * g : 0));
      u32x2 hi = *(const u32x2*)(rowp + (g < 2 ? 16 * g + 8 : 0));
      u32x4 pk4;
      pk4[0] = g < 3 ? lo[0] : 0u; pk4[1] = g < 3 ? lo[1] : 0u;
      pk4[2] = g < 2 ? hi[0] : 0u; pk4[3] = g < 2 ? hi[1] : 0u;
      pf[tg] = __builtin_bit_cast(s16x8, pk4);
    }
#pragma unroll
    for (int tg = 0; tg < 4; ++tg) d1[tg] = __builtin_amdgcn_mfma_f32_16x16x32_bf16(kvf[1], pf[tg], z4, 0, 0, 0);
#pragma unroll
    for (int tg = 0; tg < 4; ++tg) d0[tg] = __builtin_amdgcn_mfma_f32_16x16x32_bf16(kvf[0], pf[tg], z4, 0, 0, 0);
    float denr[4];
#pragma unroll
    for (int tg = 0; tg < 4; ++tg) denr[tg] = __shfl(d1[tg][0], 16 + c);
#pragma unroll
    for (int tg = 0; tg < 4; ++tg) {
      const float rden = frcp(bfr(bfr(denr[tg]) + 1e-6f));
      const float rden1 = g == 0 ? rden : 0.f;
      u32x4 ow;
      ow[0] = pack2(bfr(d0[tg][0]) * rden, bfr(d0[tg][1]) * rden); ow[1] = pack2(bfr(d0[tg][2]) * rden, bfr(d0[tg][3]) * rden);
      ow[2] = pack2(bfr(d1[tg][0]) * rden1, bfr(d1[tg][1]) * rden1); ow[3] = pack2(bfr(d1[tg][2]) * rden1, bfr(d1[tg][3]) * rden1);
      of[tg] = __builtin_bit_cast(s16x8, ow);
    }
#pragma unroll
    for (int a = 0; a < 4; ++a)
#pragma unroll
      for (int b = 0; b < 5; ++b) acc2[a][b] = __builtin_amdgcn_mfma_f32_16x16x32_bf16(w2[sl][b], of[a], acc2[a][b], 0, 0, 0);
    if (hh + K2_HD < 8) ld2(sl, hh + K2_HD);
    SCHED;
  }
  STAMP(2, 2);

  WAIT_L0; BAR;
  f32x4* red = (f32x4*)smem;
#pragma unroll
  for (int a = 0; a < 4; ++a) {
    if (a != wave) {
      const int sl = a - (a > wave ? 1 : 0);
#pragma unroll
      for (int b = 0; b < 5; ++b) {
        red[((wave * 3 + sl) * 10 + b) * 64 + lane] = acc1[a][b];
        red[((wave * 3 + sl) * 10 + 5 + b) * 64 + lane] = acc2[a][b];
      }
    }
  }
  __syncthreads();
  f32x4 y1[5], y2[5];
#pragma unroll
  for (int a = 0; a < 4; ++a)
    if (a == wave) {
#pragma unroll
      for (int b = 0; b < 5; ++b) { y1[b] = acc1[a][b]; y2[b] = acc2[a][b]; }
    }
#pragma unroll
  for (int sw = 0; sw < 4; ++sw) {
    if (sw != wave) {
      const int sl = wave - (wave > sw ? 1 : 0);
#pragma unroll
      for (int b = 0; b < 5; ++b) {
        y1[b] += red[((sw * 3 + sl) * 10 + b) * 64 + lane];
        y2[b] += red[((sw * 3 + sl) * 10 + 5 + b) * 64 + lane];
      }
    }
  }
#pragma unroll
  for (int b = 0; b < 5; ++b) {
    const int n = nt * 80 + b * 16 + 4 * g;
    float xv[4];
    xv[0] = __uint_as_float(xw[b][0] << 16); xv[1] = __uint_as_float(xw[b][0] & 0xffff0000u);
    xv[2] = __uint_as_float(xw[b][1] << 16); xv[3] = __uint_as_float(xw[b][1] & 0xffff0000u);
    float o[4];
#pragma unroll
    for (int r = 0; r < 4; ++r) {
      const float y = bfr(y1[b][r]);
      const float gs = bfr(y * frcp(1.0f + __expf(-y)));
      const float s = bfr(y2[b][r]);
      o[r] = xv[r] + bfr(s * gs);
    }
    u32x2 w; w[0] = pack2(o[0], o[1]); w[1] = pack2(o[2], o[3]);
    *(u32x2*)(out + (size_t)tok * D_ + n) = w;
  }
  STAMP(2, 3);
}

extern "C" void kernel_launch(void* const* d_in, const int* in_sizes, int n_in,
                              void* d_out, int out_size, void* d_ws, size_t ws_size,
                              hipStream_t stream) {
  const u16* x = (const u16*)d_in[0];
  const u16* wqkv = (const u16*)d_in[1];
  const u16* wo = (const u16*)d_in[2];
  const u16* wg = (const u16*)d_in[4];
  const u16* wu = (const u16*)d_in[5];
  const u16* wd = (const u16*)d_in[6];
  char* ws = (char*)d_ws;
  u16* out = (u16*)d_out;

  hipFuncSetAttribute((const void*)k1_gemm, hipFuncAttributeMaxDynamicSharedMemorySize, K1_NS * K1_STAGE);
  hipFuncSetAttribute((const void*)k2_gemm, hipFuncAttributeMaxDynamicSharedMemorySize, K2_LDS);

  k0_prep<<<256 + 108, 256, 0, stream>>>(x, wo, wd, ws);
  k1_gemm<<<240, 512, K1_NS * K1_STAGE, stream>>>(wqkv, wg, wu, ws);
  k2_gemm<<<256, 256, K2_LDS, stream>>>(x, ws, out);
}
```

```cpp
#include <hip/hip_runtime.h>
#include <hip/hip_bf16.h>
#include <stdint.h>

typedef unsigned short u16;
typedef __attribute__((ext_vector_type(8))) short s16x8;
typedef __attribute__((ext_vector_type(4))) short s16x4;
typedef __attribute__((ext_vector_type(4))) float f32x4;
typedef __attribute__((ext_vector_type(16))) float f32x16;
typedef __attribute__((ext_vector_type(2))) unsigned u32x2;
typedef __attribute__((ext_vector_type(4))) unsigned u32x4;
typedef __attribute__((address_space(3))) s16x4* lds_s16x4_ptr;
typedef __attribute__((address_space(1))) unsigned gu32;
typedef __attribute__((address_space(1))) unsigned long long gu64;

#define RLX_AGENT __ATOMIC_RELAXED, __HIP_MEMORY_SCOPE_AGENT

constexpr int S_ = 2048, D_ = 640, F_ = 1728, NQKV_ = 1920;

constexpr size_t OFF_CNT   = 0;
constexpr size_t OFF_XN    = 8192;
constexpr size_t SZ_XN     = (size_t)S_ * D_ * 2;
constexpr size_t OFF_H     = OFF_XN + SZ_XN;
constexpr size_t SZ_H      = (size_t)S_ * F_ * 2;
constexpr size_t OFF_PQ    = OFF_H + SZ_H;
constexpr size_t SZ_PQ     = (size_t)S_ * D_ * 2;
constexpr size_t OFF_WC    = OFF_PQ + SZ_PQ;
constexpr size_t SZ_WC     = (size_t)8 * 54 * 5 * 1024;
constexpr size_t OFF_WO    = OFF_WC + SZ_WC;
constexpr size_t SZ_WO     = (size_t)8 * 32 * 5 * 1024;
constexpr size_t OFF_KVP   = OFF_WO + SZ_WO;
constexpr size_t SZ_KVP    = (size_t)16 * 32 * 504 * 4;
constexpr size_t OFF_KVF   = OFF_KVP + SZ_KVP;
constexpr size_t SZ_KVF    = 65536;

__device__ __forceinline__ float bf2f(u16 v) { return __uint_as_float(((unsigned)v) << 16); }
__device__ __forceinline__ u16 f2bf(float f) { return __builtin_bit_cast(u16, (__bf16)f); }
__device__ __forceinline__ float bfr(float f) { return bf2f(f2bf(f)); }
__device__ __forceinline__ unsigned pack2(float a, float b) { return (unsigned)f2bf(a) | ((unsigned)f2bf(b) << 16); }
__device__ __forceinline__ float phi_f(float t) {
  float p = t > 0.f ? bfr(t + 1.0f) : bfr(__expf(t));
  return bfr(p + 1e-6f);
}
__device__ __forceinline__ int aswz(int row) { int x = (row >> 2) & 3; return (((x ^ (x >> 1)) & 1) << 1) | (x >> 1); }

#define WAIT_V(n) asm volatile("s_waitcnt vmcnt(" #n ")" ::: "memory")
#define STAMP(kid, idx)
#define WAIT_L0 asm volatile("s_waitcnt lgkmcnt(0)" ::: "memory")
#define BAR __builtin_amdgcn_s_barrier()
#define SCHED __builtin_amdgcn_sched_barrier(0)
__device__ __forceinline__ float frcp(float v) { return __builtin_amdgcn_rcpf(v); }

__global__ __launch_bounds__(256) void k0_prep(const u16* __restrict__ x, const u16* __restrict__ wo,
                                               const u16* __restrict__ wdown, char* __restrict__ ws) {
  const int tid = threadIdx.x;
  if (blockIdx.x >= 256) {
    const int t = (blockIdx.x - 256) * 256 + tid;
    if (t < 216 * 80 + 128 * 80) {
    if (t < 216 * 80) {
      const int koct = t / 80, noct = t - koct * 80;
      const u16* src = wdown + (size_t)(koct * 8) * D_ + noct * 8;
      u32x4 r[8];
#pragma unroll
      for (int jj = 0; jj < 8; ++jj) r[jj] = *(const u32x4*)(src + (size_t)jj * D_);
      const int n0 = noct * 8, nt = n0 / 80, nn = n0 - nt * 80, nf = nn >> 4, c0 = nn & 15, kt = koct >> 2, gg = koct & 3;
      char* dst = ws + OFF_WC + ((size_t)((nt * 54 + kt) * 5 + nf) * 64 + gg * 16 + c0) * 16;
#pragma unroll
      for (int i = 0; i < 4; ++i) {
        u32x4 lo, hi;
#pragma unroll
        for (int q = 0; q < 4; ++q) {
          lo[q] = (r[2 * q][i] & 0xffffu) | (r[2 * q + 1][i] << 16);
          hi[q] = (r[2 * q][i] >> 16) | (r[2 * q + 1][i] & 0xffff0000u);
        }
        *(u32x4*)(dst + (2 * i) * 16) = lo;
        *(u32x4*)(dst + (2 * i + 1) * 16) = hi;
      }
    } else {
      const int t2 = t - 216 * 80, hg = t2 / 80, noct = t2 - hg * 80, hd = hg >> 2, gg = hg & 3;
      const u16* src = wo + (size_t)(hd * 20) * D_ + noct * 8;
      u32x4 r[4];
#pragma unroll
      for (int jj = 0; jj < 4; ++jj) r[jj] = *(const u32x4*)(src + (size_t)(4 * gg + jj) * D_);
      const u32x4 e = *(const u32x4*)(src + (size_t)(16 + gg) * D_);
      const int n0 = noct * 8, nt = n0 / 80, nn = n0 - nt * 80, nf = nn >> 4, c0 = nn & 15;
      char* dst = ws + OFF_WO + ((size_t)((nt * 32 + hd) * 5 + nf) * 64 + c0) * 16;
#pragma unroll
      for (int i = 0; i < 4; ++i) {
        u32x4 lo, hi;
        lo[0] = (r[0][i] & 0xffffu) | (r[1][i] << 16);
        lo[1] = (r[2][i] & 0xffffu) | (r[3][i] << 16);
        hi[0] = (r[0][i] >> 16) | (r[1][i] & 0xffff0000u);
        hi[1] = (r[2][i] >> 16) | (r[3][i] & 0xffff0000u);
        lo[2] = e[i] & 0xffffu; lo[3] = 0u;
        hi[2] = e[i] >> 16; hi[3] = 0u;
        *(u32x4*)(dst + (gg * 16 + 2 * i) * 16) = lo;
        *(u32x4*)(dst + (gg * 16 + 2 * i + 1) * 16) = hi;
      }
    }
    }
  } else {
    if (blockIdx.x == 0 && tid < 4) ((unsigned*)(ws + OFF_CNT))[tid * 64] = 0u;
    const int row = blockIdx.x * 8 + (tid >> 5);
    const int l32 = tid & 31;
    const u16* xr = x + (size_t)row * D_;
    float v[20];
    float sum = 0.f;
#pragma unroll
    for (int i = 0; i < 5; ++i) {
      u32x2 w = *(const u32x2*)(xr + (l32 + 32 * i) * 4);
      v[4 * i + 0] = __uint_as_float(w[0] << 16);
      v[4 * i + 1] = __uint_as_float(w[0] & 0xffff0000u);
      v[4 * i + 2] = __uint_as_float(w[1] << 16);
      v[4 * i + 3] = __uint_as_float(w[1] & 0xffff0000u);
      sum += v[4 * i] + v[4 * i + 1] + v[4 * i + 2] + v[4 * i + 3];
    }
#pragma unroll
    for (int o = 16; o >= 1; o >>= 1) sum += __shfl_xor(sum, o);
    const float mean = sum * (1.0f / D_);
    float sq = 0.f;
#pragma unroll
    for (int i = 0; i < 20; ++i) { float d = v[i] - mean; sq += d * d; }
#pragma unroll
    for (int o = 16; o >= 1; o >>= 1) sq += __shfl_xor(sq, o);
    const float mb = bfr(mean);
    const float vb = bfr(sq * (1.0f / D_));
    const float sd = bfr(sqrtf(bfr(vb + 1e-5f)));
    u16* xn = (u16*)(ws + OFF_XN);
    const int mt = row >> 7, r = row & 127, sw = aswz(r);
#pragma unroll
    for (int i = 0; i < 5; ++i) {
      const int k = (l32 + 32 * i) * 4;
      float o0 = bfr(v[4 * i + 0] - mb) / sd, o1 = bfr(v[4 * i + 1] - mb) / sd;
      float o2 = bfr(v[4 * i + 2] - mb) / sd, o3 = bfr(v[4 * i + 3] - mb) / sd;
      u32x2 w; w[0] = pack2(o0, o1); w[1] = pack2(o2, o3);
      const int kt = k >> 5, ch = (k >> 3) & 3, hf = (k >> 2) & 1;
      size_t off = ((size_t)(mt * 20 + kt) * 128 + r) * 32 + ((ch ^ sw) << 3) + (hf << 2);
      *(u32x2*)(xn + off) = w;
    }
  }
}

constexpr int K1_STAGE = 32768;
constexpr int K1_NS = 4;

template <int TYPE>
__device__ __forceinline__ void k1_body(const u16* __restrict__ wA, const u16* __restrict__ wB, char* __restrict__ ws,
                                        int mt, int j, char* smem) {
  constexpr int NCB = TYPE == 0 ? 6 : 5;
  constexpr int NFR = TYPE == 0 ? 6 : 5;
  constexpr int LDW = TYPE == 0 ? F_ : NQKV_;
  const int tid = threadIdx.x, lane = tid & 63;
  const int wave = __builtin_amdgcn_readfirstlane(tid >> 6);
  const int wm = wave >> 2, wn = wave & 3;
  const int g = lane >> 4, c = lane & 15;

  const char* asrc = ws + OFF_XN + (size_t)mt * 20 * 8192 + wave * 1024 + lane * 16;
  const u16* bsrc[3];
#pragma unroll
  for (int t = 0; t < 3; ++t) {
    int ii = wave + 8 * t; if (ii >= NCB * 4) ii = wave;
    const int cb = ii >> 2, krow = (ii & 3) * 8 + (lane >> 3), u = lane & 7;
    const int sk = ((krow >> 1) & 1) | (((krow >> 3) & 1) << 1);
    const int ch = (u >> 1) ^ sk;
    const int lc = cb * 64 + ch * 16 + (u & 1) * 8;
    const u16* p;
    if (TYPE == 0) p = (lc < 192) ? (wA + j * 192 + lc) : (wB + j * 192 + (lc - 192));
    else if (TYPE == 1) p = wA + j * 320 + lc;
    else p = (lc < 160) ? (wA + 640 + j * 160 + lc) : (wA + 1280 + j * 160 + (lc - 160));
    bsrc[t] = p + (size_t)krow * LDW;
  }
  const bool has3 = (NCB == 6) || (wave < 4);

  auto issue = [&](int kt) {
    char* sb = smem + (kt & (K1_NS - 1)) * K1_STAGE;
    __builtin_amdgcn_global_load_lds((const unsigned*)(asrc + (size_t)kt * 8192), (unsigned*)(sb + wave * 1024), 16, 0, 0);
    __builtin_amdgcn_global_load_lds((const unsigned*)(bsrc[0] + (size_t)kt * 32 * LDW), (unsigned*)(sb + 8192 + wave * 1024), 16, 0, 0);
    __builtin_amdgcn_global_load_lds((const unsigned*)(bsrc[1] + (size_t)kt * 32 * LDW), (unsigned*)(sb + 8192 + (wave + 8) * 1024), 16, 0, 0);
    if (has3) __builtin_amdgcn_global_load_lds((const unsigned*)(bsrc[2] + (size_t)kt * 32 * LDW), (unsigned*)(sb + 8192 + (wave + 16) * 1024), 16, 0, 0);
  };

  const int xoff = (wm * 64 + c) * 64 + ((g ^ aswz(c)) << 4);
  int woff[NFR];
  {
    const int q = (lane & 15) >> 2, p = lane & 3;
    const int s = (q >> 1) | ((g & 1) << 1);
    const int lo = (8 * g + q) * 128 + (s << 5) + p * 8;
#pragma unroll
    for (int i = 0; i < NFR; ++i) {
      const int nf = TYPE == 0 ? (i < 3 ? 3 * wn + i : 12 + 3 * wn + (i - 3)) : 5 * wn + i;
      woff[i] = 8192 + (nf >> 2) * 4096 + (lo ^ ((nf & 3) << 5));
    }
  }

  f32x4 acc[4][NFR];
#pragma unroll
  for (int a = 0; a < 4; ++a)
#pragma unroll
    for (int b = 0; b < NFR; ++b) acc[a][b] = (f32x4){0.f, 0.f, 0.f, 0.f};

  auto readfr = [&](int kt, s16x8 (&xf)[4], s16x8 (&wf)[NFR]) {
    const char* sb = smem + (kt & (K1_NS - 1)) * K1_STAGE;
#pragma unroll
    for (int b = 0; b < NFR; ++b) {
      s16x4 lo = __builtin_amdgcn_ds_read_tr16_b64_v4i16((lds_s16x4_ptr)(sb + woff[b]));
      s16x4 hi = __builtin_amdgcn_ds_read_tr16_b64_v4i16((lds_s16x4_ptr)(sb + woff[b] + 512));
      wf[b] = __builtin_shufflevector(lo, hi, 0, 1, 2, 3, 4, 5, 6, 7);
    }
#pragma unroll
    for (int a = 0; a < 4; ++a) xf[a] = *(const s16x8*)(sb + xoff + a * 1024);
  };
  auto mma = [&](const s16x8 (&xf)[4], const s16x8 (&wf)[NFR]) {
#pragma unroll
    for (int a = 0; a < 4; ++a)
#pragma unroll
      for (int b = 0; b < NFR; ++b) {
        if (TYPE == 2) acc[a][b] = __builtin_amdgcn_mfma_f32_16x16x32_bf16(xf[a], wf[b], acc[a][b], 0, 0, 0);
        else           acc[a][b] = __builtin_amdgcn_mfma_f32_16x16x32_bf16(wf[b], xf[a], acc[a][b], 0, 0, 0);
      }
  };
#define K1_WAIT(n) do { if (has3) WAIT_V2(4, n); else WAIT_V2(3, n); } while (0)
#define WAIT_V2(per, n) do { if ((per) * (n) == 12) WAIT_V(12); else if ((per) * (n) == 9) WAIT_V(9); else if ((per) * (n) == 8) WAIT_V(8); \
                             else if ((per) * (n) == 6) WAIT_V(6); else if ((per) * (n) == 4) WAIT_V(4); else if ((per) * (n) == 3) WAIT_V(3); else WAIT_V(0); } while (0)

  s16x8 xA[4], wAf[NFR], xB[4], wBf[NFR];
#define SGB(m, n) __builtin_amdgcn_sched_group_barrier(m, n, 0)
  auto pattern_full = [&]() {
    SGB(0x10, 1); SGB(0x8, 1); SGB(0x10, 1); SGB(0x8, 1); SGB(0x10, 1); SGB(0x8, 1); SGB(0x10, 1); SGB(0x8, 1);
#pragma unroll
    for (int i = 0; i < 4 + 2 * NFR; ++i) { SGB(0x100, 1); SGB(0x8, 1); }
    SGB(0x8, 4);
  };
  auto pattern_tail = [&]() {
#pragma unroll
    for (int i = 0; i < 4 + 2 * NFR; ++i) { SGB(0x100, 1); SGB(0x8, 1); }
    SGB(0x8, 8);
  };
  STAMP(1, 0);
  issue(0); issue(1); issue(2); issue(3);
  K1_WAIT(3);
  BAR;
  STAMP(1, 1);
  readfr(0, xA, wAf);
  SCHED;
  for (int kt = 0; kt < 16; kt += 2) {
    K1_WAIT(2); WAIT_L0; BAR; SCHED;
    issue(kt + 4);
    readfr(kt + 1, xB, wBf);
    mma(xA, wAf);
    pattern_full();
    SCHED;
    K1_WAIT(2); WAIT_L0; BAR; SCHED;
    issue(kt + 5);
    readfr(kt + 2, xA, wAf);
    mma(xB, wBf);
    pattern_full();
    SCHED;
  }
  K1_WAIT(2); BAR; SCHED; readfr(17, xB, wBf); mma(xA, wAf); pattern_tail(); SCHED;
  K1_WAIT(1); BAR; SCHED; readfr(18, xA, wAf); mma(xB, wBf); pattern_tail(); SCHED;
  K1_WAIT(0); BAR; SCHED; readfr(19, xB, wBf); mma(xA, wAf); pattern_tail(); SCHED;
  mma(xB, wBf);

  WAIT_L0; BAR;
  STAMP(1, 2);
  if (TYPE == 0) {
#pragma unroll
    for (int a = 0; a < 4; ++a) {
#pragma unroll
      for (int b = 0; b < 3; ++b) {
        float hv[4];
#pragma unroll
        for (int r = 0; r < 4; ++r) {
          const float gv = bfr(acc[a][b][r]), uv = bfr(acc[a][b + 3][r]);
          const float sg = bfr(frcp(1.0f + __expf(-gv)));
          hv[r] = sg * uv;
        }
        u32x2 w; w[0] = pack2(hv[0], hv[1]); w[1] = pack2(hv[2], hv[3]);
        const int f0 = wn * 48 + b * 16;
        const int ktl = f0 >> 5, gk = ((f0 >> 4) & 1) * 2 + (g >> 1);
        *(u32x2*)(smem + (((wm * 4 + a) * 6 + ktl) * 64 + gk * 16 + c) * 16 + (g & 1) * 8) = w;
      }
    }
    __syncthreads();
    char* H = ws + OFF_H + ((size_t)(mt * 8) * 54 + j * 6) * 1024;
#pragma unroll
    for (int i = 0; i < 6; ++i) {
      const int q = tid + 512 * i;
      const int m16 = q / 384, rest = q - m16 * 384;
      const u32x4 v = *(const u32x4*)(smem + q * 16);
      *(u32x4*)(H + (size_t)m16 * 54 * 1024 + rest * 16) = v;
    }
  } else if (TYPE == 1) {
#pragma unroll
    for (int a = 0; a < 4; ++a) {
#pragma unroll
      for (int b = 0; b < 5; ++b) {
        float p0 = phi_f(bfr(acc[a][b][0])), p1 = phi_f(bfr(acc[a][b][1]));
        float p2 = phi_f(bfr(acc[a][b][2])), p3 = phi_f(bfr(acc[a][b][3]));
        u32x2 w; w[0] = pack2(p0, p1); w[1] = pack2(p2, p3);
        const int n = wn * 80 + b * 16 + 4 * g, hl = n / 20, d0 = n - hl * 20;
        const int ro = d0 < 16 ? (d0 >> 3) * 256 + c * 16 + (d0 & 7) * 2 : 512 + c * 8;
        *(u32x2*)(smem + ((wm * 4 + a) * 16 + hl) * 640 + ro) = w;
      }
    }
    __syncthreads();
    char* PQ = ws + OFF_PQ + ((size_t)(mt * 8) * 32 + j * 16) * 640;
#pragma unroll
    for (int i = 0; i < 10; ++i) {
      const int q = tid + 512 * i;
      const int m16 = q / 640, rest = q - m16 * 640;
      const u32x4 v = *(const u32x4*)(smem + q * 16);
      *(u32x4*)(PQ + (size_t)m16 * 20480 + rest * 16) = v;
    }
  } else {
#pragma unroll
    for (int a = 0; a < 4; ++a)
#pragma unroll
      for (int b = 0; b < 5; ++b) {
        const int col = wn * 80 + b * 16 + c;
        float v0 = bfr(acc[a][b][0]), v1 = bfr(acc[a][b][1]), v2 = bfr(acc[a][b][2]), v3 = bfr(acc[a][b][3]);
        if (wn < 2) { v0 = phi_f(v0); v1 = phi_f(v1); v2 = phi_f(v2); v3 = phi_f(v3); }
        u32x2 w; w[0] = pack2(v0, v1); w[1] = pack2(v2, v3);
        *(u32x2*)(smem + col * 272 + (wm * 64 + a * 16 + 4 * g) * 2) = w;
      }
    __syncthreads();
    const int r32 = lane & 31, hh = lane >> 5;
    f32x16 akv, az;
#pragma unroll
    for (int i = 0; i < 16; ++i) { akv[i] = 0.f; az[i] = 0.f; }
    s16x8 ones;
#pragma unroll
    for (int i = 0; i < 8; ++i) ones[i] = (short)0x3F80;
#pragma unroll
    for (int s = 0; s < 8; ++s) {
      s16x8 af = *(const s16x8*)(smem + (20 * wave + r32) * 272 + (16 * s + 8 * hh) * 2);
      s16x8 bfv = *(const s16x8*)(smem + (160 + 20 * wave + r32) * 272 + (16 * s + 8 * hh) * 2);
      akv = __builtin_amdgcn_mfma_f32_32x32x16_bf16(af, bfv, akv, 0, 0, 0);
      az = __builtin_amdgcn_mfma_f32_32x32x16_bf16(af, ones, az, 0, 0, 0);
    }
    const int head = j * 8 + wave;
    float* P = (float*)(ws + OFF_KVP) + (size_t)(mt * 32 + head) * 504;
    if (r32 <= 20) {
#pragma unroll
      for (int q = 0; q < 3; ++q) {
        const int d0 = 8 * q + 4 * hh;
        if (d0 < 20) {
          f32x4 val;
#pragma unroll
          for (int r = 0; r < 4; ++r) val[r] = (r32 == 20) ? az[4 * q + r] : akv[4 * q + r];
          asm volatile("global_store_dwordx4 %0, %1, off sc1" :: "v"(P + r32 * 24 + d0), "v"(val) : "memory");
        }
      }
    }
    asm volatile("s_waitcnt vmcnt(0)" ::: "memory");
    __syncthreads();
    volatile int* flag = (volatile int*)(smem + 98304);
    if (tid == 0) {
      unsigned tk = __hip_atomic_fetch_add((gu32*)(ws + OFF_CNT) + j * 64, 1u, RLX_AGENT);
      if (tk == 15u) {
        __builtin_amdgcn_fence(__ATOMIC_ACQUIRE, "agent");
        asm volatile("s_waitcnt vmcnt(0)" ::: "memory");
      }
      *flag = (int)tk;
    }
    __syncthreads();
    if (*flag == 15) {
      const float* Pf = (const float*)(ws + OFF_KVP) + (size_t)head * 504;
      const int l0 = lane * 4, l1 = (lane < 62 ? lane + 64 : lane) * 4;
      f32x4 a0[16], a1[16];
#pragma unroll
      for (int m = 0; m < 16; ++m) {
        a0[m] = *(const f32x4*)(Pf + (size_t)m * 32 * 504 + l0);
        a1[m] = *(const f32x4*)(Pf + (size_t)m * 32 * 504 + l1);
      }
      f32x4 s0 = a0[0], s1 = a1[0];
#pragma unroll
      for (int m = 1; m < 16; ++m) { s0 += a0[m]; s1 += a1[m]; }
      float* kvs = (float*)(smem + 100352 + wave * 2048);
      *(f32x4*)(kvs + l0) = s0;
      if (lane < 62) *(f32x4*)(kvs + l1) = s1;
      WAIT_L0;
#pragma unroll
      for (int fr = 0; fr < 2; ++fr) {
        const int e = fr == 0 ? c : ((c & 3) == 0 ? 16 + (c >> 2) : ((c & 3) == 1 ? 20 : 99)), d0 = 8 * g;
        const bool act = (e <= 20 && d0 < 20), act1 = act && (d0 < 16);
        const float* src = kvs + (act ? e * 24 + d0 : 0);
        f32x4 v0 = *(const f32x4*)src, v1 = *(const f32x4*)(src + (act1 ? 4 : 0));
        u32x4 w;
        w[0] = act ? pack2(v0[0], v0[1]) : 0u; w[1] = act ? pack2(v0[2], v0[3]) : 0u;
        w[2] = act1 ? pack2(v1[0], v1[1]) : 0u; w[3] = act1 ? pack2(v1[2], v1[3]) : 0u;
        *(u32x4*)(ws + OFF_KVF + ((size_t)(head * 2 + fr) * 64 + lane) * 16) = w;
      }
    }
  }
}

__global__ __launch_bounds__(512, 2) void k1_gemm(const u16* __restrict__ wqkv, const u16* __restrict__ wg,
                                                   const u16* __restrict__ wu, char* __restrict__ ws) {
  extern __shared__ __attribute__((aligned(16))) char smem[];
  const int bid = blockIdx.x;
  const int p = (bid & 7) * 30 + (bid >> 3);
  const int mh = p / 120, rem = p % 120, nt = rem >> 3, mt = mh * 8 + (rem & 7);
  if (nt < 4) k1_body<2>(wqkv, wqkv, ws, mt, nt, smem);
  else if (nt < 6) k1_body<1>(wqkv, wqkv, ws, mt, nt - 4, smem);
  else k1_body<0>(wg, wu, ws, mt, nt - 6, smem);
  STAMP(1, 3);
}

constexpr int K2_LDS = 163840;
#define K2DD 3
constexpr int K2_D = K2DD;

__global__ __launch_bounds__(512, 2) void k2_gemm(const u16* __restrict__ x, const char* __restrict__ ws, u16* __restrict__ out) {
  extern __shared__ __attribute__((aligned(16))) char smem[];
  const int tid = threadIdx.x, lane = tid & 63;
  const int wave = __builtin_amdgcn_readfirstlane(tid >> 6);
  const int wq = wave & 3;
  const int g = lane >> 4, c = lane & 15;
  const int bid = blockIdx.x;
  const int mt = (bid & 7) * 4 + (bid >> 6), nt = (bid >> 3) & 7;

  STAMP(2, 0);
  f32x4 acc[4][5];
#pragma unroll
  for (int a = 0; a < 4; ++a)
#pragma unroll
    for (int b = 0; b < 5; ++b) acc[a][b] = (f32x4){0.f, 0.f, 0.f, 0.f};
  const int b0 = wave < 4 ? 0 : 3, nb = wave < 4 ? 3 : 2;
  const int tok = mt * 64 + wq * 16 + c;
  u32x2 xw[3];
#pragma unroll
  for (int i = 0; i < 3; ++i) xw[i] = *(const u32x2*)(x + (size_t)tok * D_ + nt * 80 + (i < nb ? b0 + i : b0) * 16 + 4 * g);

  if (wave < 4) {
    const char* hsrc = ws + OFF_H + ((size_t)(mt * 4) * 54 + wq) * 1024 + lane * 16;
    const char* wsrc = ws + OFF_WC + ((size_t)nt * 54 + wq) * 5120 + lane * 16;
    s16x8 xr[K2_D][4], wr[K2_D][5];
    auto ld = [&](int slot, int i) {
#pragma unroll
      for (int b = 0; b < 5; ++b) wr[slot][b] = *(const s16x8*)(wsrc + (size_t)(20 * i + b) * 1024);
#pragma unroll
      for (int a = 0; a < 4; ++a) xr[slot][a] = *(const s16x8*)(hsrc + (size_t)(a * 54 + 4 * i) * 1024);
    };
    auto mma1 = [&](int slot) {
#pragma unroll
      for (int a = 0; a < 4; ++a)
#pragma unroll
        for (int b = 0; b < 5; ++b) acc[a][b] = __builtin_amdgcn_mfma_f32_16x16x32_bf16(wr[slot][b], xr[slot][a], acc[a][b], 0, 0, 0);
    };
#pragma unroll
    for (int i = 0; i < K2_D; ++i) ld(i, i);
    SCHED;
#pragma unroll
    for (int i = 0; i < 13; ++i) {
      mma1(i % K2_D);
      if (i + K2_D < 13) ld(i % K2_D, i + K2_D);
      else if (i + K2_D == 13) { if (wave < 2) ld(i % K2_D, 13); }
      SCHED;
    }
    if (wave < 2) mma1(13 % K2_D);
    STAMP(2, 1);
  } else {
    const char* pqsrc = ws + OFF_PQ + ((size_t)(mt * 4) * 32 + wq * 8) * 640 + (g < 2 ? g * 256 + c * 16 : 512 + c * 8);
    const char* ksrc = ws + OFF_KVF + (size_t)(wq * 8) * 2048 + lane * 16;
    const char* wosrc = ws + OFF_WO + ((size_t)nt * 32 + wq * 8) * 5120 + lane * 16;
    u32x4 pq[2][4];
    s16x8 kvf[2][2], w2[2][5];
    auto ldA = [&](int sl, int hh) {
#pragma unroll
      for (int fr = 0; fr < 2; ++fr) kvf[sl][fr] = *(const s16x8*)(ksrc + hh * 2048 + fr * 1024);
#pragma unroll
      for (int tg = 0; tg < 4; ++tg) pq[sl][tg] = *(const u32x4*)(pqsrc + (tg * 32 + hh) * 640);
    };
    auto ldW = [&](int sl, int hh) {
#pragma unroll
      for (int b = 0; b < 5; ++b) w2[sl][b] = *(const s16x8*)(wosrc + (hh * 5 + b) * 1024);
    };
    ldA(0, 0); ldW(0, 0); ldA(1, 1); ldW(1, 1);
    const f32x4 z4 = (f32x4){0.f, 0.f, 0.f, 0.f};
#pragma unroll
    for (int hh = 0; hh < 8; ++hh) {
      const int sl = hh & 1;
      f32x4 d0[4], d1[4];
#pragma unroll
      for (int tg = 0; tg < 4; ++tg) {
        u32x4 pk4;
        pk4[0] = g < 3 ? pq[sl][tg][0] : 0u; pk4[1] = g < 3 ? pq[sl][tg][1] : 0u;
        pk4[2] = g < 2 ? pq[sl][tg][2] : 0u; pk4[3] = g < 2 ? pq[sl][tg][3] : 0u;
        const s16x8 pf = __builtin_bit_cast(s16x8, pk4);
        d1[tg] = __builtin_amdgcn_mfma_f32_16x16x32_bf16(kvf[sl][1], pf, z4, 0, 0, 0);
        d0[tg] = __builtin_amdgcn_mfma_f32_16x16x32_bf16(kvf[sl][0], pf, z4, 0, 0, 0);
      }
      if (hh + 2 < 8) ldA(sl, hh + 2);
      s16x8 of[4];
#pragma unroll
      for (int tg = 0; tg < 4; ++tg) {
        const float rden = frcp(bfr(bfr(d1[tg][1]) + 1e-6f));
        u32x4 ow;
        ow[0] = pack2(bfr(d0[tg][0]) * rden, bfr(d0[tg][1]) * rden); ow[1] = pack2(bfr(d0[tg][2]) * rden, bfr(d0[tg][3]) * rden);
        ow[2] = (unsigned)f2bf(bfr(d1[tg][0]) * rden); ow[3] = 0u;
        of[tg] = __builtin_bit_cast(s16x8, ow);
      }
#pragma unroll
      for (int a = 0; a < 4; ++a)
#pragma unroll
        for (int b = 0; b < 5; ++b) acc[a][b] = __builtin_amdgcn_mfma_f32_16x16x32_bf16(w2[sl][b], of[a], acc[a][b], 0, 0, 0);
      if (hh + 2 < 8) ldW(sl, hh + 2);
      SCHED;
    }
  }

  f32x4* red = (f32x4*)smem;
#pragma unroll
  for (int a = 0; a < 4; ++a)
#pragma unroll
    for (int b = 0; b < 5; ++b) red[(wave * 20 + a * 5 + b) * 64 + lane] = acc[a][b];
  __syncthreads();
#pragma unroll
  for (int i = 0; i < 3; ++i) {
    if (i < nb) {
      const int f = wq * 5 + b0 + i;
      f32x4 y1 = red[(0 * 20 + f) * 64 + lane], y2 = red[(4 * 20 + f) * 64 + lane];
#pragma unroll
      for (int sw = 1; sw < 4; ++sw) { y1 += red[(sw * 20 + f) * 64 + lane]; y2 += red[((4 + sw) * 20 + f) * 64 + lane]; }
      const int n = nt * 80 + (b0 + i) * 16 + 4 * g;
      float xv[4];
      xv[0] = __uint_as_float(xw[i][0] << 16); xv[1] = __uint_as_float(xw[i][0] & 0xffff0000u);
      xv[2] = __uint_as_float(xw[i][1] << 16); xv[3] = __uint_as_float(xw[i][1] & 0xffff0000u);
      float o[4];
#pragma unroll
      for (int r = 0; r < 4; ++r) {
        const float y = bfr(y1[r]);
        const float gs = bfr(y * frcp(1.0f + __expf(-y)));
        const float s = bfr(y2[r]);
        o[r] = xv[r] + bfr(s * gs);
      }
      u32x2 w; w[0] = pack2(o[0], o[1]); w[1] = pack2(o[2], o[3]);
      *(u32x2*)(out + (size_t)tok * D_ + n) = w;
    }
  }
  STAMP(2, 3);
}

extern "C" void kernel_launch(void* const* d_in, const int* in_sizes, int n_in,
                              void* d_out, int out_size, void* d_ws, size_t ws_size,
                              hipStream_t stream) {
  const u16* x = (const u16*)d_in[0];
  const u16* wqkv = (const u16*)d_in[1];
  const u16* wo = (const u16*)d_in[2];
  const u16* wg = (const u16*)d_in[4];
  const u16* wu = (const u16*)d_in[5];
  const u16* wd = (const u16*)d_in[6];
  char* ws = (char*)d_ws;
  u16* out = (u16*)d_out;

  hipFuncSetAttribute((const void*)k1_gemm, hipFuncAttributeMaxDynamicSharedMemorySize, K1_NS * K1_STAGE);
  hipFuncSetAttribute((const void*)k2_gemm, hipFuncAttributeMaxDynamicSharedMemorySize, K2_LDS);

  k0_prep<<<256 + 108, 256, 0, stream>>>(x, wo, wd, ws);
  k1_gemm<<<240, 512, K1_NS * K1_STAGE, stream>>>(wqkv, wg, wu, ws);
  k2_gemm<<<256, 512, K2_LDS, stream>>>(x, ws, out);
}
```

```cpp
#include <hip/hip_runtime.h>
#include <hip/hip_bf16.h>
#include <stdint.h>

typedef unsigned short u16;
typedef __attribute__((ext_vector_type(8))) short s16x8;
typedef __attribute__((ext_vector_type(4))) short s16x4;
typedef __attribute__((ext_vector_type(4))) float f32x4;
typedef __attribute__((ext_vector_type(16))) float f32x16;
typedef __attribute__((ext_vector_type(2))) unsigned u32x2;
typedef __attribute__((ext_vector_type(4))) unsigned u32x4;
typedef __attribute__((address_space(3))) s16x4* lds_s16x4_ptr;
typedef __attribute__((address_space(1))) unsigned gu32;
typedef __attribute__((address_space(1))) unsigned long long gu64;

#define RLX_AGENT __ATOMIC_RELAXED, __HIP_MEMORY_SCOPE_AGENT

constexpr int S_ = 2048, D_ = 640, F_ = 1728, NQKV_ = 1920;

constexpr size_t OFF_CNT   = 0;
constexpr size_t OFF_XN    = 8192;
constexpr size_t SZ_XN     = (size_t)S_ * D_ * 2;
constexpr size_t OFF_H     = OFF_XN + SZ_XN;
constexpr size_t SZ_H      = (size_t)S_ * F_ * 2;
constexpr size_t OFF_PQ    = OFF_H + SZ_H;
constexpr size_t SZ_PQ     = (size_t)S_ * D_ * 2;
constexpr size_t OFF_WC    = OFF_PQ + SZ_PQ;
constexpr size_t SZ_WC     = (size_t)8 * 54 * 5 * 1024;
constexpr size_t OFF_WO    = OFF_WC + SZ_WC;
constexpr size_t SZ_WO     = (size_t)8 * 32 * 5 * 1024;
constexpr size_t OFF_KVP   = OFF_WO + SZ_WO;
constexpr size_t SZ_KVP    = (size_t)16 * 32 * 504 * 4;
constexpr size_t OFF_KVF   = OFF_KVP + SZ_KVP;
constexpr size_t SZ_KVF    = 65536;

__device__ __forceinline__ float bf2f(u16 v) { return __uint_as_float(((unsigned)v) << 16); }
__device__ __forceinline__ u16 f2bf(float f) { return __builtin_bit_cast(u16, (__bf16)f); }
__device__ __forceinline__ float bfr(float f) { return bf2f(f2bf(f)); }
__device__ __forceinline__ unsigned pack2(float a, float b) { return (unsigned)f2bf(a) | ((unsigned)f2bf(b) << 16); }
__device__ __forceinline__ float phi_f(float t) {
  float p = t > 0.f ? bfr(t + 1.0f) : bfr(__expf(t));
  return bfr(p + 1e-6f);
}
__device__ __forceinline__ int aswz(int row) { int x = (row >> 2) & 3; return (((x ^ (x >> 1)) & 1) << 1) | (x >> 1); }

#define WAIT_V(n) asm volatile("s_waitcnt vmcnt(" #n ")" ::: "memory")
#define STAMP(kid, idx)
#define WAIT_L0 asm volatile("s_waitcnt lgkmcnt(0)" ::: "memory")
#define BAR __builtin_amdgcn_s_barrier()
#define SCHED __builtin_amdgcn_sched_barrier(0)
__device__ __forceinline__ float frcp(float v) { return __builtin_amdgcn_rcpf(v); }

constexpr int RT_ITEMS = 216 * 80 + 128 * 80;
__device__ __forceinline__ void retile_item(int t, const u16* __restrict__ wo, const u16* __restrict__ wdown, char* __restrict__ ws) {
    if (t < 216 * 80) {
      const int koct = t / 80, noct = t - koct * 80;
      const u16* src = wdown + (size_t)(koct * 8) * D_ + noct * 8;
      u32x4 r[8];
#pragma unroll
      for (int jj = 0; jj < 8; ++jj) r[jj] = *(const u32x4*)(src + (size_t)jj * D_);
      const int n0 = noct * 8, nt = n0 / 80, nn = n0 - nt * 80, nf = nn >> 4, c0 = nn & 15, kt = koct >> 2, gg = koct & 3;
      char* dst = ws + OFF_WC + ((size_t)((nt * 54 + kt) * 5 + nf) * 64 + gg * 16 + c0) * 16;
#pragma unroll
      for (int i = 0; i < 4; ++i) {
        u32x4 lo, hi;
#pragma unroll
        for (int q = 0; q < 4; ++q) {
          lo[q] = (r[2 * q][i] & 0xffffu) | (r[2 * q + 1][i] << 16);
          hi[q] = (r[2 * q][i] >> 16) | (r[2 * q + 1][i] & 0xffff0000u);
        }
        *(u32x4*)(dst + (2 * i) * 16) = lo;
        *(u32x4*)(dst + (2 * i + 1) * 16) = hi;
      }
    } else {
      const int t2 = t - 216 * 80, hg = t2 / 80, noct = t2 - hg * 80, hd = hg >> 2, gg = hg & 3;
      const u16* src = wo + (size_t)(hd * 20) * D_ + noct * 8;
      u32x4 r[4];
#pragma unroll
      for (int jj = 0; jj < 4; ++jj) r[jj] = *(const u32x4*)(src + (size_t)(4 * gg + jj) * D_);
      const u32x4 e = *(const u32x4*)(src + (size_t)(16 + gg) * D_);
      const int n0 = noct * 8, nt = n0 / 80, nn = n0 - nt * 80, nf = nn >> 4, c0 = nn & 15;
      char* dst = ws + OFF_WO + ((size_t)((nt * 32 + hd) * 5 + nf) * 64 + c0) * 16;
#pragma unroll
      for (int i = 0; i < 4; ++i) {
        u32x4 lo, hi;
        lo[0] = (r[0][i] & 0xffffu) | (r[1][i] << 16);
        lo[1] = (r[2][i] & 0xffffu) | (r[3][i] << 16);
        hi[0] = (r[0][i] >> 16) | (r[1][i] & 0xffff0000u);
        hi[1] = (r[2][i] >> 16) | (r[3][i] & 0xffff0000u);
        lo[2] = e[i] & 0xffffu; lo[3] = 0u;
        hi[2] = e[i] >> 16; hi[3] = 0u;
        *(u32x4*)(dst + (gg * 16 + 2 * i) * 16) = lo;
        *(u32x4*)(dst + (gg * 16 + 2 * i + 1) * 16) = hi;
      }
    }
}

__global__ __launch_bounds__(256) void k0_prep(const u16* __restrict__ x, char* __restrict__ ws) {
  const int tid = threadIdx.x;
  {
    if (blockIdx.x == 0 && tid < 4) ((unsigned*)(ws + OFF_CNT))[tid * 64] = 0u;
    const int row = blockIdx.x * 8 + (tid >> 5);
    const int l32 = tid & 31;
    const u16* xr = x + (size_t)row * D_;
    float v[20];
    float sum = 0.f;
#pragma unroll
    for (int i = 0; i < 5; ++i) {
      u32x2 w = *(const u32x2*)(xr + (l32 + 32 * i) * 4);
      v[4 * i + 0] = __uint_as_float(w[0] << 16);
      v[4 * i + 1] = __uint_as_float(w[0] & 0xffff0000u);
      v[4 * i + 2] = __uint_as_float(w[1] << 16);
      v[4 * i + 3] = __uint_as_float(w[1] & 0xffff0000u);
      sum += v[4 * i] + v[4 * i + 1] + v[4 * i + 2] + v[4 * i + 3];
    }
#pragma unroll
    for (int o = 16; o >= 1; o >>= 1) sum += __shfl_xor(sum, o);
    const float mean = sum * (1.0f / D_);
    float sq = 0.f;
#pragma unroll
    for (int i = 0; i < 20; ++i) { float d = v[i] - mean; sq += d * d; }
#pragma unroll
    for (int o = 16; o >= 1; o >>= 1) sq += __shfl_xor(sq, o);
    const float mb = bfr(mean);
    const float vb = bfr(sq * (1.0f / D_));
    const float sd = bfr(sqrtf(bfr(vb + 1e-5f)));
    u16* xn = (u16*)(ws + OFF_XN);
    const int mt = row >> 7, r = row & 127, sw = aswz(r);
#pragma unroll
    for (int i = 0; i < 5; ++i) {
      const int k = (l32 + 32 * i) * 4;
      float o0 = bfr(v[4 * i + 0] - mb) / sd, o1 = bfr(v[4 * i + 1] - mb) / sd;
      float o2 = bfr(v[4 * i + 2] - mb) / sd, o3 = bfr(v[4 * i + 3] - mb) / sd;
      u32x2 w; w[0] = pack2(o0, o1); w[1] = pack2(o2, o3);
      const int kt = k >> 5, ch = (k >> 3) & 3, hf = (k >> 2) & 1;
      size_t off = ((size_t)(mt * 20 + kt) * 128 + r) * 32 + ((ch ^ sw) << 3) + (hf << 2);
      *(u32x2*)(xn + off) = w;
    }
  }
}

constexpr int K1_STAGE = 32768;
constexpr int K1_NS = 4;

template <int TYPE>
__device__ __forceinline__ void k1_body(const u16* __restrict__ wA, const u16* __restrict__ wB, char* __restrict__ ws,
                                        int mt, int j, char* smem) {
  constexpr int NCB = TYPE == 0 ? 6 : 5;
  constexpr int NFR = TYPE == 0 ? 6 : 5;
  constexpr int LDW = TYPE == 0 ? F_ : NQKV_;
  const int tid = threadIdx.x, lane = tid & 63;
  const int wave = __builtin_amdgcn_readfirstlane(tid >> 6);
  const int wm = wave >> 2, wn = (TYPE == 2 && wave >= 4) ? ((wave + 2) & 3) : (wave & 3);
  const int g = lane >> 4, c = lane & 15;

  const char* asrc = ws + OFF_XN + (size_t)mt * 20 * 8192 + wave * 1024 + lane * 16;
  const u16* bsrc[3];
#pragma unroll
  for (int t = 0; t < 3; ++t) {
    int ii = wave + 8 * t; if (ii >= NCB * 4) ii = wave;
    const int cb = ii >> 2, krow = (ii & 3) * 8 + (lane >> 3), u = lane & 7;
    const int sk = ((krow >> 1) & 1) | (((krow >> 3) & 1) << 1);
    const int ch = (u >> 1) ^ sk;
    const int lc = cb * 64 + ch * 16 + (u & 1) * 8;
    const u16* p;
    if (TYPE == 0) p = (lc < 192) ? (wA + j * 192 + lc) : (wB + j * 192 + (lc - 192));
    else if (TYPE == 1) p = wA + j * 320 + lc;
    else p = (lc < 160) ? (wA + 640 + j * 160 + lc) : (wA + 1280 + j * 160 + (lc - 160));
    bsrc[t] = p + (size_t)krow * LDW;
  }
  const bool has3 = (NCB == 6) || (wave < 4);

  auto issue = [&](int kt) {
    char* sb = smem + (kt & (K1_NS - 1)) * K1_STAGE;
    __builtin_amdgcn_global_load_lds((const unsigned*)(asrc + (size_t)kt * 8192), (unsigned*)(sb + wave * 1024), 16, 0, 0);
    __builtin_amdgcn_global_load_lds((const unsigned*)(bsrc[0] + (size_t)kt * 32 * LDW), (unsigned*)(sb + 8192 + wave * 1024), 16, 0, 0);
    __builtin_amdgcn_global_load_lds((const unsigned*)(bsrc[1] + (size_t)kt * 32 * LDW), (unsigned*)(sb + 8192 + (wave + 8) * 1024), 16, 0, 0);
    if (has3) __builtin_amdgcn_global_load_lds((const unsigned*)(bsrc[2] + (size_t)kt * 32 * LDW), (unsigned*)(sb + 8192 + (wave + 16) * 1024), 16, 0, 0);
  };

  const int xoff = (wm * 64 + c) * 64 + ((g ^ aswz(c)) << 4);
  int woff[NFR];
  {
    const int q = (lane & 15) >> 2, p = lane & 3;
    const int s = (q >> 1) | ((g & 1) << 1);
    const int lo = (8 * g + q) * 128 + (s << 5) + p * 8;
#pragma unroll
    for (int i = 0; i < NFR; ++i) {
      const int nf = TYPE == 0 ? (i < 3 ? 3 * wn + i : 12 + 3 * wn + (i - 3)) : 5 * wn + i;
      woff[i] = 8192 + (nf >> 2) * 4096 + (lo ^ ((nf & 3) << 5));
    }
  }

  f32x4 acc[4][NFR];
#pragma unroll
  for (int a = 0; a < 4; ++a)
#pragma unroll
    for (int b = 0; b < NFR; ++b) acc[a][b] = (f32x4){0.f, 0.f, 0.f, 0.f};

  auto readfr = [&](int kt, s16x8 (&xf)[4], s16x8 (&wf)[NFR]) {
    const char* sb = smem + (kt & (K1_NS - 1)) * K1_STAGE;
#pragma unroll
    for (int b = 0; b < NFR; ++b) {
      s16x4 lo = __builtin_amdgcn_ds_read_tr16_b64_v4i16((lds_s16x4_ptr)(sb + woff[b]));
      s16x4 hi = __builtin_amdgcn_ds_read_tr16_b64_v4i16((lds_s16x4_ptr)(sb + woff[b] + 512));
      wf[b] = __builtin_shufflevector(lo, hi, 0, 1, 2, 3, 4, 5, 6, 7);
    }
#pragma unroll
    for (int a = 0; a < 4; ++a) xf[a] = *(const s16x8*)(sb + xoff + a * 1024);
  };
  auto mma = [&](const s16x8 (&xf)[4], const s16x8 (&wf)[NFR]) {
#pragma unroll
    for (int a = 0; a < 4; ++a)
#pragma unroll
      for (int b = 0; b < NFR; ++b) {
        if (TYPE == 2) acc[a][b] = __builtin_amdgcn_mfma_f32_16x16x32_bf16(xf[a], wf[b], acc[a][b], 0, 0, 0);
        else           acc[a][b] = __builtin_amdgcn_mfma_f32_16x16x32_bf16(wf[b], xf[a], acc[a][b], 0, 0, 0);
      }
  };
#define K1_WAIT(n) do { if (has3) WAIT_V2(4, n); else WAIT_V2(3, n); } while (0)
#define WAIT_V2(per, n) do { if ((per) * (n) == 12) WAIT_V(12); else if ((per) * (n) == 9) WAIT_V(9); else if ((per) * (n) == 8) WAIT_V(8); \
                             else if ((per) * (n) == 6) WAIT_V(6); else if ((per) * (n) == 4) WAIT_V(4); else if ((per) * (n) == 3) WAIT_V(3); else WAIT_V(0); } while (0)

  s16x8 xA[4], wAf[NFR], xB[4], wBf[NFR];
#define SGB(m, n) __builtin_amdgcn_sched_group_barrier(m, n, 0)
  auto pattern_full = [&]() {
    SGB(0x10, 1); SGB(0x8, 1); SGB(0x10, 1); SGB(0x8, 1); SGB(0x10, 1); SGB(0x8, 1); SGB(0x10, 1); SGB(0x8, 1);
#pragma unroll
    for (int i = 0; i < 4 + 2 * NFR; ++i) { SGB(0x100, 1); SGB(0x8, 1); }
    SGB(0x8, 4);
  };
  auto pattern_tail = [&]() {
#pragma unroll
    for (int i = 0; i < 4 + 2 * NFR; ++i) { SGB(0x100, 1); SGB(0x8, 1); }
    SGB(0x8, 8);
  };
  STAMP(1, 0);
  issue(0); issue(1); issue(2); issue(3);
  K1_WAIT(3);
  BAR;
  STAMP(1, 1);
  readfr(0, xA, wAf);
  SCHED;
  for (int kt = 0; kt < 16; kt += 2) {
    K1_WAIT(2); WAIT_L0; BAR; SCHED;
    issue(kt + 4);
    readfr(kt + 1, xB, wBf);
    mma(xA, wAf);
    pattern_full();
    SCHED;
    K1_WAIT(2); WAIT_L0; BAR; SCHED;
    issue(kt + 5);
    readfr(kt + 2, xA, wAf);
    mma(xB, wBf);
    pattern_full();
    SCHED;
  }
  K1_WAIT(2); BAR; SCHED; readfr(17, xB, wBf); mma(xA, wAf); pattern_tail(); SCHED;
  K1_WAIT(1); BAR; SCHED; readfr(18, xA, wAf); mma(xB, wBf); pattern_tail(); SCHED;
  K1_WAIT(0); BAR; SCHED; readfr(19, xB, wBf); mma(xA, wAf); pattern_tail(); SCHED;
  mma(xB, wBf);

  WAIT_L0; BAR;
  STAMP(1, 2);
  if (TYPE == 0) {
#pragma unroll
    for (int a = 0; a < 4; ++a) {
#pragma unroll
      for (int b = 0; b < 3; ++b) {
        float hv[4];
#pragma unroll
        for (int r = 0; r < 4; ++r) {
          const float gv = bfr(acc[a][b][r]), uv = bfr(acc[a][b + 3][r]);
          const float sg = bfr(frcp(1.0f + __expf(-gv)));
          hv[r] = sg * uv;
        }
        u32x2 w; w[0] = pack2(hv[0], hv[1]); w[1] = pack2(hv[2], hv[3]);
        const int f0 = wn * 48 + b * 16;
        const int ktl = f0 >> 5, gk = ((f0 >> 4) & 1) * 2 + (g >> 1);
        *(u32x2*)(smem + (((wm * 4 + a) * 6 + ktl) * 64 + gk * 16 + c) * 16 + (g & 1) * 8) = w;
      }
    }
    __syncthreads();
    char* H = ws + OFF_H + ((size_t)(mt * 8) * 54 + j * 6) * 1024;
#pragma unroll
    for (int i = 0; i < 6; ++i) {
      const int q = tid + 512 * i;
      const int m16 = q / 384, rest = q - m16 * 384;
      const u32x4 v = *(const u32x4*)(smem + q * 16);
      *(u32x4*)(H + (size_t)m16 * 54 * 1024 + rest * 16) = v;
    }
  } else if (TYPE == 1) {
#pragma unroll
    for (int a = 0; a < 4; ++a) {
#pragma unroll
      for (int b = 0; b < 5; ++b) {
        float p0 = phi_f(bfr(acc[a][b][0])), p1 = phi_f(bfr(acc[a][b][1]));
        float p2 = phi_f(bfr(acc[a][b][2])), p3 = phi_f(bfr(acc[a][b][3]));
        u32x2 w; w[0] = pack2(p0, p1); w[1] = pack2(p2, p3);
        const int n = wn * 80 + b * 16 + 4 * g, hl = n / 20, d0 = n - hl * 20;
        const int ro = d0 < 16 ? (d0 >> 3) * 256 + c * 16 + (d0 & 7) * 2 : 512 + c * 8;
        *(u32x2*)(smem + ((wm * 4 + a) * 16 + hl) * 640 + ro) = w;
      }
    }
    __syncthreads();
    char* PQ = ws + OFF_PQ + ((size_t)(mt * 8) * 32 + j * 16) * 640;
#pragma unroll
    for (int i = 0; i < 10; ++i) {
      const int q = tid + 512 * i;
      const int m16 = q / 640, rest = q - m16 * 640;
      const u32x4 v = *(const u32x4*)(smem + q * 16);
      *(u32x4*)(PQ + (size_t)m16 * 20480 + rest * 16) = v;
    }
  } else {
#pragma unroll
    for (int a = 0; a < 4; ++a)
#pragma unroll
      for (int b = 0; b < 5; ++b) {
        const int col = wn * 80 + b * 16 + c;
        float v0 = bfr(acc[a][b][0]), v1 = bfr(acc[a][b][1]), v2 = bfr(acc[a][b][2]), v3 = bfr(acc[a][b][3]);
        if (wn < 2) { v0 = phi_f(v0); v1 = phi_f(v1); v2 = phi_f(v2); v3 = phi_f(v3); }
        u32x2 w; w[0] = pack2(v0, v1); w[1] = pack2(v2, v3);
        *(u32x2*)(smem + col * 272 + (wm * 64 + a * 16 + 4 * g) * 2) = w;
      }
    __syncthreads();
    const int r32 = lane & 31, hh = lane >> 5;
    f32x16 akv, az;
#pragma unroll
    for (int i = 0; i < 16; ++i) { akv[i] = 0.f; az[i] = 0.f; }
    s16x8 ones;
#pragma unroll
    for (int i = 0; i < 8; ++i) ones[i] = (short)0x3F80;
#pragma unroll
    for (int s = 0; s < 8; ++s) {
      s16x8 af = *(const s16x8*)(smem + (20 * wave + r32) * 272 + (16 * s + 8 * hh) * 2);
      s16x8 bfv = *(const s16x8*)(smem + (160 + 20 * wave + r32) * 272 + (16 * s + 8 * hh) * 2);
      akv = __builtin_amdgcn_mfma_f32_32x32x16_bf16(af, bfv, akv, 0, 0, 0);
      az = __builtin_amdgcn_mfma_f32_32x32x16_bf16(af, ones, az, 0, 0, 0);
    }
    const int head = j * 8 + wave;
    float* P = (float*)(ws + OFF_KVP) + (size_t)(mt * 32 + head) * 504;
    if (r32 <= 20) {
#pragma unroll
      for (int q = 0; q < 3; ++q) {
        const int d0 = 8 * q + 4 * hh;
        if (d0 < 20) {
          f32x4 val;
#pragma unroll
          for (int r = 0; r < 4; ++r) val[r] = (r32 == 20) ? az[4 * q + r] : akv[4 * q + r];
          asm volatile("global_store_dwordx4 %0, %1, off sc1" :: "v"(P + r32 * 24 + d0), "v"(val) : "memory");
        }
      }
    }
    asm volatile("s_waitcnt vmcnt(0)" ::: "memory");
    __syncthreads();
    volatile int* flag = (volatile int*)(smem + 98304);
    if (tid == 0) {
      unsigned tk = __hip_atomic_fetch_add((gu32*)(ws + OFF_CNT) + j * 64, 1u, RLX_AGENT);
      if (tk == 15u) {
        __builtin_amdgcn_fence(__ATOMIC_ACQUIRE, "agent");
        asm volatile("s_waitcnt vmcnt(0)" ::: "memory");
      }
      *flag = (int)tk;
    }
    __syncthreads();
    if (*flag == 15) {
      const float* Pf = (const float*)(ws + OFF_KVP) + (size_t)head * 504;
      const int l0 = lane * 4, l1 = (lane < 62 ? lane + 64 : lane) * 4;
      f32x4 a0[16], a1[16];
#pragma unroll
      for (int m = 0; m < 16; ++m) {
        a0[m] = *(const f32x4*)(Pf + (size_t)m * 32 * 504 + l0);
        a1[m] = *(const f32x4*)(Pf + (size_t)m * 32 * 504 + l1);
      }
      f32x4 s0 = a0[0], s1 = a1[0];
#pragma unroll
      for (int m = 1; m < 16; ++m) { s0 += a0[m]; s1 += a1[m]; }
      float* kvs = (float*)(smem + 100352 + wave * 2048);
      *(f32x4*)(kvs + l0) = s0;
      if (lane < 62) *(f32x4*)(kvs + l1) = s1;
      WAIT_L0;
#pragma unroll
      for (int fr = 0; fr < 2; ++fr) {
        const int e = fr == 0 ? c : ((c & 3) == 0 ? 16 + (c >> 2) : ((c & 3) == 1 ? 20 : 99)), d0 = 8 * g;
        const bool act = (e <= 20 && d0 < 20), act1 = act && (d0 < 16);
        const float* src = kvs + (act ? e * 24 + d0 : 0);
        f32x4 v0 = *(const f32x4*)src, v1 = *(const f32x4*)(src + (act1 ? 4 : 0));
        u32x4 w;
        w[0] = act ? pack2(v0[0], v0[1]) : 0u; w[1] = act ? pack2(v0[2], v0[3]) : 0u;
        w[2] = act1 ? pack2(v1[0], v1[1]) : 0u; w[3] = act1 ? pack2(v1[2], v1[3]) : 0u;
        *(u32x4*)(ws + OFF_KVF + ((size_t)(head * 2 + fr) * 64 + lane) * 16) = w;
      }
    }
  }
}

__global__ __launch_bounds__(512, 2) void k1_gemm(const u16* __restrict__ wqkv, const u16* __restrict__ wg,
                                                   const u16* __restrict__ wu, const u16* __restrict__ wo,
                                                   const u16* __restrict__ wdown, char* __restrict__ ws) {
  extern __shared__ __attribute__((aligned(16))) char smem[];
  const int bid = blockIdx.x;
  if (bid >= 240) {
    for (int t = (bid - 240) * 512 + threadIdx.x; t < RT_ITEMS; t += 16 * 512) retile_item(t, wo, wdown, ws);
    return;
  }
  const int p = (bid & 7) * 30 + (bid >> 3);
  const int mh = p / 120, rem = p % 120, nt = rem >> 3, mt = mh * 8 + (rem & 7);
  if (nt < 4) k1_body<2>(wqkv, wqkv, ws, mt, nt, smem);
  else if (nt < 6) k1_body<1>(wqkv, wqkv, ws, mt, nt - 4, smem);
  else k1_body<0>(wg, wu, ws, mt, nt - 6, smem);
  STAMP(1, 3);
}

constexpr int K2_LDS = 163840;
#define K2DD 3
constexpr int K2_D = K2DD;

__global__ __launch_bounds__(512, 2) void k2_gemm(const u16* __restrict__ x, const char* __restrict__ ws, u16* __restrict__ out) {
  extern __shared__ __attribute__((aligned(16))) char smem[];
  const int tid = threadIdx.x, lane = tid & 63;
  const int wave = __builtin_amdgcn_readfirstlane(tid >> 6);
  const int wq = wave & 3;
  const int g = lane >> 4, c = lane & 15;
  const int bid = blockIdx.x;
  const int mt = (bid & 7) * 4 + (bid >> 6), nt = (bid >> 3) & 7;

  STAMP(2, 0);
  f32x4 acc[4][5];
#pragma unroll
  for (int a = 0; a < 4; ++a)
#pragma unroll
    for (int b = 0; b < 5; ++b) acc[a][b] = (f32x4){0.f, 0.f, 0.f, 0.f};
  const int b0 = wave < 4 ? 0 : 3, nb = wave < 4 ? 3 : 2;
  const int tok = mt * 64 + wq * 16 + c;
  u32x2 xw[3];
#pragma unroll
  for (int i = 0; i < 3; ++i) xw[i] = *(const u32x2*)(x + (size_t)tok * D_ + nt * 80 + (i < nb ? b0 + i : b0) * 16 + 4 * g);

  if (wave < 4) {
    const char* hsrc = ws + OFF_H + ((size_t)(mt * 4) * 54 + wq) * 1024 + lane * 16;
    const char* wsrc = ws + OFF_WC + ((size_t)nt * 54 + wq) * 5120 + lane * 16;
    s16x8 xr[K2_D][4], wr[K2_D][5];
    auto ld = [&](int slot, int i) {
#pragma unroll
      for (int b = 0; b < 5; ++b) wr[slot][b] = *(const s16x8*)(wsrc + (size_t)(20 * i + b) * 1024);
#pragma unroll
      for (int a = 0; a < 4; ++a) xr[slot][a] = *(const s16x8*)(hsrc + (size_t)(a * 54 + 4 * i) * 1024);
    };
    auto mma1 = [&](int slot) {
#pragma unroll
      for (int a = 0; a < 4; ++a)
#pragma unroll
        for (int b = 0; b < 5; ++b) acc[a][b] = __builtin_amdgcn_mfma_f32_16x16x32_bf16(wr[slot][b], xr[slot][a], acc[a][b], 0, 0, 0);
    };
#pragma unroll
    for (int i = 0; i < K2_D; ++i) ld(i, i);
    SCHED;
#pragma unroll
    for (int i = 0; i < 13; ++i) {
      mma1(i % K2_D);
      if (i + K2_D < 13) ld(i % K2_D, i + K2_D);
      else if (i + K2_D == 13) { if (wave < 2) ld(i % K2_D, 13); }
      SCHED;
    }
    if (wave < 2) mma1(13 % K2_D);
    STAMP(2, 1);
  } else {
    const char* pqsrc = ws + OFF_PQ + ((size_t)(mt * 4) * 32 + wq * 8) * 640 + (g < 2 ? g * 256 + c * 16 : 512 + c * 8);
    const char* ksrc = ws + OFF_KVF + (size_t)(wq * 8) * 2048 + lane * 16;
    const char* wosrc = ws + OFF_WO + ((size_t)nt * 32 + wq * 8) * 5120 + lane * 16;
    u32x4 pq[2][4];
    s16x8 kvf[2][2], w2[2][5];
    auto ldA = [&](int sl, int hh) {
#pragma unroll
      for (int fr = 0; fr < 2; ++fr) kvf[sl][fr] = *(const s16x8*)(ksrc + hh * 2048 + fr * 1024);
#pragma unroll
      for (int tg = 0; tg < 4; ++tg) pq[sl][tg] = *(const u32x4*)(pqsrc + (tg * 32 + hh) * 640);
    };
    auto ldW = [&](int sl, int hh) {
#pragma unroll
      for (int b = 0; b < 5; ++b) w2[sl][b] = *(const s16x8*)(wosrc + (hh * 5 + b) * 1024);
    };
    ldA(0, 0); ldW(0, 0); ldA(1, 1); ldW(1, 1);
    const f32x4 z4 = (f32x4){0.f, 0.f, 0.f, 0.f};
#pragma unroll
    for (int hh = 0; hh < 8; ++hh) {
      const int sl = hh & 1;
      f32x4 d0[4], d1[4];
#pragma unroll
      for (int tg = 0; tg < 4; ++tg) {
        u32x4 pk4;
        pk4[0] = g < 3 ? pq[sl][tg][0] : 0u; pk4[1] = g < 3 ? pq[sl][tg][1] : 0u;
        pk4[2] = g < 2 ? pq[sl][tg][2] : 0u; pk4[3] = g < 2 ? pq[sl][tg][3] : 0u;
        const s16x8 pf = __builtin_bit_cast(s16x8, pk4);
        d1[tg] = __builtin_amdgcn_mfma_f32_16x16x32_bf16(kvf[sl][1], pf, z4, 0, 0, 0);
        d0[tg] = __builtin_amdgcn_mfma_f32_16x16x32_bf16(kvf[sl][0], pf, z4, 0, 0, 0);
      }
      if (hh + 2 < 8) ldA(sl, hh + 2);
      s16x8 of[4];
#pragma unroll
      for (int tg = 0; tg < 4; ++tg) {
        const float rden = frcp(bfr(bfr(d1[tg][1]) + 1e-6f));
        u32x4 ow;
        ow[0] = pack2(bfr(d0[tg][0]) * rden, bfr(d0[tg][1]) * rden); ow[1] = pack2(bfr(d0[tg][2]) * rden, bfr(d0[tg][3]) * rden);
        ow[2] = (unsigned)f2bf(bfr(d1[tg][0]) * rden); ow[3] = 0u;
        of[tg] = __builtin_bit_cast(s16x8, ow);
      }
#pragma unroll
      for (int a = 0; a < 4; ++a)
#pragma unroll
        for (int b = 0; b < 5; ++b) acc[a][b] = __builtin_amdgcn_mfma_f32_16x16x32_bf16(w2[sl][b], of[a], acc[a][b], 0, 0, 0);
      if (hh + 2 < 8) ldW(sl, hh + 2);
      SCHED;
    }
  }

  f32x4* red = (f32x4*)smem;
#pragma unroll
  for (int a = 0; a < 4; ++a)
#pragma unroll
    for (int b = 0; b < 5; ++b) red[(wave * 20 + a * 5 + b) * 64 + lane] = acc[a][b];
  __syncthreads();
#pragma unroll
  for (int i = 0; i < 3; ++i) {
    if (i < nb) {
      const int f = wq * 5 + b0 + i;
      f32x4 y1 = red[(0 * 20 + f) * 64 + lane], y2 = red[(4 * 20 + f) * 64 + lane];
#pragma unroll
      for (int sw = 1; sw < 4; ++sw) { y1 += red[(sw * 20 + f) * 64 + lane]; y2 += red[((4 + sw) * 20 + f) * 64 + lane]; }
      const int n = nt * 80 + (b0 + i) * 16 + 4 * g;
      float xv[4];
      xv[0] = __uint_as_float(xw[i][0] << 16); xv[1] = __uint_as_float(xw[i][0] & 0xffff0000u);
      xv[2] = __uint_as_float(xw[i][1] << 16); xv[3] = __uint_as_float(xw[i][1] & 0xffff0000u);
      float o[4];
#pragma unroll
      for (int r = 0; r < 4; ++r) {
        const float y = bfr(y1[r]);
        const float gs = bfr(y * frcp(1.0f + __expf(-y)));
        const float s = bfr(y2[r]);
        o[r] = xv[r] + bfr(s * gs);
      }
      u32x2 w; w[0] = pack2(o[0], o[1]); w[1] = pack2(o[2], o[3]);
      *(u32x2*)(out + (size_t)tok * D_ + n) = w;
    }
  }
  STAMP(2, 3);
}

extern "C" void kernel_launch(void* const* d_in, const int* in_sizes, int n_in,
                              void* d_out, int out_size, void* d_ws, size_t ws_size,
                              hipStream_t stream) {
  const u16* x = (const u16*)d_in[0];
  const u16* wqkv = (const u16*)d_in[1];
  const u16* wo = (const u16*)d_in[2];
  const u16* wg = (const u16*)d_in[4];
  const u16* wu = (const u16*)d_in[5];
  const u16* wd = (const u16*)d_in[6];
  char* ws = (char*)d_ws;
  u16* out = (u16*)d_out;

  hipFuncSetAttribute((const void*)k1_gemm, hipFuncAttributeMaxDynamicSharedMemorySize, K1_NS * K1_STAGE);
  hipFuncSetAttribute((const void*)k2_gemm, hipFuncAttributeMaxDynamicSharedMemorySize, K2_LDS);

  k0_prep<<<256, 256, 0, stream>>>(x, ws);
  k1_gemm<<<256, 512, K1_NS * K1_STAGE, stream>>>(wqkv, wg, wu, wo, wd, ws);
  k2_gemm<<<256, 512, K2_LDS, stream>>>(x, ws, out);
}
```

```cpp
#include <hip/hip_runtime.h>
#include <hip/hip_bf16.h>
#include <stdint.h>

typedef unsigned short u16;
typedef __attribute__((ext_vector_type(8))) short s16x8;
typedef __attribute__((ext_vector_type(4))) short s16x4;
typedef __attribute__((ext_vector_type(4))) float f32x4;
typedef __attribute__((ext_vector_type(16))) float f32x16;
typedef __attribute__((ext_vector_type(2))) unsigned u32x2;
typedef __attribute__((ext_vector_type(4))) unsigned u32x4;
typedef __attribute__((address_space(3))) s16x4* lds_s16x4_ptr;
typedef __attribute__((address_space(1))) unsigned gu32;
typedef __attribute__((address_space(1))) unsigned long long gu64;

#define RLX_AGENT __ATOMIC_RELAXED, __HIP_MEMORY_SCOPE_AGENT

constexpr int S_ = 2048, D_ = 640, F_ = 1728, NQKV_ = 1920;

constexpr size_t OFF_CNT   = 0;
constexpr size_t OFF_XN    = 8192;
constexpr size_t SZ_XN     = (size_t)S_ * D_ * 2;
constexpr size_t OFF_H     = OFF_XN + SZ_XN;
constexpr size_t SZ_H      = (size_t)S_ * F_ * 2;
constexpr size_t OFF_PQ    = OFF_H + SZ_H;
constexpr size_t SZ_PQ     = (size_t)S_ * D_ * 2;
constexpr size_t OFF_WC    = OFF_PQ + SZ_PQ;
constexpr size_t SZ_WC     = (size_t)8 * 54 * 5 * 1024;
constexpr size_t OFF_WO    = OFF_WC + SZ_WC;
constexpr size_t SZ_WO     = (size_t)8 * 32 * 5 * 1024;
constexpr size_t OFF_KVP   = OFF_WO + SZ_WO;
constexpr size_t SZ_KVP    = (size_t)16 * 32 * 504 * 4;
constexpr size_t OFF_KVF   = OFF_KVP + SZ_KVP;
constexpr size_t SZ_KVF    = 65536;

__device__ __forceinline__ float bf2f(u16 v) { return __uint_as_float(((unsigned)v) << 16); }
__device__ __forceinline__ u16 f2bf(float f) { return __builtin_bit_cast(u16, (__bf16)f); }
__device__ __forceinline__ float bfr(float f) { return bf2f(f2bf(f)); }
__device__ __forceinline__ unsigned pack2(float a, float b) { return (unsigned)f2bf(a) | ((unsigned)f2bf(b) << 16); }
__device__ __forceinline__ float phi_f(float t) {
  float p = t > 0.f ? bfr(t + 1.0f) : bfr(__expf(t));
  return bfr(p + 1e-6f);
}
typedef __attribute__((ext_vector_type(2))) float f32x2;
typedef __attribute__((ext_vector_type(2))) __bf16 bf16x2;
__device__ __forceinline__ unsigned cvt2(f32x2 v) { return __builtin_bit_cast(unsigned, __builtin_convertvector(v, bf16x2)); }
__device__ __forceinline__ f32x2 unp2(unsigned u) { return (f32x2){__uint_as_float(u << 16), __uint_as_float(u & 0xffff0000u)}; }
__device__ __forceinline__ f32x2 bfr2(f32x2 v) { return unp2(cvt2(v)); }
__device__ __forceinline__ f32x2 lo2(f32x4 v) { return __builtin_shufflevector(v, v, 0, 1); }
__device__ __forceinline__ f32x2 hi2(f32x4 v) { return __builtin_shufflevector(v, v, 2, 3); }
__device__ __forceinline__ unsigned phi2(f32x2 a) {
  const f32x2 t = bfr2(a), m = t * 1.44269504088896340736f, p = t + 1.0f;
  f32x2 sel;
  sel[0] = t[0] > 0.f ? p[0] : __builtin_amdgcn_exp2f(m[0]);
  sel[1] = t[1] > 0.f ? p[1] : __builtin_amdgcn_exp2f(m[1]);
  return cvt2(bfr2(sel) + 1e-6f);
}
__device__ __forceinline__ f32x2 sigm2(f32x2 g) {
  const f32x2 m = g * -1.44269504088896340736f;
  f32x2 e; e[0] = __builtin_amdgcn_exp2f(m[0]); e[1] = __builtin_amdgcn_exp2f(m[1]);
  e = e + 1.0f;
  f32x2 r; r[0] = __builtin_amdgcn_rcpf(e[0]); r[1] = __builtin_amdgcn_rcpf(e[1]);
  return r;
}
__device__ __forceinline__ int aswz(int row) { int x = (row >> 2) & 3; return (((x ^ (x >> 1)) & 1) << 1) | (x >> 1); }

#define WAIT_V(n) asm volatile("s_waitcnt vmcnt(" #n ")" ::: "memory")
#define STAMP(kid, idx)
#define WAIT_L0 asm volatile("s_waitcnt lgkmcnt(0)" ::: "memory")
#define BAR __builtin_amdgcn_s_barrier()
#define SCHED __builtin_amdgcn_sched_barrier(0)
__device__ __forceinline__ float frcp(float v) { return __builtin_amdgcn_rcpf(v); }

constexpr int RT_ITEMS = 216 * 80 + 128 * 80;
__device__ __forceinline__ void retile_item(int t, const u16* __restrict__ wo, const u16* __restrict__ wdown, char* __restrict__ ws) {
    if (t < 216 * 80) {
      const int koct = t / 80, noct = t - koct * 80;
      const u16* src = wdown + (size_t)(koct * 8) * D_ + noct * 8;
      u32x4 r[8];
#pragma unroll
      for (int jj = 0; jj < 8; ++jj) r[jj] = *(const u32x4*)(src + (size_t)jj * D_);
      const int n0 = noct * 8, nt = n0 / 80, nn = n0 - nt * 80, nf = nn >> 4, c0 = nn & 15, kt = koct >> 2, gg = koct & 3;
      char* dst = ws + OFF_WC + ((size_t)((nt * 54 + kt) * 5 + nf) * 64 + gg * 16 + c0) * 16;
#pragma unroll
      for (int i = 0; i < 4; ++i) {
        u32x4 lo, hi;
#pragma unroll
        for (int q = 0; q < 4; ++q) {
          lo[q] = (r[2 * q][i] & 0xffffu) | (r[2 * q + 1][i] << 16);
          hi[q] = (r[2 * q][i] >> 16) | (r[2 * q + 1][i] & 0xffff0000u);
        }
        *(u32x4*)(dst + (2 * i) * 16) = lo;
        *(u32x4*)(dst + (2 * i + 1) * 16) = hi;
      }
    } else {
      const int t2 = t - 216 * 80, hg = t2 / 80, noct = t2 - hg * 80, hd = hg >> 2, gg = hg & 3;
      const u16* src = wo + (size_t)(hd * 20) * D_ + noct * 8;
      u32x4 r[4];
#pragma unroll
      for (int jj = 0; jj < 4; ++jj) r[jj] = *(const u32x4*)(src + (size_t)(4 * gg + jj) * D_);
      const u32x4 e = *(const u32x4*)(src + (size_t)(16 + gg) * D_);
      const int n0 = noct * 8, nt = n0 / 80, nn = n0 - nt * 80, nf = nn >> 4, c0 = nn & 15;
      char* dst = ws + OFF_WO + ((size_t)((nt * 32 + hd) * 5 + nf) * 64 + c0) * 16;
#pragma unroll
      for (int i = 0; i < 4; ++i) {
        u32x4 lo, hi;
        lo[0] = (r[0][i] & 0xffffu) | (r[1][i] << 16);
        lo[1] = (r[2][i] & 0xffffu) | (r[3][i] << 16);
        hi[0] = (r[0][i] >> 16) | (r[1][i] & 0xffff0000u);
        hi[1] = (r[2][i] >> 16) | (r[3][i] & 0xffff0000u);
        lo[2] = e[i] & 0xffffu; lo[3] = 0u;
        hi[2] = e[i] >> 16; hi[3] = 0u;
        *(u32x4*)(dst + (gg * 16 + 2 * i) * 16) = lo;
        *(u32x4*)(dst + (gg * 16 + 2 * i + 1) * 16) = hi;
      }
    }
}

__global__ __launch_bounds__(256) void k0_prep(const u16* __restrict__ x, char* __restrict__ ws) {
  const int tid = threadIdx.x;
  {
    if (blockIdx.x == 0 && tid < 4) ((unsigned*)(ws + OFF_CNT))[tid * 64] = 0u;
    const int row = blockIdx.x * 8 + (tid >> 5);
    const int l32 = tid & 31;
    const u16* xr = x + (size_t)row * D_;
    float v[20];
    float sum = 0.f;
#pragma unroll
    for (int i = 0; i < 5; ++i) {
      u32x2 w = *(const u32x2*)(xr + (l32 + 32 * i) * 4);
      v[4 * i + 0] = __uint_as_float(w[0] << 16);
      v[4 * i + 1] = __uint_as_float(w[0] & 0xffff0000u);
      v[4 * i + 2] = __uint_as_float(w[1] << 16);
      v[4 * i + 3] = __uint_as_float(w[1] & 0xffff0000u);
      sum += v[4 * i] + v[4 * i + 1] + v[4 * i + 2] + v[4 * i + 3];
    }
#pragma unroll
    for (int o = 16; o >= 1; o >>= 1) sum += __shfl_xor(sum, o);
    const float mean = sum * (1.0f / D_);
    float sq = 0.f;
#pragma unroll
    for (int i = 0; i < 20; ++i) { float d = v[i] - mean; sq += d * d; }
#pragma unroll
    for (int o = 16; o >= 1; o >>= 1) sq += __shfl_xor(sq, o);
    const float mb = bfr(mean);
    const float vb = bfr(sq * (1.0f / D_));
    const float sd = bfr(sqrtf(bfr(vb + 1e-5f)));
    u16* xn = (u16*)(ws + OFF_XN);
    const int mt = row >> 7, r = row & 127, sw = aswz(r);
#pragma unroll
    for (int i = 0; i < 5; ++i) {
      const int k = (l32 + 32 * i) * 4;
      float o0 = bfr(v[4 * i + 0] - mb) / sd, o1 = bfr(v[4 * i + 1] - mb) / sd;
      float o2 = bfr(v[4 * i + 2] - mb) / sd, o3 = bfr(v[4 * i + 3] - mb) / sd;
      u32x2 w; w[0] = pack2(o0, o1); w[1] = pack2(o2, o3);
      const int kt = k >> 5, ch = (k >> 3) & 3, hf = (k >> 2) & 1;
      size_t off = ((size_t)(mt * 20 + kt) * 128 + r) * 32 + ((ch ^ sw) << 3) + (hf << 2);
      *(u32x2*)(xn + off) = w;
    }
  }
}

constexpr int K1_STAGE = 32768;
#define K1NS 4
constexpr int K1_NS = K1NS;

template <int TYPE>
__device__ __forceinline__ void k1_body(const u16* __restrict__ wA, const u16* __restrict__ wB, char* __restrict__ ws,
                                        int mt, int j, char* smem) {
  constexpr int NCB = TYPE == 0 ? 6 : 5;
  constexpr int NFR = TYPE == 0 ? 6 : 5;
  constexpr int LDW = TYPE == 0 ? F_ : NQKV_;
  const int tid = threadIdx.x, lane = tid & 63;
  const int wave = __builtin_amdgcn_readfirstlane(tid >> 6);
  const int wm = wave >> 2, wn = (TYPE == 2 && wave >= 4) ? ((wave + 2) & 3) : (wave & 3);
  const int g = lane >> 4, c = lane & 15;

  const char* asrc = ws + OFF_XN + (size_t)mt * 20 * 8192 + wave * 1024 + lane * 16;
  const u16* bsrc[3];
#pragma unroll
  for (int t = 0; t < 3; ++t) {
    int ii = wave + 8 * t; if (ii >= NCB * 4) ii = wave;
    const int cb = ii >> 2, krow = (ii & 3) * 8 + (lane >> 3), u = lane & 7;
    const int sk = ((krow >> 1) & 1) | (((krow >> 3) & 1) << 1);
    const int ch = (u >> 1) ^ sk;
    const int lc = cb * 64 + ch * 16 + (u & 1) * 8;
    const u16* p;
    if (TYPE == 0) p = (lc < 192) ? (wA + j * 192 + lc) : (wB + j * 192 + (lc - 192));
    else if (TYPE == 1) p = wA + j * 320 + lc;
    else p = (lc < 160) ? (wA + 640 + j * 160 + lc) : (wA + 1280 + j * 160 + (lc - 160));
    bsrc[t] = p + (size_t)krow * LDW;
  }
  const bool has3 = (NCB == 6) || (wave < 4);

  auto issue = [&](int kt) {
    char* sb = smem + (kt % K1_NS) * K1_STAGE;
    __builtin_amdgcn_global_load_lds((const unsigned*)(asrc + (size_t)kt * 8192), (unsigned*)(sb + wave * 1024), 16, 0, 0);
    __builtin_amdgcn_global_load_lds((const unsigned*)(bsrc[0] + (size_t)kt * 32 * LDW), (unsigned*)(sb + 8192 + wave * 1024), 16, 0, 0);
    __builtin_amdgcn_global_load_lds((const unsigned*)(bsrc[1] + (size_t)kt * 32 * LDW), (unsigned*)(sb + 8192 + (wave + 8) * 1024), 16, 0, 0);
    if (has3) __builtin_amdgcn_global_load_lds((const unsigned*)(bsrc[2] + (size_t)kt * 32 * LDW), (unsigned*)(sb + 8192 + (wave + 16) * 1024), 16, 0, 0);
  };

  const int xoff = (wm * 64 + c) * 64 + ((g ^ aswz(c)) << 4);
  int woff[NFR];
  {
    const int q = (lane & 15) >> 2, p = lane & 3;
    const int s = (q >> 1) | ((g & 1) << 1);
    const int lo = (8 * g + q) * 128 + (s << 5) + p * 8;
#pragma unroll
    for (int i = 0; i < NFR; ++i) {
      const int nf = TYPE == 0 ? (i < 3 ? 3 * wn + i : 12 + 3 * wn + (i - 3)) : 5 * wn + i;
      woff[i] = 8192 + (nf >> 2) * 4096 + (lo ^ ((nf & 3) << 5));
    }
  }

  f32x4 acc[4][NFR];
#pragma unroll
  for (int a = 0; a < 4; ++a)
#pragma unroll
    for (int b = 0; b < NFR; ++b) acc[a][b] = (f32x4){0.f, 0.f, 0.f, 0.f};

  auto readfr = [&](int kt, s16x8 (&xf)[4], s16x8 (&wf)[NFR]) {
    const char* sb = smem + (kt % K1_NS) * K1_STAGE;
#pragma unroll
    for (int b = 0; b < NFR; ++b) {
      s16x4 lo = __builtin_amdgcn_ds_read_tr16_b64_v4i16((lds_s16x4_ptr)(sb + woff[b]));
      s16x4 hi = __builtin_amdgcn_ds_read_tr16_b64_v4i16((lds_s16x4_ptr)(sb + woff[b] + 512));
      wf[b] = __builtin_shufflevector(lo, hi, 0, 1, 2, 3, 4, 5, 6, 7);
    }
#pragma unroll
    for (int a = 0; a < 4; ++a) xf[a] = *(const s16x8*)(sb + xoff + a * 1024);
  };
  auto mma = [&](const s16x8 (&xf)[4], const s16x8 (&wf)[NFR]) {
#pragma unroll
    for (int a = 0; a < 4; ++a)
#pragma unroll
      for (int b = 0; b < NFR; ++b) {
        if (TYPE == 2) acc[a][b] = __builtin_amdgcn_mfma_f32_16x16x32_bf16(xf[a], wf[b], acc[a][b], 0, 0, 0);
        else           acc[a][b] = __builtin_amdgcn_mfma_f32_16x16x32_bf16(wf[b], xf[a], acc[a][b], 0, 0, 0);
      }
  };
#define WAITN(N) asm volatile("s_waitcnt vmcnt(%0)" :: "n"(N) : "memory")
#define K1_WAIT(n) do { if (has3) WAITN(4 * (n)); else WAITN(3 * (n)); } while (0)

  s16x8 xA[4], wAf[NFR], xB[4], wBf[NFR];
#define SGB(m, n) __builtin_amdgcn_sched_group_barrier(m, n, 0)
  auto pattern_full = [&]() {
    SGB(0x10, 1); SGB(0x8, 1); SGB(0x10, 1); SGB(0x8, 1); SGB(0x10, 1); SGB(0x8, 1); SGB(0x10, 1); SGB(0x8, 1);
#pragma unroll
    for (int i = 0; i < 4 + 2 * NFR; ++i) { SGB(0x100, 1); SGB(0x8, 1); }
    SGB(0x8, 4);
  };
  auto pattern_tail = [&]() {
#pragma unroll
    for (int i = 0; i < 4 + 2 * NFR; ++i) { SGB(0x100, 1); SGB(0x8, 1); }
    SGB(0x8, 8);
  };
  STAMP(1, 0);
#pragma unroll
  for (int i = 0; i < K1_NS; ++i) issue(i);
  K1_WAIT(K1_NS - 1);
  BAR;
  STAMP(1, 1);
  readfr(0, xA, wAf);
  SCHED;
#pragma unroll
  for (int kt = 0; kt < 19; ++kt) {
    if (kt + K1_NS <= 20) K1_WAIT(K1_NS - 2); else K1_WAIT(18 - kt);
    WAIT_L0; BAR; SCHED;
    if (kt + K1_NS < 20) issue(kt + K1_NS);
    if (kt & 1) { readfr(kt + 1, xA, wAf); mma(xB, wBf); }
    else        { readfr(kt + 1, xB, wBf); mma(xA, wAf); }
    if (kt + K1_NS < 20) pattern_full(); else pattern_tail();
    SCHED;
  }
  mma(xB, wBf);

  WAIT_L0; BAR;
  STAMP(1, 2);
  if (TYPE == 0) {
#pragma unroll
    for (int a = 0; a < 4; ++a) {
#pragma unroll
      for (int b = 0; b < 3; ++b) {
        u32x2 w;
        w[0] = cvt2(bfr2(sigm2(bfr2(lo2(acc[a][b])))) * bfr2(lo2(acc[a][b + 3])));
        w[1] = cvt2(bfr2(sigm2(bfr2(hi2(acc[a][b])))) * bfr2(hi2(acc[a][b + 3])));
        const int f0 = wn * 48 + b * 16;
        const int ktl = f0 >> 5, gk = ((f0 >> 4) & 1) * 2 + (g >> 1);
        *(u32x2*)(smem + (((wm * 4 + a) * 6 + ktl) * 64 + gk * 16 + c) * 16 + (g & 1) * 8) = w;
      }
    }
    __syncthreads();
    char* H = ws + OFF_H + ((size_t)(mt * 8) * 54 + j * 6) * 1024;
#pragma unroll
    for (int i = 0; i < 6; ++i) {
      const int q = tid + 512 * i;
      const int m16 = q / 384, rest = q - m16 * 384;
      const u32x4 v = *(const u32x4*)(smem + q * 16);
      *(u32x4*)(H + (size_t)m16 * 54 * 1024 + rest * 16) = v;
    }
  } else if (TYPE == 1) {
#pragma unroll
    for (int a = 0; a < 4; ++a) {
#pragma unroll
      for (int b = 0; b < 5; ++b) {
        u32x2 w; w[0] = phi2(lo2(acc[a][b])); w[1] = phi2(hi2(acc[a][b]));
        const int n = wn * 80 + b * 16 + 4 * g, hl = n / 20, d0 = n - hl * 20;
        const int ro = d0 < 16 ? (d0 >> 3) * 256 + c * 16 + (d0 & 7) * 2 : 512 + c * 8;
        *(u32x2*)(smem + ((wm * 4 + a) * 16 + hl) * 640 + ro) = w;
      }
    }
    __syncthreads();
    char* PQ = ws + OFF_PQ + ((size_t)(mt * 8) * 32 + j * 16) * 640;
#pragma unroll
    for (int i = 0; i < 10; ++i) {
      const int q = tid + 512 * i;
      const int m16 = q / 640, rest = q - m16 * 640;
      const u32x4 v = *(const u32x4*)(smem + q * 16);
      *(u32x4*)(PQ + (size_t)m16 * 20480 + rest * 16) = v;
    }
  } else {
#pragma unroll
    for (int a = 0; a < 4; ++a)
#pragma unroll
      for (int b = 0; b < 5; ++b) {
        const int col = wn * 80 + b * 16 + c;
        u32x2 w;
        if (wn < 2) { w[0] = phi2(lo2(acc[a][b])); w[1] = phi2(hi2(acc[a][b])); }
        else        { w[0] = cvt2(lo2(acc[a][b])); w[1] = cvt2(hi2(acc[a][b])); }
        *(u32x2*)(smem + col * 272 + (wm * 64 + a * 16 + 4 * g) * 2) = w;
      }
    __syncthreads();
    const int r32 = lane & 31, hh = lane >> 5;
    f32x16 akv, az;
#pragma unroll
    for (int i = 0; i < 16; ++i) { akv[i] = 0.f; az[i] = 0.f; }
    s16x8 ones;
#pragma unroll
    for (int i = 0; i < 8; ++i) ones[i] = (short)0x3F80;
#pragma unroll
    for (int s = 0; s < 8; ++s) {
      s16x8 af = *(const s16x8*)(smem + (20 * wave + r32) * 272 + (16 * s + 8 * hh) * 2);
      s16x8 bfv = *(const s16x8*)(smem + (160 + 20 * wave + r32) * 272 + (16 * s + 8 * hh) * 2);
      akv = __builtin_amdgcn_mfma_f32_32x32x16_bf16(af, bfv, akv, 0, 0, 0);
      az = __builtin_amdgcn_mfma_f32_32x32x16_bf16(af, ones, az, 0, 0, 0);
    }
    const int head = j * 8 + wave;
    float* P = (float*)(ws + OFF_KVP) + (size_t)(mt * 32 + head) * 504;
    if (r32 <= 20) {
#pragma unroll
      for (int q = 0; q < 3; ++q) {
        const int d0 = 8 * q + 4 * hh;
        if (d0 < 20) {
          f32x4 val;
#pragma unroll
          for (int r = 0; r < 4; ++r) val[r] = (r32 == 20) ? az[4 * q + r] : akv[4 * q + r];
          asm volatile("global_store_dwordx4 %0, %1, off sc1" :: "v"(P + r32 * 24 + d0), "v"(val) : "memory");
        }
      }
    }
    asm volatile("s_waitcnt vmcnt(0)" ::: "memory");
    __syncthreads();
    volatile int* flag = (volatile int*)(smem + 98304);
    if (tid == 0) {
      unsigned tk = __hip_atomic_fetch_add((gu32*)(ws + OFF_CNT) + j * 64, 1u, RLX_AGENT);
      if (tk == 15u) {
        __builtin_amdgcn_fence(__ATOMIC_ACQUIRE, "agent");
        asm volatile("s_waitcnt vmcnt(0)" ::: "memory");
      }
      *flag = (int)tk;
    }
    __syncthreads();
    if (*flag == 15) {
      const float* Pf = (const float*)(ws + OFF_KVP) + (size_t)head * 504;
      const int l0 = lane * 4, l1 = (lane < 62 ? lane + 64 : lane) * 4;
      f32x4 a0[16], a1[16];
#pragma unroll
      for (int m = 0; m < 16; ++m) {
        a0[m] = *(const f32x4*)(Pf + (size_t)m * 32 * 504 + l0);
        a1[m] = *(const f32x4*)(Pf + (size_t)m * 32 * 504 + l1);
      }
      f32x4 s0 = a0[0], s1 = a1[0];
#pragma unroll
      for (int m = 1; m < 16; ++m) { s0 += a0[m]; s1 += a1[m]; }
      float* kvs = (float*)(smem + 100352 + wave * 2048);
      *(f32x4*)(kvs + l0) = s0;
      if (lane < 62) *(f32x4*)(kvs + l1) = s1;
      WAIT_L0;
#pragma unroll
      for (int fr = 0; fr < 2; ++fr) {
        const int e = fr == 0 ? c : ((c & 3) == 0 ? 16 + (c >> 2) : ((c & 3) == 1 ? 20 : 99)), d0 = 8 * g;
        const bool act = (e <= 20 && d0 < 20), act1 = act && (d0 < 16);
        const float* src = kvs + (act ? e * 24 + d0 : 0);
        f32x4 v0 = *(const f32x4*)src, v1 = *(const f32x4*)(src + (act1 ? 4 : 0));
        u32x4 w;
        w[0] = act ? pack2(v0[0], v0[1]) : 0u; w[1] = act ? pack2(v0[2], v0[3]) : 0u;
        w[2] = act1 ? pack2(v1[0], v1[1]) : 0u; w[3] = act1 ? pack2(v1[2], v1[3]) : 0u;
        *(u32x4*)(ws + OFF_KVF + ((size_t)(head * 2 + fr) * 64 + lane) * 16) = w;
      }
    }
  }
}

__global__ __launch_bounds__(512, 2) void k1_gemm(const u16* __restrict__ wqkv, const u16* __restrict__ wg,
                                                   const u16* __restrict__ wu, const u16* __restrict__ wo,
                                                   const u16* __restrict__ wdown, char* __restrict__ ws) {
  extern __shared__ __attribute__((aligned(16))) char smem[];
  const int bid = blockIdx.x;
  if (bid >= 240) {
    for (int t = (bid - 240) * 512 + threadIdx.x; t < RT_ITEMS; t += 16 * 512) retile_item(t, wo, wdown, ws);
    return;
  }
  const int p = (bid & 7) * 30 + (bid >> 3);
  const int mh = p / 120, rem = p % 120, nt = rem >> 3, mt = mh * 8 + (rem & 7);
  if (nt < 4) k1_body<2>(wqkv, wqkv, ws, mt, nt, smem);
  else if (nt < 6) k1_body<1>(wqkv, wqkv, ws, mt, nt - 4, smem);
  else k1_body<0>(wg, wu, ws, mt, nt - 6, smem);
  STAMP(1, 3);
}

constexpr int K2_LDS = 163840;
#define K2DD 3
constexpr int K2_D = K2DD;

__global__ __launch_bounds__(512, 2) void k2_gemm(const u16* __restrict__ x, const char* __restrict__ ws, u16* __restrict__ out) {
  extern __shared__ __attribute__((aligned(16))) char smem[];
  const int tid = threadIdx.x, lane = tid & 63;
  const int wave = __builtin_amdgcn_readfirstlane(tid >> 6);
  const int wq = wave & 3;
  const int g = lane >> 4, c = lane & 15;
  const int bid = blockIdx.x;
  const int mt = (bid & 7) * 4 + (bid >> 6), nt = (bid >> 3) & 7;

  STAMP(2, 0);
  f32x4 acc[4][5];
#pragma unroll
  for (int a = 0; a < 4; ++a)
#pragma unroll
    for (int b = 0; b < 5; ++b) acc[a][b] = (f32x4){0.f, 0.f, 0.f, 0.f};
  const int b0 = wave < 4 ? 0 : 3, nb = wave < 4 ? 3 : 2;
  const int tok = mt * 64 + wq * 16 + c;
  u32x2 xw[3];
#pragma unroll
  for (int i = 0; i < 3; ++i) xw[i] = *(const u32x2*)(x + (size_t)tok * D_ + nt * 80 + (i < nb ? b0 + i : b0) * 16 + 4 * g);

  if (wave < 4) {
    const char* hsrc = ws + OFF_H + ((size_t)(mt * 4) * 54 + wq) * 1024 + lane * 16;
    const char* wsrc = ws + OFF_WC + ((size_t)nt * 54 + wq) * 5120 + lane * 16;
    s16x8 xr[K2_D][4], wr[K2_D][5];
    auto ld = [&](int slot, int i) {
#pragma unroll
      for (int b = 0; b < 5; ++b) wr[slot][b] = *(const s16x8*)(wsrc + (size_t)(20 * i + b) * 1024);
#pragma unroll
      for (int a = 0; a < 4; ++a) xr[slot][a] = *(const s16x8*)(hsrc + (size_t)(a * 54 + 4 * i) * 1024);
    };
    auto mma1 = [&](int slot) {
#pragma unroll
      for (int a = 0; a < 4; ++a)
#pragma unroll
        for (int b = 0; b < 5; ++b) acc[a][b] = __builtin_amdgcn_mfma_f32_16x16x32_bf16(wr[slot][b], xr[slot][a], acc[a][b], 0, 0, 0);
    };
#pragma unroll
    for (int i = 0; i < K2_D; ++i) ld(i, i);
    SCHED;
#pragma unroll
    for (int i = 0; i < 13; ++i) {
      mma1(i % K2_D);
      if (i + K2_D < 13) ld(i % K2_D, i + K2_D);
      else if (i + K2_D == 13) { if (wave < 2) ld(i % K2_D, 13); }
      SCHED;
    }
    if (wave < 2) mma1(13 % K2_D);
    STAMP(2, 1);
  } else {
    const char* pqsrc = ws + OFF_PQ + ((size_t)(mt * 4) * 32 + wq * 8) * 640 + (g < 2 ? g * 256 + c * 16 : 512 + c * 8);
    const char* ksrc = ws + OFF_KVF + (size_t)(wq * 8) * 2048 + lane * 16;
    const char* wosrc = ws + OFF_WO + ((size_t)nt * 32 + wq * 8) * 5120 + lane * 16;
    u32x4 pq[2][4];
    s16x8 kvf[2][2], w2[2][5];
    auto ldA = [&](int sl, int hh) {
#pragma unroll
      for (int fr = 0; fr < 2; ++fr) kvf[sl][fr] = *(const s16x8*)(ksrc + hh * 2048 + fr * 1024);
#pragma unroll
      for (int tg = 0; tg < 4; ++tg) pq[sl][tg] = *(const u32x4*)(pqsrc + (tg * 32 + hh) * 640);
    };
    auto ldW = [&](int sl, int hh) {
#pragma unroll
      for (int b = 0; b < 5; ++b) w2[sl][b] = *(const s16x8*)(wosrc + (hh * 5 + b) * 1024);
    };
    ldA(0, 0); ldW(0, 0); ldA(1, 1); ldW(1, 1);
    const f32x4 z4 = (f32x4){0.f, 0.f, 0.f, 0.f};
#pragma unroll
    for (int hh = 0; hh < 8; ++hh) {
      const int sl = hh & 1;
      f32x4 d0[4], d1[4];
#pragma unroll
      for (int tg = 0; tg < 4; ++tg) {
        u32x4 pk4;
        pk4[0] = g < 3 ? pq[sl][tg][0] : 0u; pk4[1] = g < 3 ? pq[sl][tg][1] : 0u;
        pk4[2] = g < 2 ? pq[sl][tg][2] : 0u; pk4[3] = g < 2 ? pq[sl][tg][3] : 0u;
        const s16x8 pf = __builtin_bit_cast(s16x8, pk4);
        d1[tg] = __builtin_amdgcn_mfma_f32_16x16x32_bf16(kvf[sl][1], pf, z4, 0, 0, 0);
        d0[tg] = __builtin_amdgcn_mfma_f32_16x16x32_bf16(kvf[sl][0], pf, z4, 0, 0, 0);
      }
      if (hh + 2 < 8) ldA(sl, hh + 2);
      s16x8 of[4];
#pragma unroll
      for (int tg = 0; tg < 4; ++tg) {
        const float rden = frcp(bfr(bfr(d1[tg][1]) + 1e-6f));
        u32x4 ow;
        ow[0] = cvt2(bfr2(lo2(d0[tg])) * rden); ow[1] = cvt2(bfr2(hi2(d0[tg])) * rden);
        ow[2] = (unsigned)f2bf(bfr(d1[tg][0]) * rden); ow[3] = 0u;
        of[tg] = __builtin_bit_cast(s16x8, ow);
      }
#pragma unroll
      for (int a = 0; a < 4; ++a)
#pragma unroll
        for (int b = 0; b < 5; ++b) acc[a][b] = __builtin_amdgcn_mfma_f32_16x16x32_bf16(w2[sl][b], of[a], acc[a][b], 0, 0, 0);
      if (hh + 2 < 8) ldW(sl, hh + 2);
      SCHED;
    }
  }

  f32x4* red = (f32x4*)smem;
#pragma unroll
  for (int a = 0; a < 4; ++a)
#pragma unroll
    for (int b = 0; b < 5; ++b) red[(wave * 20 + a * 5 + b) * 64 + lane] = acc[a][b];
  __syncthreads();
#pragma unroll
  for (int i = 0; i < 3; ++i) {
    if (i < nb) {
      const int f = wq * 5 + b0 + i;
      f32x4 y1 = red[(0 * 20 + f) * 64 + lane], y2 = red[(4 * 20 + f) * 64 + lane];
#pragma unroll
      for (int sw = 1; sw < 4; ++sw) { y1 += red[(sw * 20 + f) * 64 + lane]; y2 += red[((4 + sw) * 20 + f) * 64 + lane]; }
      const int n = nt * 80 + (b0 + i) * 16 + 4 * g;
      u32x2 w;
      {
        const f32x2 y = bfr2(lo2(y1)), gs = bfr2(y * sigm2(y)), sv = bfr2(lo2(y2));
        w[0] = cvt2(unp2(xw[i][0]) + bfr2(sv * gs));
      }
      {
        const f32x2 y = bfr2(hi2(y1)), gs = bfr2(y * sigm2(y)), sv = bfr2(hi2(y2));
        w[1] = cvt2(unp2(xw[i][1]) + bfr2(sv * gs));
      }
      *(u32x2*)(out + (size_t)tok * D_ + n) = w;
    }
  }
  STAMP(2, 3);
}

extern "C" void kernel_launch(void* const* d_in, const int* in_sizes, int n_in,
                              void* d_out, int out_size, void* d_ws, size_t ws_size,
                              hipStream_t stream) {
  const u16* x = (const u16*)d_in[0];
  const u16* wqkv = (const u16*)d_in[1];
  const u16* wo = (const u16*)d_in[2];
  const u16* wg = (const u16*)d_in[4];
  const u16* wu = (const u16*)d_in[5];
  const u16* wd = (const u16*)d_in[6];
  char* ws = (char*)d_ws;
  u16* out = (u16*)d_out;

  hipFuncSetAttribute((const void*)k1_gemm, hipFuncAttributeMaxDynamicSharedMemorySize, K1_NS * K1_STAGE);
  hipFuncSetAttribute((const void*)k2_gemm, hipFuncAttributeMaxDynamicSharedMemorySize, K2_LDS);

  k0_prep<<<256, 256, 0, stream>>>(x, ws);
  k1_gemm<<<256, 512, K1_NS * K1_STAGE, stream>>>(wqkv, wg, wu, wo, wd, ws);
  k2_gemm<<<256, 512, K2_LDS, stream>>>(x, ws, out);
}
```

```cpp
#include <hip/hip_runtime.h>
#include <hip/hip_bf16.h>
#include <stdint.h>

typedef unsigned short u16;
typedef __attribute__((ext_vector_type(8))) short s16x8;
typedef __attribute__((ext_vector_type(4))) short s16x4;
typedef __attribute__((ext_vector_type(4))) float f32x4;
typedef __attribute__((ext_vector_type(16))) float f32x16;
typedef __attribute__((ext_vector_type(2))) unsigned u32x2;
typedef __attribute__((ext_vector_type(4))) unsigned u32x4;
typedef __attribute__((address_space(3))) s16x4* lds_s16x4_ptr;
typedef __attribute__((address_space(1))) unsigned gu32;
typedef __attribute__((address_space(1))) unsigned long long gu64;

#define RLX_AGENT __ATOMIC_RELAXED, __HIP_MEMORY_SCOPE_AGENT

constexpr int S_ = 2048, D_ = 640, F_ = 1728, NQKV_ = 1920;

constexpr size_t OFF_CNT   = 0;
constexpr size_t OFF_XN    = 8192;
constexpr size_t SZ_XN     = (size_t)S_ * D_ * 2;
constexpr size_t OFF_H     = OFF_XN + SZ_XN;
constexpr size_t SZ_H      = (size_t)S_ * F_ * 2;
constexpr size_t OFF_PQ    = OFF_H + SZ_H;
constexpr size_t SZ_PQ     = (size_t)S_ * D_ * 2;
constexpr size_t OFF_WC    = OFF_PQ + SZ_PQ;
constexpr size_t SZ_WC     = (size_t)8 * 54 * 5 * 1024;
constexpr size_t OFF_WO    = OFF_WC + SZ_WC;
constexpr size_t SZ_WO     = (size_t)8 * 32 * 5 * 1024;
constexpr size_t OFF_KVP   = OFF_WO + SZ_WO;
constexpr size_t SZ_KVP    = (size_t)16 * 32 * 504 * 4;
constexpr size_t OFF_KVF   = OFF_KVP + SZ_KVP;
constexpr size_t SZ_KVF    = 65536;

__device__ __forceinline__ float bf2f(u16 v) { return __uint_as_float(((unsigned)v) << 16); }
__device__ __forceinline__ u16 f2bf(float f) { return __builtin_bit_cast(u16, (__bf16)f); }
__device__ __forceinline__ float bfr(float f) { return bf2f(f2bf(f)); }
__device__ __forceinline__ unsigned pack2(float a, float b) { return (unsigned)f2bf(a) | ((unsigned)f2bf(b) << 16); }
__device__ __forceinline__ float phi_f(float t) {
  float p = t > 0.f ? bfr(t + 1.0f) : bfr(__expf(t));
  return bfr(p + 1e-6f);
}
typedef __attribute__((ext_vector_type(2))) float f32x2;
typedef __attribute__((ext_vector_type(2))) __bf16 bf16x2;
__device__ __forceinline__ unsigned cvt2(f32x2 v) { return __builtin_bit_cast(unsigned, __builtin_convertvector(v, bf16x2)); }
__device__ __forceinline__ f32x2 unp2(unsigned u) { return (f32x2){__uint_as_float(u << 16), __uint_as_float(u & 0xffff0000u)}; }
__device__ __forceinline__ f32x2 bfr2(f32x2 v) { return unp2(cvt2(v)); }
__device__ __forceinline__ f32x2 lo2(f32x4 v) { return __builtin_shufflevector(v, v, 0, 1); }
__device__ __forceinline__ f32x2 hi2(f32x4 v) { return __builtin_shufflevector(v, v, 2, 3); }
__device__ __forceinline__ unsigned phi2(f32x2 a) {
  const f32x2 t = bfr2(a), m = t * 1.44269504088896340736f, p = t + 1.0f;
  f32x2 sel;
  sel[0] = t[0] > 0.f ? p[0] : __builtin_amdgcn_exp2f(m[0]);
  sel[1] = t[1] > 0.f ? p[1] : __builtin_amdgcn_exp2f(m[1]);
  return cvt2(bfr2(sel) + 1e-6f);
}
__device__ __forceinline__ f32x2 sigm2(f32x2 g) {
  const f32x2 m = g * -1.44269504088896340736f;
  f32x2 e; e[0] = __builtin_amdgcn_exp2f(m[0]); e[1] = __builtin_amdgcn_exp2f(m[1]);
  e = e + 1.0f;
  f32x2 r; r[0] = __builtin_amdgcn_rcpf(e[0]); r[1] = __builtin_amdgcn_rcpf(e[1]);
  return r;
}
__device__ __forceinline__ int aswz(int row) { int x = (row >> 2) & 3; return (((x ^ (x >> 1)) & 1) << 1) | (x >> 1); }

#define WAIT_V(n) asm volatile("s_waitcnt vmcnt(" #n ")" ::: "memory")
#define STAMP(kid, idx)
#define WAIT_L0 asm volatile("s_waitcnt lgkmcnt(0)" ::: "memory")
#define BAR __builtin_amdgcn_s_barrier()
#define SCHED __builtin_amdgcn_sched_barrier(0)
__device__ __forceinline__ float frcp(float v) { return __builtin_amdgcn_rcpf(v); }

constexpr int RT_ITEMS = 216 * 80 + 128 * 80;
__device__ __forceinline__ void retile_item(int t, const u16* __restrict__ wo, const u16* __restrict__ wdown, char* __restrict__ ws) {
    if (t < 216 * 80) {
      const int koct = t / 80, noct = t - koct * 80;
      const u16* src = wdown + (size_t)(koct * 8) * D_ + noct * 8;
      u32x4 r[8];
#pragma unroll
      for (int jj = 0; jj < 8; ++jj) r[jj] = *(const u32x4*)(src + (size_t)jj * D_);
      const int n0 = noct * 8, nt = n0 / 80, nn = n0 - nt * 80, nf = nn >> 4, c0 = nn & 15, kt = koct >> 2, gg = koct & 3;
      char* dst = ws + OFF_WC + ((size_t)((nt * 54 + kt) * 5 + nf) * 64 + gg * 16 + c0) * 16;
#pragma unroll
      for (int i = 0; i < 4; ++i) {
        u32x4 lo, hi;
#pragma unroll
        for (int q = 0; q < 4; ++q) {
          lo[q] = (r[2 * q][i] & 0xffffu) | (r[2 * q + 1][i] << 16);
          hi[q] = (r[2 * q][i] >> 16) | (r[2 * q + 1][i] & 0xffff0000u);
        }
        *(u32x4*)(dst + (2 * i) * 16) = lo;
        *(u32x4*)(dst + (2 * i + 1) * 16) = hi;
      }
    } else {
      const int t2 = t - 216 * 80, hg = t2 / 80, noct = t2 - hg * 80, hd = hg >> 2, gg = hg & 3;
      const u16* src = wo + (size_t)(hd * 20) * D_ + noct * 8;
      u32x4 r[4];
#pragma unroll
      for (int jj = 0; jj < 4; ++jj) r[jj] = *(const u32x4*)(src + (size_t)(4 * gg + jj) * D_);
      const u32x4 e = *(const u32x4*)(src + (size_t)(16 + gg) * D_);
      const int n0 = noct * 8, nt = n0 / 80, nn = n0 - nt * 80, nf = nn >> 4, c0 = nn & 15;
      char* dst = ws + OFF_WO + ((size_t)((nt * 32 + hd) * 5 + nf) * 64 + c0) * 16;
#pragma unroll
      for (int i = 0; i < 4; ++i) {
        u32x4 lo, hi;
        lo[0] = (r[0][i] & 0xffffu) | (r[1][i] << 16);
        lo[1] = (r[2][i] & 0xffffu) | (r[3][i] << 16);
        hi[0] = (r[0][i] >> 16) | (r[1][i] & 0xffff0000u);
        hi[1] = (r[2][i] >> 16) | (r[3][i] & 0xffff0000u);
        lo[2] = e[i] & 0xffffu; lo[3] = 0u;
        hi[2] = e[i] >> 16; hi[3] = 0u;
        *(u32x4*)(dst + (gg * 16 + 2 * i) * 16) = lo;
        *(u32x4*)(dst + (gg * 16 + 2 * i + 1) * 16) = hi;
      }
    }
}

__global__ __launch_bounds__(256) void k0_prep(const u16* __restrict__ x, char* __restrict__ ws) {
  const int tid = threadIdx.x;
  {
    if (blockIdx.x == 0 && tid == 0) *(unsigned*)(ws + OFF_CNT) = 0u;
    if (tid < 16) *(u32x4*)(ws + OFF_KVF + (blockIdx.x * 16 + tid) * 16) = (u32x4){0u, 0u, 0u, 0u};
    const int row = blockIdx.x * 8 + (tid >> 5);
    const int l32 = tid & 31;
    const u16* xr = x + (size_t)row * D_;
    float v[20];
    float sum = 0.f;
#pragma unroll
    for (int i = 0; i < 5; ++i) {
      u32x2 w = *(const u32x2*)(xr + (l32 + 32 * i) * 4);
      v[4 * i + 0] = __uint_as_float(w[0] << 16);
      v[4 * i + 1] = __uint_as_float(w[0] & 0xffff0000u);
      v[4 * i + 2] = __uint_as_float(w[1] << 16);
      v[4 * i + 3] = __uint_as_float(w[1] & 0xffff0000u);
      sum += v[4 * i] + v[4 * i + 1] + v[4 * i + 2] + v[4 * i + 3];
    }
#pragma unroll
    for (int o = 16; o >= 1; o >>= 1) sum += __shfl_xor(sum, o);
    const float mean = sum * (1.0f / D_);
    float sq = 0.f;
#pragma unroll
    for (int i = 0; i < 20; ++i) { float d = v[i] - mean; sq += d * d; }
#pragma unroll
    for (int o = 16; o >= 1; o >>= 1) sq += __shfl_xor(sq, o);
    const float mb = bfr(mean);
    const float vb = bfr(sq * (1.0f / D_));
    const float sd = bfr(sqrtf(bfr(vb + 1e-5f)));
    u16* xn = (u16*)(ws + OFF_XN);
    const int mt = row >> 7, r = row & 127, sw = aswz(r);
#pragma unroll
    for (int i = 0; i < 5; ++i) {
      const int k = (l32 + 32 * i) * 4;
      float o0 = bfr(v[4 * i + 0] - mb) / sd, o1 = bfr(v[4 * i + 1] - mb) / sd;
      float o2 = bfr(v[4 * i + 2] - mb) / sd, o3 = bfr(v[4 * i + 3] - mb) / sd;
      u32x2 w; w[0] = pack2(o0, o1); w[1] = pack2(o2, o3);
      const int kt = k >> 5, ch = (k >> 3) & 3, hf = (k >> 2) & 1;
      size_t off = ((size_t)(mt * 20 + kt) * 128 + r) * 32 + ((ch ^ sw) << 3) + (hf << 2);
      *(u32x2*)(xn + off) = w;
    }
  }
}

constexpr int K1_STAGE = 32768;
#define K1NS 4
constexpr int K1_NS = K1NS;

template <int TYPE>
__device__ __forceinline__ void k1_body(const u16* __restrict__ wA, const u16* __restrict__ wB, char* __restrict__ ws,
                                        int mt, int j, char* smem) {
  constexpr int NCB = TYPE == 0 ? 6 : 5;
  constexpr int NFR = TYPE == 0 ? 6 : 5;
  constexpr int LDW = TYPE == 0 ? F_ : NQKV_;
  const int tid = threadIdx.x, lane = tid & 63;
  const int wave = __builtin_amdgcn_readfirstlane(tid >> 6);
  const int wm = wave >> 2, wn = (TYPE == 2 && wave >= 4) ? ((wave + 2) & 3) : (wave & 3);
  const int g = lane >> 4, c = lane & 15;

  const char* asrc = ws + OFF_XN + (size_t)mt * 20 * 8192 + wave * 1024 + lane * 16;
  const u16* bsrc[3];
#pragma unroll
  for (int t = 0; t < 3; ++t) {
    int ii = wave + 8 * t; if (ii >= NCB * 4) ii = wave;
    const int cb = ii >> 2, krow = (ii & 3) * 8 + (lane >> 3), u = lane & 7;
    const int sk = ((krow >> 1) & 1) | (((krow >> 3) & 1) << 1);
    const int ch = (u >> 1) ^ sk;
    const int lc = cb * 64 + ch * 16 + (u & 1) * 8;
    const u16* p;
    if (TYPE == 0) p = (lc < 192) ? (wA + j * 192 + lc) : (wB + j * 192 + (lc - 192));
    else if (TYPE == 1) p = wA + j * 320 + lc;
    else p = (lc < 160) ? (wA + 640 + j * 160 + lc) : (wA + 1280 + j * 160 + (lc - 160));
    bsrc[t] = p + (size_t)krow * LDW;
  }
  const bool has3 = (NCB == 6) || (wave < 4);

  auto issue = [&](int kt) {
    char* sb = smem + (kt % K1_NS) * K1_STAGE;
    __builtin_amdgcn_global_load_lds((const unsigned*)(asrc + (size_t)kt * 8192), (unsigned*)(sb + wave * 1024), 16, 0, 0);
    __builtin_amdgcn_global_load_lds((const unsigned*)(bsrc[0] + (size_t)kt * 32 * LDW), (unsigned*)(sb + 8192 + wave * 1024), 16, 0, 0);
    __builtin_amdgcn_global_load_lds((const unsigned*)(bsrc[1] + (size_t)kt * 32 * LDW), (unsigned*)(sb + 8192 + (wave + 8) * 1024), 16, 0, 0);
    if (has3) __builtin_amdgcn_global_load_lds((const unsigned*)(bsrc[2] + (size_t)kt * 32 * LDW), (unsigned*)(sb + 8192 + (wave + 16) * 1024), 16, 0, 0);
  };

  const int xoff = (wm * 64 + c) * 64 + ((g ^ aswz(c)) << 4);
  int woff[NFR];
  {
    const int q = (lane & 15) >> 2, p = lane & 3;
    const int s = (q >> 1) | ((g & 1) << 1);
    const int lo = (8 * g + q) * 128 + (s << 5) + p * 8;
#pragma unroll
    for (int i = 0; i < NFR; ++i) {
      const int nf = TYPE == 0 ? (i < 3 ? 3 * wn + i : 12 + 3 * wn + (i - 3)) : 5 * wn + i;
      woff[i] = 8192 + (nf >> 2) * 4096 + (lo ^ ((nf & 3) << 5));
    }
  }

  f32x4 acc[4][NFR];
#pragma unroll
  for (int a = 0; a < 4; ++a)
#pragma unroll
    for (int b = 0; b < NFR; ++b) acc[a][b] = (f32x4){0.f, 0.f, 0.f, 0.f};

  auto readfr = [&](int kt, s16x8 (&xf)[4], s16x8 (&wf)[NFR]) {
    const char* sb = smem + (kt % K1_NS) * K1_STAGE;
#pragma unroll
    for (int b = 0; b < NFR; ++b) {
      s16x4 lo = __builtin_amdgcn_ds_read_tr16_b64_v4i16((lds_s16x4_ptr)(sb + woff[b]));
      s16x4 hi = __builtin_amdgcn_ds_read_tr16_b64_v4i16((lds_s16x4_ptr)(sb + woff[b] + 512));
      wf[b] = __builtin_shufflevector(lo, hi, 0, 1, 2, 3, 4, 5, 6, 7);
    }
#pragma unroll
    for (int a = 0; a < 4; ++a) xf[a] = *(const s16x8*)(sb + xoff + a * 1024);
  };
  auto mma = [&](const s16x8 (&xf)[4], const s16x8 (&wf)[NFR]) {
#pragma unroll
    for (int a = 0; a < 4; ++a)
#pragma unroll
      for (int b = 0; b < NFR; ++b) {
        if (TYPE == 2) acc[a][b] = __builtin_amdgcn_mfma_f32_16x16x32_bf16(xf[a], wf[b], acc[a][b], 0, 0, 0);
        else           acc[a][b] = __builtin_amdgcn_mfma_f32_16x16x32_bf16(wf[b], xf[a], acc[a][b], 0, 0, 0);
      }
  };
#define WAITN(N) asm volatile("s_waitcnt vmcnt(%0)" :: "n"(N) : "memory")
#define K1_WAIT(n) do { if (has3) WAITN(4 * (n)); else WAITN(3 * (n)); } while (0)

  s16x8 xA[4], wAf[NFR], xB[4], wBf[NFR];
#define SGB(m, n) __builtin_amdgcn_sched_group_barrier(m, n, 0)
  auto pattern_full = [&]() {
    SGB(0x10, 1); SGB(0x8, 1); SGB(0x10, 1); SGB(0x8, 1); SGB(0x10, 1); SGB(0x8, 1); SGB(0x10, 1); SGB(0x8, 1);
#pragma unroll
    for (int i = 0; i < 4 + 2 * NFR; ++i) { SGB(0x100, 1); SGB(0x8, 1); }
    SGB(0x8, 4);
  };
  auto pattern_tail = [&]() {
#pragma unroll
    for (int i = 0; i < 4 + 2 * NFR; ++i) { SGB(0x100, 1); SGB(0x8, 1); }
    SGB(0x8, 8);
  };
  STAMP(1, 0);
#pragma unroll
  for (int i = 0; i < K1_NS; ++i) issue(i);
  K1_WAIT(K1_NS - 1);
  BAR;
  STAMP(1, 1);
  readfr(0, xA, wAf);
  SCHED;
#pragma unroll
  for (int kt = 0; kt < 19; ++kt) {
    if (kt + K1_NS <= 20) K1_WAIT(K1_NS - 2); else K1_WAIT(18 - kt);
    WAIT_L0; BAR; SCHED;
    if (kt + K1_NS < 20) issue(kt + K1_NS);
    if (kt & 1) { readfr(kt + 1, xA, wAf); mma(xB, wBf); }
    else        { readfr(kt + 1, xB, wBf); mma(xA, wAf); }
    if (kt + K1_NS < 20) pattern_full(); else pattern_tail();
    SCHED;
  }
  mma(xB, wBf);

  WAIT_L0; BAR;
  STAMP(1, 2);
  if (TYPE == 0) {
#pragma unroll
    for (int a = 0; a < 4; ++a) {
#pragma unroll
      for (int b = 0; b < 3; ++b) {
        u32x2 w;
        w[0] = cvt2(bfr2(sigm2(bfr2(lo2(acc[a][b])))) * bfr2(lo2(acc[a][b + 3])));
        w[1] = cvt2(bfr2(sigm2(bfr2(hi2(acc[a][b])))) * bfr2(hi2(acc[a][b + 3])));
        const int f0 = wn * 48 + b * 16;
        const int ktl = f0 >> 5, gk = ((f0 >> 4) & 1) * 2 + (g >> 1);
        *(u32x2*)(smem + (((wm * 4 + a) * 6 + ktl) * 64 + gk * 16 + c) * 16 + (g & 1) * 8) = w;
      }
    }
    __syncthreads();
    char* H = ws + OFF_H + ((size_t)(mt * 8) * 54 + j * 6) * 1024;
#pragma unroll
    for (int i = 0; i < 6; ++i) {
      const int q = tid + 512 * i;
      const int m16 = q / 384, rest = q - m16 * 384;
      const u32x4 v = *(const u32x4*)(smem + q * 16);
      *(u32x4*)(H + (size_t)m16 * 54 * 1024 + rest * 16) = v;
    }
  } else if (TYPE == 1) {
#pragma unroll
    for (int a = 0; a < 4; ++a) {
#pragma unroll
      for (int b = 0; b < 5; ++b) {
        u32x2 w; w[0] = phi2(lo2(acc[a][b])); w[1] = phi2(hi2(acc[a][b]));
        const int n = wn * 80 + b * 16 + 4 * g, hl = n / 20, d0 = n - hl * 20;
        const int ro = d0 < 16 ? (d0 >> 3) * 256 + c * 16 + (d0 & 7) * 2 : 512 + c * 8;
        *(u32x2*)(smem + ((wm * 4 + a) * 16 + hl) * 640 + ro) = w;
      }
    }
    __syncthreads();
    char* PQ = ws + OFF_PQ + ((size_t)(mt * 8) * 32 + j * 16) * 640;
#pragma unroll
    for (int i = 0; i < 10; ++i) {
      const int q = tid + 512 * i;
      const int m16 = q / 640, rest = q - m16 * 640;
      const u32x4 v = *(const u32x4*)(smem + q * 16);
      *(u32x4*)(PQ + (size_t)m16 * 20480 + rest * 16) = v;
    }
  } else {
#pragma unroll
    for (int a = 0; a < 4; ++a)
#pragma unroll
      for (int b = 0; b < 5; ++b) {
        const int col = wn * 80 + b * 16 + c;
        u32x2 w;
        if (wn < 2) { w[0] = phi2(lo2(acc[a][b])); w[1] = phi2(hi2(acc[a][b])); }
        else        { w[0] = cvt2(lo2(acc[a][b])); w[1] = cvt2(hi2(acc[a][b])); }
        *(u32x2*)(smem + col * 272 + (wm * 64 + a * 16 + 4 * g) * 2) = w;
      }
    __syncthreads();
    const int r32 = lane & 31, hh = lane >> 5;
    f32x16 akv, az;
#pragma unroll
    for (int i = 0; i < 16; ++i) { akv[i] = 0.f; az[i] = 0.f; }
    s16x8 ones;
#pragma unroll
    for (int i = 0; i < 8; ++i) ones[i] = (short)0x3F80;
#pragma unroll
    for (int s = 0; s < 8; ++s) {
      s16x8 af = *(const s16x8*)(smem + (20 * wave + r32) * 272 + (16 * s + 8 * hh) * 2);
      s16x8 bfv = *(const s16x8*)(smem + (160 + 20 * wave + r32) * 272 + (16 * s + 8 * hh) * 2);
      akv = __builtin_amdgcn_mfma_f32_32x32x16_bf16(af, bfv, akv, 0, 0, 0);
      az = __builtin_amdgcn_mfma_f32_32x32x16_bf16(af, ones, az, 0, 0, 0);
    }
    const int head = j * 8 + wave;
    float* P = (float*)(ws + OFF_KVP) + (size_t)(mt * 32 + head) * 504;
    if (r32 <= 20) {
#pragma unroll
      for (int q = 0; q < 3; ++q) {
        const int d0 = 8 * q + 4 * hh;
        if (d0 < 20) {
          f32x4 val;
#pragma unroll
          for (int r = 0; r < 4; ++r) val[r] = (r32 == 20) ? az[4 * q + r] : akv[4 * q + r];
          *(f32x4*)(P + r32 * 24 + d0) = val;
        }
      }
    }
  }
}

__global__ __launch_bounds__(512, 2) void k1_gemm(const u16* __restrict__ wqkv, const u16* __restrict__ wg,
                                                   const u16* __restrict__ wu, const u16* __restrict__ wo,
                                                   const u16* __restrict__ wdown, char* __restrict__ ws) {
  extern __shared__ __attribute__((aligned(16))) char smem[];
  const int bid = blockIdx.x;
  if (bid >= 240) {
    for (int t = (bid - 240) * 512 + threadIdx.x; t < RT_ITEMS; t += 16 * 512) retile_item(t, wo, wdown, ws);
    return;
  }
  const int p = (bid & 7) * 30 + (bid >> 3);
  const int mh = p / 120, rem = p % 120, nt = rem >> 3, mt = mh * 8 + (rem & 7);
  if (nt < 4) k1_body<2>(wqkv, wqkv, ws, mt, nt, smem);
  else if (nt < 6) k1_body<1>(wqkv, wqkv, ws, mt, nt - 4, smem);
  else k1_body<0>(wg, wu, ws, mt, nt - 6, smem);
  STAMP(1, 3);
}

constexpr int K2_LDS = 163840;
#define K2DD 3
constexpr int K2_D = K2DD;

__global__ __launch_bounds__(512, 2) void k2_gemm(const u16* __restrict__ x, char* ws, u16* __restrict__ out) {
  extern __shared__ __attribute__((aligned(16))) char smem[];
  const int tid = threadIdx.x, lane = tid & 63;
  const int wave = __builtin_amdgcn_readfirstlane(tid >> 6);
  const int wq = wave & 3;
  const int g = lane >> 4, c = lane & 15;
  const int bid = blockIdx.x;
  const int mt = (bid & 7) * 4 + (bid >> 6), nt = (bid >> 3) & 7;

  STAMP(2, 0);
  gu32* cnt = (gu32*)(ws + OFF_CNT);
  if (wave == 4) {
    const int item = bid * 16 + lane;
    if (lane < 16 && item < 32 * 126) {
      const int head = item / 126, q = item - head * 126, e = q / 6, dq = q - e * 6;
      if (dq < 5) {
        const float* P = (const float*)(ws + OFF_KVP) + (size_t)head * 504 + q * 4;
        f32x4 pv[16];
#pragma unroll
        for (int m = 0; m < 16; ++m) pv[m] = *(const f32x4*)(P + (size_t)m * 32 * 504);
        f32x4 sv = pv[0];
#pragma unroll
        for (int m = 1; m < 16; ++m) sv += pv[m];
        const unsigned long long w = (unsigned long long)cvt2(lo2(sv)) | ((unsigned long long)cvt2(hi2(sv)) << 32);
        char* dst = (char*)ws + OFF_KVF + (size_t)head * 2048 + (dq >> 1) * 256 + (dq & 1) * 8;
        if (e < 16) __hip_atomic_store((gu64*)(dst + e * 16), w, RLX_AGENT);
        else if (e < 20) __hip_atomic_store((gu64*)(dst + 1024 + (e - 16) * 64), w, RLX_AGENT);
        else {
#pragma unroll
          for (int k = 0; k < 4; ++k) __hip_atomic_store((gu64*)(dst + 1024 + k * 64 + 16), w, RLX_AGENT);
        }
      }
    }
    asm volatile("s_waitcnt vmcnt(0)" ::: "memory");
    if (lane == 0) __hip_atomic_fetch_add(cnt, 1u, RLX_AGENT);
  }
  f32x4 acc[4][5];
#pragma unroll
  for (int a = 0; a < 4; ++a)
#pragma unroll
    for (int b = 0; b < 5; ++b) acc[a][b] = (f32x4){0.f, 0.f, 0.f, 0.f};
  const int b0 = wave < 4 ? 0 : 3, nb = wave < 4 ? 3 : 2;
  const int tok = mt * 64 + wq * 16 + c;

  if (wave < 4) {
    const char* hsrc = ws + OFF_H + ((size_t)(mt * 4) * 54 + wq) * 1024 + lane * 16;
    const char* wsrc = ws + OFF_WC + ((size_t)nt * 54 + wq) * 5120 + lane * 16;
    s16x8 xr[K2_D][4], wr[K2_D][5];
    auto ld = [&](int slot, int i) {
#pragma unroll
      for (int b = 0; b < 5; ++b) wr[slot][b] = *(const s16x8*)(wsrc + (size_t)(20 * i + b) * 1024);
#pragma unroll
      for (int a = 0; a < 4; ++a) xr[slot][a] = *(const s16x8*)(hsrc + (size_t)(a * 54 + 4 * i) * 1024);
    };
    auto mma1 = [&](int slot) {
#pragma unroll
      for (int a = 0; a < 4; ++a)
#pragma unroll
        for (int b = 0; b < 5; ++b) acc[a][b] = __builtin_amdgcn_mfma_f32_16x16x32_bf16(wr[slot][b], xr[slot][a], acc[a][b], 0, 0, 0);
    };
#pragma unroll
    for (int i = 0; i < K2_D; ++i) ld(i, i);
    SCHED;
#pragma unroll
    for (int i = 0; i < 13; ++i) {
      mma1(i % K2_D);
      if (i + K2_D < 13) ld(i % K2_D, i + K2_D);
      else if (i + K2_D == 13) { if (wave < 2) ld(i % K2_D, 13); }
      SCHED;
    }
    if (wave < 2) mma1(13 % K2_D);
    STAMP(2, 1);
  } else {
    const char* pqsrc = ws + OFF_PQ + ((size_t)(mt * 4) * 32 + wq * 8) * 640 + (g < 2 ? g * 256 + c * 16 : 512 + c * 8);
    const char* ksrc = ws + OFF_KVF + (size_t)(wq * 8) * 2048 + lane * 16;
    const char* wosrc = ws + OFF_WO + ((size_t)nt * 32 + wq * 8) * 5120 + lane * 16;
    u32x4 pq[2][4];
    s16x8 kvf[2][2], w2[2][5];
    auto ldK = [&](int sl, int hh) {
#pragma unroll
      for (int fr = 0; fr < 2; ++fr) {
        const unsigned long long lo = __hip_atomic_load((gu64*)(ksrc + hh * 2048 + fr * 1024), RLX_AGENT);
        const unsigned long long hi = __hip_atomic_load((gu64*)(ksrc + hh * 2048 + fr * 1024 + 8), RLX_AGENT);
        u32x4 t; t[0] = (unsigned)lo; t[1] = (unsigned)(lo >> 32); t[2] = (unsigned)hi; t[3] = (unsigned)(hi >> 32);
        kvf[sl][fr] = __builtin_bit_cast(s16x8, t);
      }
    };
    auto ldA = [&](int sl, int hh) {
#pragma unroll
      for (int tg = 0; tg < 4; ++tg) pq[sl][tg] = *(const u32x4*)(pqsrc + (tg * 32 + hh) * 640);
    };
    auto ldW = [&](int sl, int hh) {
#pragma unroll
      for (int b = 0; b < 5; ++b) w2[sl][b] = *(const s16x8*)(wosrc + (hh * 5 + b) * 1024);
    };
    ldA(0, 0); ldW(0, 0); ldA(1, 1); ldW(1, 1);
    while (__hip_atomic_load(cnt, RLX_AGENT) < 256u) __builtin_amdgcn_s_sleep(4);
    asm volatile("" ::: "memory");
    ldK(0, 0); ldK(1, 1);
    const f32x4 z4 = (f32x4){0.f, 0.f, 0.f, 0.f};
#pragma unroll
    for (int hh = 0; hh < 8; ++hh) {
      const int sl = hh & 1;
      f32x4 d0[4], d1[4];
#pragma unroll
      for (int tg = 0; tg < 4; ++tg) {
        u32x4 pk4;
        pk4[0] = g < 3 ? pq[sl][tg][0] : 0u; pk4[1] = g < 3 ? pq[sl][tg][1] : 0u;
        pk4[2] = g < 2 ? pq[sl][tg][2] : 0u; pk4[3] = g < 2 ? pq[sl][tg][3] : 0u;
        const s16x8 pf = __builtin_bit_cast(s16x8, pk4);
        d1[tg] = __builtin_amdgcn_mfma_f32_16x16x32_bf16(kvf[sl][1], pf, z4, 0, 0, 0);
        d0[tg] = __builtin_amdgcn_mfma_f32_16x16x32_bf16(kvf[sl][0], pf, z4, 0, 0, 0);
      }
      if (hh + 2 < 8) { ldK(sl, hh + 2); ldA(sl, hh + 2); }
      s16x8 of[4];
#pragma unroll
      for (int tg = 0; tg < 4; ++tg) {
        const float rden = frcp(bfr(bfr(d1[tg][1]) + 1e-6f));
        u32x4 ow;
        ow[0] = cvt2(bfr2(lo2(d0[tg])) * rden); ow[1] = cvt2(bfr2(hi2(d0[tg])) * rden);
        ow[2] = (unsigned)f2bf(bfr(d1[tg][0]) * rden); ow[3] = 0u;
        of[tg] = __builtin_bit_cast(s16x8, ow);
      }
#pragma unroll
      for (int a = 0; a < 4; ++a)
#pragma unroll
        for (int b = 0; b < 5; ++b) acc[a][b] = __builtin_amdgcn_mfma_f32_16x16x32_bf16(w2[sl][b], of[a], acc[a][b], 0, 0, 0);
      if (hh + 2 < 8) ldW(sl, hh + 2);
      SCHED;
    }
  }

  u32x2 xw[3];
#pragma unroll
  for (int i = 0; i < 3; ++i) xw[i] = *(const u32x2*)(x + (size_t)tok * D_ + nt * 80 + (i < nb ? b0 + i : b0) * 16 + 4 * g);

  f32x4* red = (f32x4*)smem;
#pragma unroll
  for (int a = 0; a < 4; ++a)
#pragma unroll
    for (int b = 0; b < 5; ++b) red[(wave * 20 + a * 5 + b) * 64 + lane] = acc[a][b];
  __syncthreads();
#pragma unroll
  for (int i = 0; i < 3; ++i) {
    if (i < nb) {
      const int f = wq * 5 + b0 + i;
      f32x4 y1 = red[(0 * 20 + f) * 64 + lane], y2 = red[(4 * 20 + f) * 64 + lane];
#pragma unroll
      for (int sw = 1; sw < 4; ++sw) { y1 += red[(sw * 20 + f) * 64 + lane]; y2 += red[((4 + sw) * 20 + f) * 64 + lane]; }
      const int n = nt * 80 + (b0 + i) * 16 + 4 * g;
      u32x2 w;
      {
        const f32x2 y = bfr2(lo2(y1)), gs = bfr2(y * sigm2(y)), sv = bfr2(lo2(y2));
        w[0] = cvt2(unp2(xw[i][0]) + bfr2(sv * gs));
      }
      {
        const f32x2 y = bfr2(hi2(y1)), gs = bfr2(y * sigm2(y)), sv = bfr2(hi2(y2));
        w[1] = cvt2(unp2(xw[i][1]) + bfr2(sv * gs));
      }
      *(u32x2*)(out + (size_t)tok * D_ + n) = w;
    }
  }
  STAMP(2, 3);
}

extern "C" void kernel_launch(void* const* d_in, const int* in_sizes, int n_in,
                              void* d_out, int out_size, void* d_ws, size_t ws_size,
                              hipStream_t stream) {
  const u16* x = (const u16*)d_in[0];
  const u16* wqkv = (const u16*)d_in[1];
  const u16* wo = (const u16*)d_in[2];
  const u16* wg = (const u16*)d_in[4];
  const u16* wu = (const u16*)d_in[5];
  const u16* wd = (const u16*)d_in[6];
  char* ws = (char*)d_ws;
  u16* out = (u16*)d_out;

  hipFuncSetAttribute((const void*)k1_gemm, hipFuncAttributeMaxDynamicSharedMemorySize, K1_NS * K1_STAGE);
  hipFuncSetAttribute((const void*)k2_gemm, hipFuncAttributeMaxDynamicSharedMemorySize, K2_LDS);

  k0_prep<<<256, 256, 0, stream>>>(x, ws);
  k1_gemm<<<256, 512, K1_NS * K1_STAGE, stream>>>(wqkv, wg, wu, wo, wd, ws);
  k2_gemm<<<256, 512, K2_LDS, stream>>>(x, ws, out);
}
```

```cpp
#include <hip/hip_runtime.h>
#include <hip/hip_bf16.h>
#include <stdint.h>

typedef unsigned short u16;
typedef __attribute__((ext_vector_type(8))) short s16x8;
typedef __attribute__((ext_vector_type(4))) short s16x4;
typedef __attribute__((ext_vector_type(4))) float f32x4;
typedef __attribute__((ext_vector_type(16))) float f32x16;
typedef __attribute__((ext_vector_type(2))) unsigned u32x2;
typedef __attribute__((ext_vector_type(4))) unsigned u32x4;
typedef __attribute__((address_space(3))) s16x4* lds_s16x4_ptr;
typedef __attribute__((address_space(1))) unsigned gu32;
typedef __attribute__((address_space(1))) unsigned long long gu64;

#define RLX_AGENT __ATOMIC_RELAXED, __HIP_MEMORY_SCOPE_AGENT

constexpr int S_ = 2048, D_ = 640, F_ = 1728, NQKV_ = 1920;

constexpr size_t OFF_CNT   = 0;
constexpr size_t OFF_XN    = 8192;
constexpr size_t SZ_XN     = (size_t)S_ * D_ * 2;
constexpr size_t OFF_H     = OFF_XN + SZ_XN;
constexpr size_t SZ_H      = (size_t)S_ * F_ * 2;
constexpr size_t OFF_PQ    = OFF_H + SZ_H;
constexpr size_t SZ_PQ     = (size_t)S_ * D_ * 2;
constexpr size_t OFF_WC    = OFF_PQ + SZ_PQ;
constexpr size_t SZ_WC     = (size_t)8 * 54 * 5 * 1024;
constexpr size_t OFF_WO    = OFF_WC + SZ_WC;
constexpr size_t SZ_WO     = (size_t)8 * 32 * 5 * 1024;
constexpr size_t OFF_KVP   = OFF_WO + SZ_WO;
constexpr size_t SZ_KVP    = (size_t)16 * 32 * 504 * 4;
constexpr size_t OFF_KVF   = OFF_KVP + SZ_KVP;
constexpr size_t SZ_KVF    = 65536;

__device__ __forceinline__ float bf2f(u16 v) { return __uint_as_float(((unsigned)v) << 16); }
__device__ __forceinline__ u16 f2bf(float f) { return __builtin_bit_cast(u16, (__bf16)f); }
__device__ __forceinline__ float bfr(float f) { return bf2f(f2bf(f)); }
__device__ __forceinline__ unsigned pack2(float a, float b) { return (unsigned)f2bf(a) | ((unsigned)f2bf(b) << 16); }
__device__ __forceinline__ float phi_f(float t) {
  float p = t > 0.f ? bfr(t + 1.0f) : bfr(__expf(t));
  return bfr(p + 1e-6f);
}
typedef __attribute__((ext_vector_type(2))) float f32x2;
typedef __attribute__((ext_vector_type(2))) __bf16 bf16x2;
__device__ __forceinline__ unsigned cvt2(f32x2 v) { return __builtin_bit_cast(unsigned, __builtin_convertvector(v, bf16x2)); }
__device__ __forceinline__ f32x2 unp2(unsigned u) { return (f32x2){__uint_as_float(u << 16), __uint_as_float(u & 0xffff0000u)}; }
__device__ __forceinline__ f32x2 bfr2(f32x2 v) { return unp2(cvt2(v)); }
__device__ __forceinline__ f32x2 lo2(f32x4 v) { return __builtin_shufflevector(v, v, 0, 1); }
__device__ __forceinline__ f32x2 hi2(f32x4 v) { return __builtin_shufflevector(v, v, 2, 3); }
__device__ __forceinline__ unsigned phi2(f32x2 a) {
  const f32x2 t = bfr2(a), m = t * 1.44269504088896340736f, p = t + 1.0f;
  f32x2 sel;
  sel[0] = t[0] > 0.f ? p[0] : __builtin_amdgcn_exp2f(m[0]);
  sel[1] = t[1] > 0.f ? p[1] : __builtin_amdgcn_exp2f(m[1]);
  return cvt2(bfr2(sel) + 1e-6f);
}
__device__ __forceinline__ f32x2 sigm2(f32x2 g) {
  const f32x2 m = g * -1.44269504088896340736f;
  f32x2 e; e[0] = __builtin_amdgcn_exp2f(m[0]); e[1] = __builtin_amdgcn_exp2f(m[1]);
  e = e + 1.0f;
  f32x2 r; r[0] = __builtin_amdgcn_rcpf(e[0]); r[1] = __builtin_amdgcn_rcpf(e[1]);
  return r;
}
__device__ __forceinline__ int aswz(int row) { int x = (row >> 2) & 3; return (((x ^ (x >> 1)) & 1) << 1) | (x >> 1); }

#define WAIT_V(n) asm volatile("s_waitcnt vmcnt(" #n ")" ::: "memory")
#define STAMP(kid, idx)
#define WAIT_L0 asm volatile("s_waitcnt lgkmcnt(0)" ::: "memory")
#define BAR __builtin_amdgcn_s_barrier()
#define SCHED __builtin_amdgcn_sched_barrier(0)
__device__ __forceinline__ float frcp(float v) { return __builtin_amdgcn_rcpf(v); }

constexpr int RT_ITEMS = 216 * 80 + 128 * 80;
__device__ __forceinline__ void retile_item(int t, const u16* __restrict__ wo, const u16* __restrict__ wdown, char* __restrict__ ws) {
    if (t < 216 * 80) {
      const int koct = t / 80, noct = t - koct * 80;
      const u16* src = wdown + (size_t)(koct * 8) * D_ + noct * 8;
      u32x4 r[8];
#pragma unroll
      for (int jj = 0; jj < 8; ++jj) r[jj] = *(const u32x4*)(src + (size_t)jj * D_);
      const int n0 = noct * 8, nt = n0 / 80, nn = n0 - nt * 80, nf = nn >> 4, c0 = nn & 15, kt = koct >> 2, gg = koct & 3;
      char* dst = ws + OFF_WC + ((size_t)((nt * 54 + kt) * 5 + nf) * 64 + gg * 16 + c0) * 16;
#pragma unroll
      for (int i = 0; i < 4; ++i) {
        u32x4 lo, hi;
#pragma unroll
        for (int q = 0; q < 4; ++q) {
          lo[q] = (r[2 * q][i] & 0xffffu) | (r[2 * q + 1][i] << 16);
          hi[q] = (r[2 * q][i] >> 16) | (r[2 * q + 1][i] & 0xffff0000u);
        }
        *(u32x4*)(dst + (2 * i) * 16) = lo;
        *(u32x4*)(dst + (2 * i + 1) * 16) = hi;
      }
    } else {
      const int t2 = t - 216 * 80, hg = t2 / 80, noct = t2 - hg * 80, hd = hg >> 2, gg = hg & 3;
      const u16* src = wo + (size_t)(hd * 20) * D_ + noct * 8;
      u32x4 r[4];
#pragma unroll
      for (int jj = 0; jj < 4; ++jj) r[jj] = *(const u32x4*)(src + (size_t)(4 * gg + jj) * D_);
      const u32x4 e = *(const u32x4*)(src + (size_t)(16 + gg) * D_);
      const int n0 = noct * 8, nt = n0 / 80, nn = n0 - nt * 80, nf = nn >> 4, c0 = nn & 15;
      char* dst = ws + OFF_WO + ((size_t)((nt * 32 + hd) * 5 + nf) * 64 + c0) * 16;
#pragma unroll
      for (int i = 0; i < 4; ++i) {
        u32x4 lo, hi;
        lo[0] = (r[0][i] & 0xffffu) | (r[1][i] << 16);
        lo[1] = (r[2][i] & 0xffffu) | (r[3][i] << 16);
        hi[0] = (r[0][i] >> 16) | (r[1][i] & 0xffff0000u);
        hi[1] = (r[2][i] >> 16) | (r[3][i] & 0xffff0000u);
        lo[2] = e[i] & 0xffffu; lo[3] = 0u;
        hi[2] = e[i] >> 16; hi[3] = 0u;
        *(u32x4*)(dst + (gg * 16 + 2 * i) * 16) = lo;
        *(u32x4*)(dst + (gg * 16 + 2 * i + 1) * 16) = hi;
      }
    }
}

__global__ __launch_bounds__(256) void k0_prep(const u16* __restrict__ x, char* __restrict__ ws) {
  const int tid = threadIdx.x;
  {
    if (blockIdx.x == 0 && tid == 0) *(unsigned*)(ws + OFF_CNT) = 0u;
    const int row = blockIdx.x * 8 + (tid >> 5);
    const int l32 = tid & 31;
    const u16* xr = x + (size_t)row * D_;
    float v[20];
    float sum = 0.f;
#pragma unroll
    for (int i = 0; i < 5; ++i) {
      u32x2 w = *(const u32x2*)(xr + (l32 + 32 * i) * 4);
      v[4 * i + 0] = __uint_as_float(w[0] << 16);
      v[4 * i + 1] = __uint_as_float(w[0] & 0xffff0000u);
      v[4 * i + 2] = __uint_as_float(w[1] << 16);
      v[4 * i + 3] = __uint_as_float(w[1] & 0xffff0000u);
      sum += v[4 * i] + v[4 * i + 1] + v[4 * i + 2] + v[4 * i + 3];
    }
#pragma unroll
    for (int o = 16; o >= 1; o >>= 1) sum += __shfl_xor(sum, o);
    const float mean = sum * (1.0f / D_);
    float sq = 0.f;
#pragma unroll
    for (int i = 0; i < 20; ++i) { float d = v[i] - mean; sq += d * d; }
#pragma unroll
    for (int o = 16; o >= 1; o >>= 1) sq += __shfl_xor(sq, o);
    const float mb = bfr(mean);
    const float vb = bfr(sq * (1.0f / D_));
    const float sd = bfr(sqrtf(bfr(vb + 1e-5f)));
    u16* xn = (u16*)(ws + OFF_XN);
    const int mt = row >> 7, r = row & 127, sw = aswz(r);
#pragma unroll
    for (int i = 0; i < 5; ++i) {
      const int k = (l32 + 32 * i) * 4;
      float o0 = bfr(v[4 * i + 0] - mb) / sd, o1 = bfr(v[4 * i + 1] - mb) / sd;
      float o2 = bfr(v[4 * i + 2] - mb) / sd, o3 = bfr(v[4 * i + 3] - mb) / sd;
      u32x2 w; w[0] = pack2(o0, o1); w[1] = pack2(o2, o3);
      const int kt = k >> 5, ch = (k >> 3) & 3, hf = (k >> 2) & 1;
      size_t off = ((size_t)(mt * 20 + kt) * 128 + r) * 32 + ((ch ^ sw) << 3) + (hf << 2);
      *(u32x2*)(xn + off) = w;
    }
  }
}

constexpr int K1_STAGE = 32768;
#define K1NS 4
constexpr int K1_NS = K1NS;

template <int TYPE>
__device__ __forceinline__ void k1_body(const u16* __restrict__ wA, const u16* __restrict__ wB, char* __restrict__ ws,
                                        int mt, int j, char* smem) {
  constexpr int NCB = TYPE == 0 ? 6 : 5;
  constexpr int NFR = TYPE == 0 ? 6 : 5;
  constexpr int LDW = TYPE == 0 ? F_ : NQKV_;
  const int tid = threadIdx.x, lane = tid & 63;
  const int wave = __builtin_amdgcn_readfirstlane(tid >> 6);
  const int wm = wave >> 2, wn = (TYPE == 2 && wave >= 4) ? ((wave + 2) & 3) : (wave & 3);
  const int g = lane >> 4, c = lane & 15;

  const char* asrc = ws + OFF_XN + (size_t)mt * 20 * 8192 + wave * 1024 + lane * 16;
  const u16* bsrc[3];
#pragma unroll
  for (int t = 0; t < 3; ++t) {
    int ii = wave + 8 * t; if (ii >= NCB * 4) ii = wave;
    const int cb = ii >> 2, krow = (ii & 3) * 8 + (lane >> 3), u = lane & 7;
    const int sk = ((krow >> 1) & 1) | (((krow >> 3) & 1) << 1);
    const int ch = (u >> 1) ^ sk;
    const int lc = cb * 64 + ch * 16 + (u & 1) * 8;
    const u16* p;
    if (TYPE == 0) p = (lc < 192) ? (wA + j * 192 + lc) : (wB + j * 192 + (lc - 192));
    else if (TYPE == 1) p = wA + j * 320 + lc;
    else p = (lc < 160) ? (wA + 640 + j * 160 + lc) : (wA + 1280 + j * 160 + (lc - 160));
    bsrc[t] = p + (size_t)krow * LDW;
  }
  const bool has3 = (NCB == 6) || (wave < 4);

  auto issue = [&](int kt) {
    char* sb = smem + (kt % K1_NS) * K1_STAGE;
    __builtin_amdgcn_global_load_lds((const unsigned*)(asrc + (size_t)kt * 8192), (unsigned*)(sb + wave * 1024), 16, 0, 0);
    __builtin_amdgcn_global_load_lds((const unsigned*)(bsrc[0] + (size_t)kt * 32 * LDW), (unsigned*)(sb + 8192 + wave * 1024), 16, 0, 0);
    __builtin_amdgcn_global_load_lds((const unsigned*)(bsrc[1] + (size_t)kt * 32 * LDW), (unsigned*)(sb + 8192 + (wave + 8) * 1024), 16, 0, 0);
    if (has3) __builtin_amdgcn_global_load_lds((const unsigned*)(bsrc[2] + (size_t)kt * 32 * LDW), (unsigned*)(sb + 8192 + (wave + 16) * 1024), 16, 0, 0);
  };

  const int xoff = (wm * 64 + c) * 64 + ((g ^ aswz(c)) << 4);
  int woff[NFR];
  {
    const int q = (lane & 15) >> 2, p = lane & 3;
    const int s = (q >> 1) | ((g & 1) << 1);
    const int lo = (8 * g + q) * 128 + (s << 5) + p * 8;
#pragma unroll
    for (int i = 0; i < NFR; ++i) {
      const int nf = TYPE == 0 ? (i < 3 ? 3 * wn + i : 12 + 3 * wn + (i - 3)) : 5 * wn + i;
      woff[i] = 8192 + (nf >> 2) * 4096 + (lo ^ ((nf & 3) << 5));
    }
  }

  f32x4 acc[4][NFR];
#pragma unroll
  for (int a = 0; a < 4; ++a)
#pragma unroll
    for (int b = 0; b < NFR; ++b) acc[a][b] = (f32x4){0.f, 0.f, 0.f, 0.f};

  auto readfr = [&](int kt, s16x8 (&xf)[4], s16x8 (&wf)[NFR]) {
    const char* sb = smem + (kt % K1_NS) * K1_STAGE;
#pragma unroll
    for (int b = 0; b < NFR; ++b) {
      s16x4 lo = __builtin_amdgcn_ds_read_tr16_b64_v4i16((lds_s16x4_ptr)(sb + woff[b]));
      s16x4 hi = __builtin_amdgcn_ds_read_tr16_b64_v4i16((lds_s16x4_ptr)(sb + woff[b] + 512));
      wf[b] = __builtin_shufflevector(lo, hi, 0, 1, 2, 3, 4, 5, 6, 7);
    }
#pragma unroll
    for (int a = 0; a < 4; ++a) xf[a] = *(const s16x8*)(sb + xoff + a * 1024);
  };
  auto mma = [&](const s16x8 (&xf)[4], const s16x8 (&wf)[NFR]) {
#pragma unroll
    for (int a = 0; a < 4; ++a)
#pragma unroll
      for (int b = 0; b < NFR; ++b) {
        if (TYPE == 2) acc[a][b] = __builtin_amdgcn_mfma_f32_16x16x32_bf16(xf[a], wf[b], acc[a][b], 0, 0, 0);
        else           acc[a][b] = __builtin_amdgcn_mfma_f32_16x16x32_bf16(wf[b], xf[a], acc[a][b], 0, 0, 0);
      }
  };
#define WAITN(N) asm volatile("s_waitcnt vmcnt(%0)" :: "n"(N) : "memory")
#define K1_WAIT(n) do { if (has3) WAITN(4 * (n)); else WAITN(3 * (n)); } while (0)

  s16x8 xA[4], wAf[NFR], xB[4], wBf[NFR];
#define SGB(m, n) __builtin_amdgcn_sched_group_barrier(m, n, 0)
  auto pattern_full = [&]() {
    SGB(0x10, 1); SGB(0x8, 1); SGB(0x10, 1); SGB(0x8, 1); SGB(0x10, 1); SGB(0x8, 1); SGB(0x10, 1); SGB(0x8, 1);
#pragma unroll
    for (int i = 0; i < 4 + 2 * NFR; ++i) { SGB(0x100, 1); SGB(0x8, 1); }
    SGB(0x8, 4);
  };
  auto pattern_tail = [&]() {
#pragma unroll
    for (int i = 0; i < 4 + 2 * NFR; ++i) { SGB(0x100, 1); SGB(0x8, 1); }
    SGB(0x8, 8);
  };
  STAMP(1, 0);
#pragma unroll
  for (int i = 0; i < K1_NS; ++i) issue(i);
  K1_WAIT(K1_NS - 1);
  BAR;
  STAMP(1, 1);
  readfr(0, xA, wAf);
  SCHED;
#pragma unroll
  for (int kt = 0; kt < 19; ++kt) {
    if (kt + K1_NS <= 20) K1_WAIT(K1_NS - 2); else K1_WAIT(18 - kt);
    WAIT_L0; BAR; SCHED;
    if (kt + K1_NS < 20) issue(kt + K1_NS);
    if (kt & 1) { readfr(kt + 1, xA, wAf); mma(xB, wBf); }
    else        { readfr(kt + 1, xB, wBf); mma(xA, wAf); }
    if (kt + K1_NS < 20) pattern_full(); else pattern_tail();
    SCHED;
  }
  mma(xB, wBf);

  WAIT_L0; BAR;
  STAMP(1, 2);
  if (TYPE == 0) {
#pragma unroll
    for (int a = 0; a < 4; ++a) {
#pragma unroll
      for (int b = 0; b < 3; ++b) {
        u32x2 w;
        w[0] = cvt2(bfr2(sigm2(bfr2(lo2(acc[a][b])))) * bfr2(lo2(acc[a][b + 3])));
        w[1] = cvt2(bfr2(sigm2(bfr2(hi2(acc[a][b])))) * bfr2(hi2(acc[a][b + 3])));
        const int f0 = wn * 48 + b * 16;
        const int ktl = f0 >> 5, gk = ((f0 >> 4) & 1) * 2 + (g >> 1);
        *(u32x2*)(smem + (((wm * 4 + a) * 6 + ktl) * 64 + gk * 16 + c) * 16 + (g & 1) * 8) = w;
      }
    }
    __syncthreads();
    char* H = ws + OFF_H + ((size_t)(mt * 8) * 54 + j * 6) * 1024;
#pragma unroll
    for (int i = 0; i < 6; ++i) {
      const int q = tid + 512 * i;
      const int m16 = q / 384, rest = q - m16 * 384;
      const u32x4 v = *(const u32x4*)(smem + q * 16);
      *(u32x4*)(H + (size_t)m16 * 54 * 1024 + rest * 16) = v;
    }
  } else if (TYPE == 1) {
#pragma unroll
    for (int a = 0; a < 4; ++a) {
#pragma unroll
      for (int b = 0; b < 5; ++b) {
        u32x2 w; w[0] = phi2(lo2(acc[a][b])); w[1] = phi2(hi2(acc[a][b]));
        const int n = wn * 80 + b * 16 + 4 * g, hl = n / 20, d0 = n - hl * 20;
        const int ro = d0 < 16 ? (d0 >> 3) * 256 + c * 16 + (d0 & 7) * 2 : 512 + c * 8;
        *(u32x2*)(smem + ((wm * 4 + a) * 16 + hl) * 640 + ro) = w;
      }
    }
    __syncthreads();
    char* PQ = ws + OFF_PQ + ((size_t)(mt * 8) * 32 + j * 16) * 640;
#pragma unroll
    for (int i = 0; i < 10; ++i) {
      const int q = tid + 512 * i;
      const int m16 = q / 640, rest = q - m16 * 640;
      const u32x4 v = *(const u32x4*)(smem + q * 16);
      *(u32x4*)(PQ + (size_t)m16 * 20480 + rest * 16) = v;
    }
  } else {
#pragma unroll
    for (int a = 0; a < 4; ++a)
#pragma unroll
      for (int b = 0; b < 5; ++b) {
        const int col = wn * 80 + b * 16 + c;
        u32x2 w;
        if (wn < 2) { w[0] = phi2(lo2(acc[a][b])); w[1] = phi2(hi2(acc[a][b])); }
        else        { w[0] = cvt2(lo2(acc[a][b])); w[1] = cvt2(hi2(acc[a][b])); }
        *(u32x2*)(smem + col * 272 + (wm * 64 + a * 16 + 4 * g) * 2) = w;
      }
    __syncthreads();
    const int r32 = lane & 31, hh = lane >> 5;
    f32x16 akv, az;
#pragma unroll
    for (int i = 0; i < 16; ++i) { akv[i] = 0.f; az[i] = 0.f; }
    s16x8 ones;
#pragma unroll
    for (int i = 0; i < 8; ++i) ones[i] = (short)0x3F80;
#pragma unroll
    for (int s = 0; s < 8; ++s) {
      s16x8 af = *(const s16x8*)(smem + (20 * wave + r32) * 272 + (16 * s + 8 * hh) * 2);
      s16x8 bfv = *(const s16x8*)(smem + (160 + 20 * wave + r32) * 272 + (16 * s + 8 * hh) * 2);
      akv = __builtin_amdgcn_mfma_f32_32x32x16_bf16(af, bfv, akv, 0, 0, 0);
      az = __builtin_amdgcn_mfma_f32_32x32x16_bf16(af, ones, az, 0, 0, 0);
    }
    const int head = j * 8 + wave;
    float* P = (float*)(ws + OFF_KVP) + (size_t)(mt * 32 + head) * 504;
    if (r32 <= 20) {
#pragma unroll
      for (int q = 0; q < 3; ++q) {
        const int d0 = 8 * q + 4 * hh;
        if (d0 < 20) {
          f32x4 val;
#pragma unroll
          for (int r = 0; r < 4; ++r) val[r] = (r32 == 20) ? az[4 * q + r] : akv[4 * q + r];
          *(f32x4*)(P + r32 * 24 + d0) = val;
        }
      }
    }
  }
}

__global__ __launch_bounds__(512, 2) void k1_gemm(const u16* __restrict__ wqkv, const u16* __restrict__ wg,
                                                   const u16* __restrict__ wu, const u16* __restrict__ wo,
                                                   const u16* __restrict__ wdown, char* __restrict__ ws) {
  extern __shared__ __attribute__((aligned(16))) char smem[];
  const int bid = blockIdx.x;
  if (bid >= 240) {
    for (int t = (bid - 240) * 512 + threadIdx.x; t < RT_ITEMS; t += 16 * 512) retile_item(t, wo, wdown, ws);
    return;
  }
  const int p = (bid & 7) * 30 + (bid >> 3);
  const int mh = p / 120, rem = p % 120, nt = rem >> 3, mt = mh * 8 + (rem & 7);
  if (nt < 4) k1_body<2>(wqkv, wqkv, ws, mt, nt, smem);
  else if (nt < 6) k1_body<1>(wqkv, wqkv, ws, mt, nt - 4, smem);
  else k1_body<0>(wg, wu, ws, mt, nt - 6, smem);
  STAMP(1, 3);
}

constexpr int K2_LDS = 163840;
#define K2DD 3
constexpr int K2_D = K2DD;

__global__ __launch_bounds__(512, 2) void k2_gemm(const u16* __restrict__ x, char* ws, u16* __restrict__ out) {
  extern __shared__ __attribute__((aligned(16))) char smem[];
  const int tid = threadIdx.x, lane = tid & 63;
  const int wave = __builtin_amdgcn_readfirstlane(tid >> 6);
  const int wq = wave & 3;
  const int g = lane >> 4, c = lane & 15;
  const int bid = blockIdx.x;
  const int mt = (bid & 7) * 4 + (bid >> 6), nt = (bid >> 3) & 7;

  STAMP(2, 0);
  gu32* cnt = (gu32*)(ws + OFF_CNT);
  volatile int* lflag = (volatile int*)(smem + K2_LDS - 1024);
  if (tid == 0) *lflag = 0;
  __syncthreads();
  if (wave == 4 && bid < 32) {
    const float* Pf = (const float*)(ws + OFF_KVP) + (size_t)bid * 504;
    const int l0 = lane * 4, l1 = (lane < 62 ? lane + 64 : lane) * 4;
    f32x4 a0[16], a1[16];
#pragma unroll
    for (int m = 0; m < 16; ++m) {
      a0[m] = *(const f32x4*)(Pf + (size_t)m * 32 * 504 + l0);
      a1[m] = *(const f32x4*)(Pf + (size_t)m * 32 * 504 + l1);
    }
    f32x4 s0 = a0[0], s1 = a1[0];
#pragma unroll
    for (int m = 1; m < 16; ++m) { s0 += a0[m]; s1 += a1[m]; }
    float* kvs = (float*)(smem + 4 * 20480 + 16384);
    *(f32x4*)(kvs + l0) = s0;
    if (lane < 62) *(f32x4*)(kvs + l1) = s1;
    WAIT_L0;
#pragma unroll
    for (int fr = 0; fr < 2; ++fr) {
      const int e = fr == 0 ? c : ((c & 3) == 0 ? 16 + (c >> 2) : ((c & 3) == 1 ? 20 : 99)), d0 = 8 * g;
      const bool act = (e <= 20 && d0 < 20), act1 = act && (d0 < 16);
      const float* src = kvs + (act ? e * 24 + d0 : 0);
      f32x4 v0 = *(const f32x4*)src, v1 = *(const f32x4*)(src + (act1 ? 4 : 0));
      u32x4 w;
      w[0] = act ? cvt2(lo2(v0)) : 0u; w[1] = act ? cvt2(hi2(v0)) : 0u;
      w[2] = act1 ? cvt2(lo2(v1)) : 0u; w[3] = act1 ? cvt2(hi2(v1)) : 0u;
      asm volatile("global_store_dwordx4 %0, %1, off sc1" :: "v"(ws + OFF_KVF + ((size_t)(bid * 2 + fr) * 64 + lane) * 16), "v"(w) : "memory");
    }
    asm volatile("s_waitcnt vmcnt(0)" ::: "memory");
    if (lane == 0) __hip_atomic_fetch_add(cnt, 1u, RLX_AGENT);
  }
  f32x4 acc[4][5];
#pragma unroll
  for (int a = 0; a < 4; ++a)
#pragma unroll
    for (int b = 0; b < 5; ++b) acc[a][b] = (f32x4){0.f, 0.f, 0.f, 0.f};
  const int b0 = wave < 4 ? 0 : 3, nb = wave < 4 ? 3 : 2;
  const int tok = mt * 64 + wq * 16 + c;

  if (wave < 4) {
    const char* hsrc = ws + OFF_H + ((size_t)(mt * 4) * 54 + wq) * 1024 + lane * 16;
    const char* wsrc = ws + OFF_WC + ((size_t)nt * 54 + wq) * 5120 + lane * 16;
    s16x8 xr[K2_D][4], wr[K2_D][5];
    auto ld = [&](int slot, int i) {
#pragma unroll
      for (int b = 0; b < 5; ++b) wr[slot][b] = *(const s16x8*)(wsrc + (size_t)(20 * i + b) * 1024);
#pragma unroll
      for (int a = 0; a < 4; ++a) xr[slot][a] = *(const s16x8*)(hsrc + (size_t)(a * 54 + 4 * i) * 1024);
    };
    auto mma1 = [&](int slot) {
#pragma unroll
      for (int a = 0; a < 4; ++a)
#pragma unroll
        for (int b = 0; b < 5; ++b) acc[a][b] = __builtin_amdgcn_mfma_f32_16x16x32_bf16(wr[slot][b], xr[slot][a], acc[a][b], 0, 0, 0);
    };
#pragma unroll
    for (int i = 0; i < K2_D; ++i) ld(i, i);
    SCHED;
#pragma unroll
    for (int i = 0; i < 13; ++i) {
      mma1(i % K2_D);
      if (i + K2_D < 13) ld(i % K2_D, i + K2_D);
      else if (i + K2_D == 13) { if (wave < 2) ld(i % K2_D, 13); }
      SCHED;
    }
    if (wave < 2) mma1(13 % K2_D);
    STAMP(2, 1);
  } else {
    const char* pqsrc = ws + OFF_PQ + ((size_t)(mt * 4) * 32 + wq * 8) * 640 + (g < 2 ? g * 256 + c * 16 : 512 + c * 8);
    const char* ksrc = ws + OFF_KVF + (size_t)(wq * 8) * 2048 + lane * 16;
    const char* wosrc = ws + OFF_WO + ((size_t)nt * 32 + wq * 8) * 5120 + lane * 16;
    u32x4 pq[2][4];
    s16x8 kvf[2][2], w2[2][5];
    char* kl = smem + wave * 20480;
    auto ldK = [&](int sl, int hh) {
#pragma unroll
      for (int fr = 0; fr < 2; ++fr) kvf[sl][fr] = *(const s16x8*)(kl + hh * 2048 + fr * 1024 + lane * 16);
    };
    auto ldA = [&](int sl, int hh) {
#pragma unroll
      for (int tg = 0; tg < 4; ++tg) pq[sl][tg] = *(const u32x4*)(pqsrc + (tg * 32 + hh) * 640);
    };
    auto ldW = [&](int sl, int hh) {
#pragma unroll
      for (int b = 0; b < 5; ++b) w2[sl][b] = *(const s16x8*)(wosrc + (hh * 5 + b) * 1024);
    };
    ldA(0, 0); ldW(0, 0); ldA(1, 1); ldW(1, 1);
    {
      if (wave == 4) {
        while (__hip_atomic_load(cnt, RLX_AGENT) < 32u) __builtin_amdgcn_s_sleep(16);
        if (lane == 0) *lflag = 1;
      } else {
        while (*lflag != 1) __builtin_amdgcn_s_sleep(8);
      }
    }
    asm volatile("" ::: "memory");
#pragma unroll
    for (int i = 0; i < 16; ++i)
      __builtin_amdgcn_global_load_lds((const unsigned*)(ksrc + i * 1024), (unsigned*)(kl + i * 1024), 16, 0, 16);
    asm volatile("s_waitcnt vmcnt(0)" ::: "memory");
    ldK(0, 0); ldK(1, 1);
    const f32x4 z4 = (f32x4){0.f, 0.f, 0.f, 0.f};
#pragma unroll
    for (int hh = 0; hh < 8; ++hh) {
      const int sl = hh & 1;
      f32x4 d0[4], d1[4];
#pragma unroll
      for (int tg = 0; tg < 4; ++tg) {
        u32x4 pk4;
        pk4[0] = g < 3 ? pq[sl][tg][0] : 0u; pk4[1] = g < 3 ? pq[sl][tg][1] : 0u;
        pk4[2] = g < 2 ? pq[sl][tg][2] : 0u; pk4[3] = g < 2 ? pq[sl][tg][3] : 0u;
        const s16x8 pf = __builtin_bit_cast(s16x8, pk4);
        d1[tg] = __builtin_amdgcn_mfma_f32_16x16x32_bf16(kvf[sl][1], pf, z4, 0, 0, 0);
        d0[tg] = __builtin_amdgcn_mfma_f32_16x16x32_bf16(kvf[sl][0], pf, z4, 0, 0, 0);
      }
      if (hh + 2 < 8) { ldK(sl, hh + 2); ldA(sl, hh + 2); }
      s16x8 of[4];
#pragma unroll
      for (int tg = 0; tg < 4; ++tg) {
        const float rden = frcp(bfr(bfr(d1[tg][1]) + 1e-6f));
        u32x4 ow;
        ow[0] = cvt2(bfr2(lo2(d0[tg])) * rden); ow[1] = cvt2(bfr2(hi2(d0[tg])) * rden);
        ow[2] = (unsigned)f2bf(bfr(d1[tg][0]) * rden); ow[3] = 0u;
        of[tg] = __builtin_bit_cast(s16x8, ow);
      }
#pragma unroll
      for (int a = 0; a < 4; ++a)
#pragma unroll
        for (int b = 0; b < 5; ++b) acc[a][b] = __builtin_amdgcn_mfma_f32_16x16x32_bf16(w2[sl][b], of[a], acc[a][b], 0, 0, 0);
      if (hh + 2 < 8) ldW(sl, hh + 2);
      SCHED;
    }
  }

  u32x2 xw[3];
#pragma unroll
  for (int i = 0; i < 3; ++i) xw[i] = *(const u32x2*)(x + (size_t)tok * D_ + nt * 80 + (i < nb ? b0 + i : b0) * 16 + 4 * g);

  f32x4* red = (f32x4*)smem;
  f32x4 own[3];
#pragma unroll
  for (int a = 0; a < 4; ++a)
#pragma unroll
    for (int b = 0; b < 5; ++b) {
      const bool mine = (a == wq) && (b >= b0) && (b < b0 + nb);
      if (mine) own[b < 3 ? b : b - 3] = acc[a][b];
      else red[(wave * 20 + a * 5 + b) * 64 + lane] = acc[a][b];
    }
  __syncthreads();
#pragma unroll
  for (int i = 0; i < 3; ++i) {
    if (i < nb) {
      const int f = wq * 5 + b0 + i;
      f32x4 p[8];
#pragma unroll
      for (int sw = 0; sw < 8; ++sw) p[sw] = sw == wave ? own[i] : red[(sw * 20 + f) * 64 + lane];
      const f32x4 y1 = ((p[0] + p[1]) + p[2]) + p[3], y2 = ((p[4] + p[5]) + p[6]) + p[7];
      const int n = nt * 80 + (b0 + i) * 16 + 4 * g;
      u32x2 w;
      {
        const f32x2 y = bfr2(lo2(y1)), gs = bfr2(y * sigm2(y)), sv = bfr2(lo2(y2));
        w[0] = cvt2(unp2(xw[i][0]) + bfr2(sv * gs));
      }
      {
        const f32x2 y = bfr2(hi2(y1)), gs = bfr2(y * sigm2(y)), sv = bfr2(hi2(y2));
        w[1] = cvt2(unp2(xw[i][1]) + bfr2(sv * gs));
      }
      *(u32x2*)(out + (size_t)tok * D_ + n) = w;
    }
  }
  STAMP(2, 3);
}

extern "C" void kernel_launch(void* const* d_in, const int* in_sizes, int n_in,
                              void* d_out, int out_size, void* d_ws, size_t ws_size,
                              hipStream_t stream) {
  const u16* x = (const u16*)d_in[0];
  const u16* wqkv = (const u16*)d_in[1];
  const u16* wo = (const u16*)d_in[2];
  const u16* wg = (const u16*)d_in[4];
  const u16* wu = (const u16*)d_in[5];
  const u16* wd = (const u16*)d_in[6];
  char* ws = (char*)d_ws;
  u16* out = (u16*)d_out;

  hipFuncSetAttribute((const void*)k1_gemm, hipFuncAttributeMaxDynamicSharedMemorySize, K1_NS * K1_STAGE);
  hipFuncSetAttribute((const void*)k2_gemm, hipFuncAttributeMaxDynamicSharedMemorySize, K2_LDS);

  k0_prep<<<256, 256, 0, stream>>>(x, ws);
  k1_gemm<<<256, 512, K1_NS * K1_STAGE, stream>>>(wqkv, wg, wu, wo, wd, ws);
  k2_gemm<<<256, 512, K2_LDS, stream>>>(x, ws, out);
}
```

```cpp
#include <hip/hip_runtime.h>
#include <hip/hip_bf16.h>
#include <stdint.h>

typedef unsigned short u16;
typedef __attribute__((ext_vector_type(8))) short s16x8;
typedef __attribute__((ext_vector_type(4))) short s16x4;
typedef __attribute__((ext_vector_type(4))) float f32x4;
typedef __attribute__((ext_vector_type(16))) float f32x16;
typedef __attribute__((ext_vector_type(2))) unsigned u32x2;
typedef __attribute__((ext_vector_type(4))) unsigned u32x4;
typedef __attribute__((address_space(3))) s16x4* lds_s16x4_ptr;
typedef __attribute__((address_space(1))) unsigned gu32;
typedef __attribute__((address_space(1))) unsigned long long gu64;

#define RLX_AGENT __ATOMIC_RELAXED, __HIP_MEMORY_SCOPE_AGENT

constexpr int S_ = 2048, D_ = 640, F_ = 1728, NQKV_ = 1920;

constexpr size_t OFF_CNT   = 0;
constexpr size_t OFF_XN    = 8192;
constexpr size_t SZ_XN     = (size_t)S_ * D_ * 2;
constexpr size_t OFF_H     = OFF_XN + SZ_XN;
constexpr size_t SZ_H      = (size_t)S_ * F_ * 2;
constexpr size_t OFF_PQ    = OFF_H + SZ_H;
constexpr size_t SZ_PQ     = (size_t)S_ * D_ * 2;
constexpr size_t OFF_WC    = OFF_PQ + SZ_PQ;
constexpr size_t SZ_WC     = (size_t)8 * 54 * 5 * 1024;
constexpr size_t OFF_WO    = OFF_WC + SZ_WC;
constexpr size_t SZ_WO     = (size_t)8 * 32 * 5 * 1024;
constexpr size_t OFF_KVP   = OFF_WO + SZ_WO;
constexpr size_t SZ_KVP    = (size_t)16 * 32 * 504 * 4;
constexpr size_t OFF_KVF   = OFF_KVP + SZ_KVP;
constexpr size_t SZ_KVF    = 65536;

__device__ __forceinline__ float bf2f(u16 v) { return __uint_as_float(((unsigned)v) << 16); }
__device__ __forceinline__ u16 f2bf(float f) { return __builtin_bit_cast(u16, (__bf16)f); }
__device__ __forceinline__ float bfr(float f) { return bf2f(f2bf(f)); }
__device__ __forceinline__ unsigned pack2(float a, float b) { return (unsigned)f2bf(a) | ((unsigned)f2bf(b) << 16); }
__device__ __forceinline__ float phi_f(float t) {
  float p = t > 0.f ? bfr(t + 1.0f) : bfr(__expf(t));
  return bfr(p + 1e-6f);
}
typedef __attribute__((ext_vector_type(2))) float f32x2;
typedef __attribute__((ext_vector_type(2))) __bf16 bf16x2;
__device__ __forceinline__ unsigned cvt2(f32x2 v) { return __builtin_bit_cast(unsigned, __builtin_convertvector(v, bf16x2)); }
__device__ __forceinline__ f32x2 unp2(unsigned u) { return (f32x2){__uint_as_float(u << 16), __uint_as_float(u & 0xffff0000u)}; }
__device__ __forceinline__ f32x2 bfr2(f32x2 v) { return unp2(cvt2(v)); }
__device__ __forceinline__ f32x2 lo2(f32x4 v) { return __builtin_shufflevector(v, v, 0, 1); }
__device__ __forceinline__ f32x2 hi2(f32x4 v) { return __builtin_shufflevector(v, v, 2, 3); }
__device__ __forceinline__ unsigned phi2(f32x2 a) {
  const f32x2 t = bfr2(a), m = t * 1.44269504088896340736f, p = t + 1.0f;
  f32x2 sel;
  sel[0] = t[0] > 0.f ? p[0] : __builtin_amdgcn_exp2f(m[0]);
  sel[1] = t[1] > 0.f ? p[1] : __builtin_amdgcn_exp2f(m[1]);
  return cvt2(bfr2(sel) + 1e-6f);
}
__device__ __forceinline__ f32x2 sigm2(f32x2 g) {
  const f32x2 m = g * -1.44269504088896340736f;
  f32x2 e; e[0] = __builtin_amdgcn_exp2f(m[0]); e[1] = __builtin_amdgcn_exp2f(m[1]);
  e = e + 1.0f;
  f32x2 r; r[0] = __builtin_amdgcn_rcpf(e[0]); r[1] = __builtin_amdgcn_rcpf(e[1]);
  return r;
}
__device__ __forceinline__ int aswz(int row) { int x = (row >> 2) & 3; return (((x ^ (x >> 1)) & 1) << 1) | (x >> 1); }

#define WAIT_V(n) asm volatile("s_waitcnt vmcnt(" #n ")" ::: "memory")
#define STAMP(kid, idx)
#define WAIT_L0 asm volatile("s_waitcnt lgkmcnt(0)" ::: "memory")
#define BAR __builtin_amdgcn_s_barrier()
#define SCHED __builtin_amdgcn_sched_barrier(0)
__device__ __forceinline__ float frcp(float v) { return __builtin_amdgcn_rcpf(v); }

constexpr int RT_ITEMS = 216 * 80 + 128 * 80;
__device__ __forceinline__ void retile_load(int t, const u16* __restrict__ wo, const u16* __restrict__ wdown, u32x4 (&r)[8]) {
  const bool isA = t < 216 * 80;
  const int t2 = isA ? 0 : t - 216 * 80, hg = t2 / 80, noctB = t2 - hg * 80, hd = hg >> 2, gg = hg & 3;
  const int koct = t / 80, noctA = t - koct * 80;
  const u16* src = isA ? wdown + (size_t)(koct * 8) * D_ + noctA * 8 : wo + (size_t)(hd * 20) * D_ + noctB * 8;
#pragma unroll
  for (int jj = 0; jj < 8; ++jj) {
    const int row = isA ? jj : (jj < 4 ? 4 * gg + jj : 16 + gg);
    r[jj] = *(const u32x4*)(src + (size_t)row * D_);
  }
}
__device__ __forceinline__ void retile_store(int t, char* __restrict__ ws, const u32x4 (&r)[8]) {
  if (t < 216 * 80) {
    const int koct = t / 80, noct = t - koct * 80;
    const int n0 = noct * 8, nt = n0 / 80, nn = n0 - nt * 80, nf = nn >> 4, c0 = nn & 15, kt = koct >> 2, gg = koct & 3;
    char* dst = ws + OFF_WC + ((size_t)((nt * 54 + kt) * 5 + nf) * 64 + gg * 16 + c0) * 16;
#pragma unroll
    for (int i = 0; i < 4; ++i) {
      u32x4 lo, hi;
#pragma unroll
      for (int q = 0; q < 4; ++q) {
        lo[q] = (r[2 * q][i] & 0xffffu) | (r[2 * q + 1][i] << 16);
        hi[q] = (r[2 * q][i] >> 16) | (r[2 * q + 1][i] & 0xffff0000u);
      }
      *(u32x4*)(dst + (2 * i) * 16) = lo;
      *(u32x4*)(dst + (2 * i + 1) * 16) = hi;
    }
  } else {
    const int t2 = t - 216 * 80, hg = t2 / 80, noct = t2 - hg * 80, hd = hg >> 2, gg = hg & 3;
    const int n0 = noct * 8, nt = n0 / 80, nn = n0 - nt * 80, nf = nn >> 4, c0 = nn & 15;
    char* dst = ws + OFF_WO + ((size_t)((nt * 32 + hd) * 5 + nf) * 64 + c0) * 16;
#pragma unroll
    for (int i = 0; i < 4; ++i) {
      u32x4 lo, hi;
      lo[0] = (r[0][i] & 0xffffu) | (r[1][i] << 16);
      lo[1] = (r[2][i] & 0xffffu) | (r[3][i] << 16);
      hi[0] = (r[0][i] >> 16) | (r[1][i] & 0xffff0000u);
      hi[1] = (r[2][i] >> 16) | (r[3][i] & 0xffff0000u);
      lo[2] = r[4][i] & 0xffffu; lo[3] = 0u;
      hi[2] = r[4][i] >> 16; hi[3] = 0u;
      *(u32x4*)(dst + (gg * 16 + 2 * i) * 16) = lo;
      *(u32x4*)(dst + (gg * 16 + 2 * i + 1) * 16) = hi;
    }
  }
}

constexpr int K1_STAGE = 32768;
#define K1NS 4
constexpr int K1_NS = K1NS;

constexpr int k1_step(int t, int nB) { return (t + 5 <= 19 ? 1 : 0) + (t + 4 <= 19 ? nB : 0); }
constexpr int k1_allowed(int kt, int nB) {
  return kt == 0 ? 2 * nB : kt == 1 ? nB + k1_step(0, nB) : kt == 2 ? k1_step(0, nB) + k1_step(1, nB)
       : k1_step(kt - 2, nB) + k1_step(kt - 1, nB);
}

template <int TYPE, int NFR>
__device__ __forceinline__ void k1_epilogue(f32x4 (&acc)[4][NFR], char* __restrict__ ws, int mt, int j, char* smem,
                                            int tid, int wm, int wn, int wave);

template <int TYPE>
__device__ __forceinline__ void k1_body(const u16* __restrict__ x, const u16* __restrict__ wA, const u16* __restrict__ wB,
                                        char* __restrict__ ws, int mt, int j, char* smem,
                                        const u16* __restrict__ wo, const u16* __restrict__ wdown) {
  constexpr int NCB = TYPE == 0 ? 6 : 5;
  constexpr int NFR = TYPE == 0 ? 6 : 5;
  constexpr int LDW = TYPE == 0 ? F_ : NQKV_;
  const int tid = threadIdx.x, lane = tid & 63;
  const int wave = __builtin_amdgcn_readfirstlane(tid >> 6);
  const int wm = wave >> 2, wn = (TYPE == 2 && wave >= 4) ? ((wave + 2) & 3) : (wave & 3);
  const int g = lane >> 4, c = lane & 15;

  const u16* bbase[3];
  unsigned boff[3];
#pragma unroll
  for (int t = 0; t < 3; ++t) {
    int ii = wave + 8 * t; if (ii >= NCB * 4) ii = wave;
    const int cb = ii >> 2, krow = (ii & 3) * 8 + (lane >> 3), u = lane & 7;
    const int sk = ((krow >> 1) & 1) | (((krow >> 3) & 1) << 1);
    const int ch = (u >> 1) ^ sk;
    const int lc = cb * 64 + ch * 16 + (u & 1) * 8;
    int col;
    if (TYPE == 0) { bbase[t] = cb < 3 ? wA : wB; col = j * 192 + (cb < 3 ? lc : lc - 192); }
    else if (TYPE == 1) { bbase[t] = wA; col = j * 320 + lc; }
    else { bbase[t] = wA; col = (lc < 160) ? (640 + j * 160 + lc) : (1280 + j * 160 + (lc - 160)); }
    boff[t] = (unsigned)(krow * LDW + col);
  }
  const bool has3 = (NCB == 6) || (wave < 4);

  auto issue = [&](int kt) {
    char* sb = smem + (kt % K1_NS) * K1_STAGE;
    __builtin_amdgcn_global_load_lds((const unsigned*)(bbase[0] + (size_t)kt * 32 * LDW + boff[0]), (unsigned*)(sb + 8192 + wave * 1024), 16, 0, 0);
    __builtin_amdgcn_global_load_lds((const unsigned*)(bbase[1] + (size_t)kt * 32 * LDW + boff[1]), (unsigned*)(sb + 8192 + (wave + 8) * 1024), 16, 0, 0);
    if (has3) __builtin_amdgcn_global_load_lds((const unsigned*)(bbase[2] + (size_t)kt * 32 * LDW + boff[2]), (unsigned*)(sb + 8192 + (wave + 16) * 1024), 16, 0, 0);
  };

  const int xoff = (wm * 64 + c) * 64 + ((g ^ aswz(c)) << 4);
  int woff[NFR];
  {
    const int q = (lane & 15) >> 2, p = lane & 3;
    const int s = (q >> 1) | ((g & 1) << 1);
    const int lo = (8 * g + q) * 128 + (s << 5) + p * 8;
#pragma unroll
    for (int i = 0; i < NFR; ++i) {
      const int nf = TYPE == 0 ? (i < 3 ? 3 * wn + i : 12 + 3 * wn + (i - 3)) : 5 * wn + i;
      woff[i] = (TYPE == 0 && i >= 3) ? woff[i - 3] + 12288 : 8192 + (nf >> 2) * 4096 + (lo ^ ((nf & 3) << 5));
    }
  }

  f32x4 acc[4][NFR];

  auto readfr = [&](int kt, s16x8 (&xf)[4], s16x8 (&wf)[NFR]) {
    const char* sb = smem + (kt % K1_NS) * K1_STAGE;
#pragma unroll
    for (int b = 0; b < NFR; ++b) {
      s16x4 lo = __builtin_amdgcn_ds_read_tr16_b64_v4i16((lds_s16x4_ptr)(sb + woff[b]));
      s16x4 hi = __builtin_amdgcn_ds_read_tr16_b64_v4i16((lds_s16x4_ptr)(sb + woff[b] + 512));
      wf[b] = __builtin_shufflevector(lo, hi, 0, 1, 2, 3, 4, 5, 6, 7);
    }
#pragma unroll
    for (int a = 0; a < 4; ++a) xf[a] = *(const s16x8*)(sb + xoff + a * 1024);
  };
  auto mma = [&](const s16x8 (&xf)[4], const s16x8 (&wf)[NFR]) {
#pragma unroll
    for (int a = 0; a < 4; ++a)
#pragma unroll
      for (int b = 0; b < NFR; ++b) {
        if (TYPE == 2) acc[a][b] = __builtin_amdgcn_mfma_f32_16x16x32_bf16(xf[a], wf[b], acc[a][b], 0, 0, 0);
        else           acc[a][b] = __builtin_amdgcn_mfma_f32_16x16x32_bf16(wf[b], xf[a], acc[a][b], 0, 0, 0);
      }
  };
#define WAITN(N) asm volatile("s_waitcnt vmcnt(%0)" :: "n"(N) : "memory")
#define K1_WAITK(kt) do { if (has3) WAITN(k1_allowed(kt, 3)); else WAITN(k1_allowed(kt, 2)); } while (0)

  s16x8 xA[4], wAf[NFR], xB[4], wBf[NFR];
#define SGB(m, n) __builtin_amdgcn_sched_group_barrier(m, n, 0)
  auto pattern_full = [&]() {
    SGB(0x10, 1); SGB(0x8, 1); SGB(0x10, 1); SGB(0x8, 1); SGB(0x10, 1); SGB(0x8, 1); SGB(0x10, 1); SGB(0x8, 1);
#pragma unroll
    for (int i = 0; i < 4 + 2 * NFR; ++i) { SGB(0x100, 1); SGB(0x8, 1); }
    SGB(0x8, 4);
  };
  auto pattern_tail = [&]() {
#pragma unroll
    for (int i = 0; i < 4 + 2 * NFR; ++i) { SGB(0x100, 1); SGB(0x8, 1); }
    SGB(0x8, 8);
  };
  STAMP(1, 0);
  const int rt = blockIdx.x * 120 + wave * 15 + (lane < 15 ? lane : 14);
  const bool rt_on = lane < 15 && rt < RT_ITEMS;
  const int ar = tid >> 2, aq = tid & 3;
  const u16* xrow = x + (size_t)(mt * 128 + ar) * D_ + aq * 8;
  const int aoff = ar * 64 + ((aq ^ aswz(ar)) << 4);
  u32x4 xq[3];
  float mb, rs;
  auto norm_store = [&](int st, const u32x4 v) {
    u32x4 o;
#pragma unroll
    for (int i = 0; i < 4; ++i) {
      const f32x2 q = bfr2(unp2(v[i]) - mb) * rs;
      o[i] = cvt2(q);
    }
    *(u32x4*)(smem + (st % K1_NS) * K1_STAGE + aoff) = o;
  };
  {
    u32x4 xc[20];
#pragma unroll
    for (int kt = 0; kt < 20; ++kt) xc[kt] = *(const u32x4*)(xrow + kt * 32);
    u32x4 rtr[8];
    retile_load(rt < RT_ITEMS ? rt : RT_ITEMS - 1, wo, wdown, rtr);
#pragma unroll
    for (int i = 0; i < K1_NS; ++i) issue(i);
    f32x2 s2 = (f32x2){0.f, 0.f};
#pragma unroll
    for (int kt = 0; kt < 20; ++kt)
#pragma unroll
      for (int i = 0; i < 4; ++i) s2 += unp2(xc[kt][i]);
    float sum = s2[0] + s2[1];
    sum += __shfl_xor(sum, 1); sum += __shfl_xor(sum, 2);
    const float mean = sum * (1.0f / D_);
#pragma unroll
    for (int kt = 0; kt < 20; ++kt) asm volatile("" : "+v"(xc[kt]));
    f32x2 q2 = (f32x2){0.f, 0.f};
#pragma unroll
    for (int kt = 0; kt < 20; ++kt)
#pragma unroll
      for (int i = 0; i < 4; ++i) { const f32x2 d = unp2(xc[kt][i]) - mean; q2 = __builtin_elementwise_fma(d, d, q2); }
    float sq = q2[0] + q2[1];
    sq += __shfl_xor(sq, 1); sq += __shfl_xor(sq, 2);
#pragma unroll
    for (int kt = 0; kt < 5; ++kt) asm volatile("" : "+v"(xc[kt]));
    mb = bfr(mean);
    rs = 1.0f / bfr(sqrtf(bfr(bfr(sq * (1.0f / D_)) + 1e-5f)));
    norm_store(0, xc[0]); norm_store(1, xc[1]); norm_store(2, xc[2]);
    xq[0] = xc[3]; xq[1] = xc[4];
    if (rt_on) retile_store(rt, ws, rtr);
  }
  if (has3) WAITN(9); else WAITN(6);
  WAIT_L0;
  BAR;
  STAMP(1, 1);
#pragma unroll
  for (int a = 0; a < 4; ++a)
#pragma unroll
    for (int b = 0; b < NFR; ++b) acc[a][b] = (f32x4){0.f, 0.f, 0.f, 0.f};
  readfr(0, xA, wAf);
  SCHED;
#pragma unroll
  for (int kt = 0; kt < 19; ++kt) {
    K1_WAITK(kt);
    WAIT_L0; BAR; SCHED;
    if (kt + 5 < 20) xq[(kt + 5) % 3] = *(const u32x4*)(xrow + (kt + 5) * 32);
    if (kt + K1_NS < 20) issue(kt + K1_NS);
    if (kt + 3 < 20) norm_store(kt + 3, xq[(kt + 3) % 3]);
    if (kt & 1) { readfr(kt + 1, xA, wAf); mma(xB, wBf); }
    else        { readfr(kt + 1, xB, wBf); mma(xA, wAf); }
    if (kt + K1_NS < 20) pattern_full(); else pattern_tail();
    SCHED;
  }
  mma(xB, wBf);

  WAIT_L0; BAR;
  STAMP(1, 2);
  int tid_e = threadIdx.x;
  asm volatile("" : "+v"(tid_e));
  k1_epilogue<TYPE, NFR>(acc, ws, mt, j, smem, tid_e, wm, wn, wave);
}

template <int TYPE, int NFR>
__device__ __forceinline__ void k1_epilogue(f32x4 (&acc)[4][NFR], char* __restrict__ ws, int mt, int j, char* smem,
                                            int tid, int wm, int wn, int wave) {
  const int lane = tid & 63, g = lane >> 4, c = lane & 15;
  if (TYPE == 0) {
#pragma unroll
    for (int a = 0; a < 4; ++a) {
#pragma unroll
      for (int b = 0; b < 3; ++b) {
        u32x2 w;
        w[0] = cvt2(bfr2(sigm2(bfr2(lo2(acc[a][b])))) * bfr2(lo2(acc[a][b + 3])));
        w[1] = cvt2(bfr2(sigm2(bfr2(hi2(acc[a][b])))) * bfr2(hi2(acc[a][b + 3])));
        const int f0 = wn * 48 + b * 16;
        const int ktl = f0 >> 5, gk = ((f0 >> 4) & 1) * 2 + (g >> 1);
        *(u32x2*)(smem + (((wm * 4 + a) * 6 + ktl) * 64 + gk * 16 + c) * 16 + (g & 1) * 8) = w;
      }
    }
    __syncthreads();
    char* H = ws + OFF_H + ((size_t)(mt * 8) * 54 + j * 6) * 1024;
#pragma unroll
    for (int i = 0; i < 6; ++i) {
      const int q = tid + 512 * i;
      const int m16 = q / 384, rest = q - m16 * 384;
      const u32x4 v = *(const u32x4*)(smem + q * 16);
      *(u32x4*)(H + (size_t)m16 * 54 * 1024 + rest * 16) = v;
    }
  } else if (TYPE == 1) {
#pragma unroll
    for (int a = 0; a < 4; ++a) {
#pragma unroll
      for (int b = 0; b < 5; ++b) {
        u32x2 w; w[0] = phi2(lo2(acc[a][b])); w[1] = phi2(hi2(acc[a][b]));
        const int n = wn * 80 + b * 16 + 4 * g, hl = n / 20, d0 = n - hl * 20;
        const int ro = d0 < 16 ? (d0 >> 3) * 256 + c * 16 + (d0 & 7) * 2 : 512 + c * 8;
        *(u32x2*)(smem + ((wm * 4 + a) * 16 + hl) * 640 + ro) = w;
      }
    }
    __syncthreads();
    char* PQ = ws + OFF_PQ + ((size_t)(mt * 8) * 32 + j * 16) * 640;
#pragma unroll
    for (int i = 0; i < 10; ++i) {
      const int q = tid + 512 * i;
      const int m16 = q / 640, rest = q - m16 * 640;
      const u32x4 v = *(const u32x4*)(smem + q * 16);
      *(u32x4*)(PQ + (size_t)m16 * 20480 + rest * 16) = v;
    }
  } else {
#pragma unroll
    for (int a = 0; a < 4; ++a)
#pragma unroll
      for (int b = 0; b < 5; ++b) {
        const int col = wn * 80 + b * 16 + c;
        u32x2 w;
        if (wn < 2) { w[0] = phi2(lo2(acc[a][b])); w[1] = phi2(hi2(acc[a][b])); }
        else        { w[0] = cvt2(lo2(acc[a][b])); w[1] = cvt2(hi2(acc[a][b])); }
        *(u32x2*)(smem + col * 272 + (wm * 64 + a * 16 + 4 * g) * 2) = w;
      }
    __syncthreads();
    const int r32 = lane & 31, hh = lane >> 5;
    f32x16 akv, az;
#pragma unroll
    for (int i = 0; i < 16; ++i) { akv[i] = 0.f; az[i] = 0.f; }
    s16x8 ones;
#pragma unroll
    for (int i = 0; i < 8; ++i) ones[i] = (short)0x3F80;
#pragma unroll
    for (int s = 0; s < 8; ++s) {
      s16x8 af = *(const s16x8*)(smem + (20 * wave + r32) * 272 + (16 * s + 8 * hh) * 2);
      s16x8 bfv = *(const s16x8*)(smem + (160 + 20 * wave + r32) * 272 + (16 * s + 8 * hh) * 2);
      akv = __builtin_amdgcn_mfma_f32_32x32x16_bf16(af, bfv, akv, 0, 0, 0);
      az = __builtin_amdgcn_mfma_f32_32x32x16_bf16(af, ones, az, 0, 0, 0);
    }
    const int head = j * 8 + wave;
    float* P = (float*)(ws + OFF_KVP) + (size_t)(mt * 32 + head) * 504;
    if (r32 <= 20) {
#pragma unroll
      for (int q = 0; q < 3; ++q) {
        const int d0 = 8 * q + 4 * hh;
        if (d0 < 20) {
          f32x4 val;
#pragma unroll
          for (int r = 0; r < 4; ++r) val[r] = (r32 == 20) ? az[4 * q + r] : akv[4 * q + r];
          *(f32x4*)(P + r32 * 24 + d0) = val;
        }
      }
    }
  }
}

__global__ __launch_bounds__(512, 2) void k1_gemm(const u16* __restrict__ x, const u16* __restrict__ wqkv, const u16* __restrict__ wg,
                                                   const u16* __restrict__ wu, const u16* __restrict__ wo,
                                                   const u16* __restrict__ wdown, char* __restrict__ ws) {
  extern __shared__ __attribute__((aligned(16))) char smem[];
  const int bid = blockIdx.x;
  if (bid == 0 && threadIdx.x == 0) *(unsigned*)(ws + OFF_CNT) = 0u;
  const int p = (bid & 7) * 30 + (bid >> 3);
  const int mh = p / 120, rem = p % 120, nt = rem >> 3, mt = mh * 8 + (rem & 7);
  if (nt < 4) k1_body<2>(x, wqkv, wqkv, ws, mt, nt, smem, wo, wdown);
  else if (nt < 6) k1_body<1>(x, wqkv, wqkv, ws, mt, nt - 4, smem, wo, wdown);
  else k1_body<0>(x, wg, wu, ws, mt, nt - 6, smem, wo, wdown);
  STAMP(1, 3);
}

constexpr int K2_LDS = 163840;
#define K2DD 3
constexpr int K2_D = K2DD;

__global__ __launch_bounds__(512, 2) void k2_gemm(const u16* __restrict__ x, char* ws, u16* __restrict__ out) {
  extern __shared__ __attribute__((aligned(16))) char smem[];
  const int tid = threadIdx.x, lane = tid & 63;
  const int wave = __builtin_amdgcn_readfirstlane(tid >> 6);
  const int wq = wave & 3;
  const int g = lane >> 4, c = lane & 15;
  const int bid = blockIdx.x;
  const int mt = (bid & 7) * 4 + (bid >> 6), nt = (bid >> 3) & 7;

  STAMP(2, 0);
  gu32* cnt = (gu32*)(ws + OFF_CNT);
  volatile int* lflag = (volatile int*)(smem + K2_LDS - 1024);
  if (tid == 0) *lflag = 0;
  __syncthreads();
  if (wave == 4 && bid < 32) {
    const float* Pf = (const float*)(ws + OFF_KVP) + (size_t)bid * 504;
    const int l0 = lane * 4, l1 = (lane < 62 ? lane + 64 : lane) * 4;
    f32x4 a0[16], a1[16];
#pragma unroll
    for (int m = 0; m < 16; ++m) {
      a0[m] = *(const f32x4*)(Pf + (size_t)m * 32 * 504 + l0);
      a1[m] = *(const f32x4*)(Pf + (size_t)m * 32 * 504 + l1);
    }
    f32x4 s0 = a0[0], s1 = a1[0];
#pragma unroll
    for (int m = 1; m < 16; ++m) { s0 += a0[m]; s1 += a1[m]; }
    float* kvs = (float*)(smem + 4 * 20480 + 16384);
    *(f32x4*)(kvs + l0) = s0;
    if (lane < 62) *(f32x4*)(kvs + l1) = s1;
    WAIT_L0;
#pragma unroll
    for (int fr = 0; fr < 2; ++fr) {
      const int e = fr == 0 ? c : ((c & 3) == 0 ? 16 + (c >> 2) : ((c & 3) == 1 ? 20 : 99)), d0 = 8 * g;
      const bool act = (e <= 20 && d0 < 20), act1 = act && (d0 < 16);
      const float* src = kvs + (act ? e * 24 + d0 : 0);
      f32x4 v0 = *(const f32x4*)src, v1 = *(const f32x4*)(src + (act1 ? 4 : 0));
      u32x4 w;
      w[0] = act ? cvt2(lo2(v0)) : 0u; w[1] = act ? cvt2(hi2(v0)) : 0u;
      w[2] = act1 ? cvt2(lo2(v1)) : 0u; w[3] = act1 ? cvt2(hi2(v1)) : 0u;
      asm volatile("global_store_dwordx4 %0, %1, off sc1" :: "v"(ws + OFF_KVF + ((size_t)(bid * 2 + fr) * 64 + lane) * 16), "v"(w) : "memory");
    }
    asm volatile("s_waitcnt vmcnt(0)" ::: "memory");
    if (lane == 0) __hip_atomic_fetch_add(cnt, 1u, RLX_AGENT);
  }
  f32x4 acc[4][5];
#pragma unroll
  for (int a = 0; a < 4; ++a)
#pragma unroll
    for (int b = 0; b < 5; ++b) acc[a][b] = (f32x4){0.f, 0.f, 0.f, 0.f};
  const int b0 = wave < 4 ? 0 : 3, nb = wave < 4 ? 3 : 2;
  const int tok = mt * 64 + wq * 16 + c;

  if (wave < 4) {
    const char* hsrc = ws + OFF_H + ((size_t)(mt * 4) * 54 + wq) * 1024 + lane * 16;
    const char* wsrc = ws + OFF_WC + ((size_t)nt * 54 + wq) * 5120 + lane * 16;
    s16x8 xr[K2_D][4], wr[K2_D][5];
    auto ld = [&](int slot, int i) {
#pragma unroll
      for (int b = 0; b < 5; ++b) wr[slot][b] = *(const s16x8*)(wsrc + (size_t)(20 * i + b) * 1024);
#pragma unroll
      for (int a = 0; a < 4; ++a) xr[slot][a] = *(const s16x8*)(hsrc + (size_t)(a * 54 + 4 * i) * 1024);
    };
    auto mma1 = [&](int slot) {
#pragma unroll
      for (int a = 0; a < 4; ++a)
#pragma unroll
        for (int b = 0; b < 5; ++b) acc[a][b] = __builtin_amdgcn_mfma_f32_16x16x32_bf16(wr[slot][b], xr[slot][a], acc[a][b], 0, 0, 0);
    };
#pragma unroll
    for (int i = 0; i < K2_D; ++i) ld(i, i);
    SCHED;
#pragma unroll
    for (int i = 0; i < 13; ++i) {
      mma1(i % K2_D);
      if (i + K2_D < 13) ld(i % K2_D, i + K2_D);
      else if (i + K2_D == 13) { if (wave < 2) ld(i % K2_D, 13); }
      SCHED;
    }
    if (wave < 2) mma1(13 % K2_D);
    STAMP(2, 1);
  } else {
    const char* pqsrc = ws + OFF_PQ + ((size_t)(mt * 4) * 32 + wq * 8) * 640 + (g < 2 ? g * 256 + c * 16 : 512 + c * 8);
    const char* ksrc = ws + OFF_KVF + (size_t)(wq * 8) * 2048 + lane * 16;
    const char* wosrc = ws + OFF_WO + ((size_t)nt * 32 + wq * 8) * 5120 + lane * 16;
    u32x4 pq[2][4];
    s16x8 kvf[2][2], w2[2][5];
    char* kl = smem + wave * 20480;
    auto ldK = [&](int sl, int hh) {
#pragma unroll
      for (int fr = 0; fr < 2; ++fr) kvf[sl][fr] = *(const s16x8*)(kl + hh * 2048 + fr * 1024 + lane * 16);
    };
    auto ldA = [&](int sl, int hh) {
#pragma unroll
      for (int tg = 0; tg < 4; ++tg) pq[sl][tg] = *(const u32x4*)(pqsrc + (tg * 32 + hh) * 640);
    };
    auto ldW = [&](int sl, int hh) {
#pragma unroll
      for (int b = 0; b < 5; ++b) w2[sl][b] = *(const s16x8*)(wosrc + (hh * 5 + b) * 1024);
    };
    ldA(0, 0); ldW(0, 0); ldA(1, 1); ldW(1, 1);
    {
      if (wave == 4) {
        while (__hip_atomic_load(cnt, RLX_AGENT) < 32u) __builtin_amdgcn_s_sleep(16);
        if (lane == 0) *lflag = 1;
      } else {
        while (*lflag != 1) __builtin_amdgcn_s_sleep(8);
      }
    }
    asm volatile("" ::: "memory");
#pragma unroll
    for (int i = 0; i < 16; ++i)
      __builtin_amdgcn_global_load_lds((const unsigned*)(ksrc + i * 1024), (unsigned*)(kl + i * 1024), 16, 0, 16);
    asm volatile("s_waitcnt vmcnt(0)" ::: "memory");
    ldK(0, 0); ldK(1, 1);
    const f32x4 z4 = (f32x4){0.f, 0.f, 0.f, 0.f};
#pragma unroll
    for (int hh = 0; hh < 8; ++hh) {
      const int sl = hh & 1;
      f32x4 d0[4], d1[4];
#pragma unroll
      for (int tg = 0; tg < 4; ++tg) {
        u32x4 pk4;
        pk4[0] = g < 3 ? pq[sl][tg][0] : 0u; pk4[1] = g < 3 ? pq[sl][tg][1] : 0u;
        pk4[2] = g < 2 ? pq[sl][tg][2] : 0u; pk4[3] = g < 2 ? pq[sl][tg][3] : 0u;
        const s16x8 pf = __builtin_bit_cast(s16x8, pk4);
        d1[tg] = __builtin_amdgcn_mfma_f32_16x16x32_bf16(kvf[sl][1], pf, z4, 0, 0, 0);
        d0[tg] = __builtin_amdgcn_mfma_f32_16x16x32_bf16(kvf[sl][0], pf, z4, 0, 0, 0);
      }
      if (hh + 2 < 8) { ldK(sl, hh + 2); ldA(sl, hh + 2); }
      s16x8 of[4];
#pragma unroll
      for (int tg = 0; tg < 4; ++tg) {
        const float rden = frcp(bfr(bfr(d1[tg][1]) + 1e-6f));
        u32x4 ow;
        ow[0] = cvt2(bfr2(lo2(d0[tg])) * rden); ow[1] = cvt2(bfr2(hi2(d0[tg])) * rden);
        ow[2] = (unsigned)f2bf(bfr(d1[tg][0]) * rden); ow[3] = 0u;
        of[tg] = __builtin_bit_cast(s16x8, ow);
      }
#pragma unroll
      for (int a = 0; a < 4; ++a)
#pragma unroll
        for (int b = 0; b < 5; ++b) acc[a][b] = __builtin_amdgcn_mfma_f32_16x16x32_bf16(w2[sl][b], of[a], acc[a][b], 0, 0, 0);
      if (hh + 2 < 8) ldW(sl, hh + 2);
      SCHED;
    }
  }

  u32x2 xw[3];
#pragma unroll
  for (int i = 0; i < 3; ++i) xw[i] = *(const u32x2*)(x + (size_t)tok * D_ + nt * 80 + (i < nb ? b0 + i : b0) * 16 + 4 * g);

  f32x4* red = (f32x4*)smem;
  f32x4 own[3];
#pragma unroll
  for (int a = 0; a < 4; ++a)
#pragma unroll
    for (int b = 0; b < 5; ++b) {
      const bool mine = (a == wq) && (b >= b0) && (b < b0 + nb);
      if (mine) own[b < 3 ? b : b - 3] = acc[a][b];
      else red[(wave * 20 + a * 5 + b) * 64 + lane] = acc[a][b];
    }
  __syncthreads();
#pragma unroll
  for (int i = 0; i < 3; ++i) {
    if (i < nb) {
      const int f = wq * 5 + b0 + i;
      f32x4 p[8];
#pragma unroll
      for (int sw = 0; sw < 8; ++sw) p[sw] = sw == wave ? own[i] : red[(sw * 20 + f) * 64 + lane];
      const f32x4 y1 = ((p[0] + p[1]) + p[2]) + p[3], y2 = ((p[4] + p[5]) + p[6]) + p[7];
      const int n = nt * 80 + (b0 + i) * 16 + 4 * g;
      u32x2 w;
      {
        const f32x2 y = bfr2(lo2(y1)), gs = bfr2(y * sigm2(y)), sv = bfr2(lo2(y2));
        w[0] = cvt2(unp2(xw[i][0]) + bfr2(sv * gs));
      }
      {
        const f32x2 y = bfr2(hi2(y1)), gs = bfr2(y * sigm2(y)), sv = bfr2(hi2(y2));
        w[1] = cvt2(unp2(xw[i][1]) + bfr2(sv * gs));
      }
      *(u32x2*)(out + (size_t)tok * D_ + n) = w;
    }
  }
  STAMP(2, 3);
}

extern "C" void kernel_launch(void* const* d_in, const int* in_sizes, int n_in,
                              void* d_out, int out_size, void* d_ws, size_t ws_size,
                              hipStream_t stream) {
  const u16* x = (const u16*)d_in[0];
  const u16* wqkv = (const u16*)d_in[1];
  const u16* wo = (const u16*)d_in[2];
  const u16* wg = (const u16*)d_in[4];
  const u16* wu = (const u16*)d_in[5];
  const u16* wd = (const u16*)d_in[6];
  char* ws = (char*)d_ws;
  u16* out = (u16*)d_out;

  hipFuncSetAttribute((const void*)k1_gemm, hipFuncAttributeMaxDynamicSharedMemorySize, K1_NS * K1_STAGE);
  hipFuncSetAttribute((const void*)k2_gemm, hipFuncAttributeMaxDynamicSharedMemorySize, K2_LDS);

  k1_gemm<<<240, 512, K1_NS * K1_STAGE, stream>>>(x, wqkv, wg, wu, wo, wd, ws);
  k2_gemm<<<256, 512, K2_LDS, stream>>>(x, ws, out);
}
```

```cpp
#include <hip/hip_runtime.h>
#include <hip/hip_bf16.h>
#include <stdint.h>

typedef unsigned short u16;
typedef __attribute__((ext_vector_type(8))) short s16x8;
typedef __attribute__((ext_vector_type(4))) short s16x4;
typedef __attribute__((ext_vector_type(4))) float f32x4;
typedef __attribute__((ext_vector_type(16))) float f32x16;
typedef __attribute__((ext_vector_type(2))) unsigned u32x2;
typedef __attribute__((ext_vector_type(4))) unsigned u32x4;
typedef u32x4 u32x4_a8 __attribute__((aligned(8)));
typedef __attribute__((address_space(3))) s16x4* lds_s16x4_ptr;
typedef __attribute__((address_space(1))) unsigned gu32;
typedef __attribute__((address_space(1))) unsigned long long gu64;

#define RLX_AGENT __ATOMIC_RELAXED, __HIP_MEMORY_SCOPE_AGENT

constexpr int S_ = 2048, D_ = 640, F_ = 1728, NQKV_ = 1920;

constexpr size_t OFF_CNT   = 0;
constexpr size_t OFF_XN    = 8192;
constexpr size_t SZ_XN     = (size_t)S_ * D_ * 2;
constexpr size_t OFF_H     = OFF_XN + SZ_XN;
constexpr size_t SZ_H      = (size_t)S_ * F_ * 2;
constexpr size_t OFF_PQ    = OFF_H + SZ_H;
constexpr size_t SZ_PQ     = (size_t)S_ * D_ * 2;
constexpr size_t OFF_WC    = OFF_PQ + SZ_PQ;
constexpr size_t SZ_WC     = (size_t)8 * 54 * 5 * 1024;
constexpr size_t OFF_WO    = OFF_WC + SZ_WC;
constexpr size_t SZ_WO     = (size_t)8 * 4 * 5 * 5 * 1024;
constexpr size_t OFF_KVP   = OFF_WO + SZ_WO;
constexpr size_t SZ_KVP    = (size_t)16 * 32 * 504 * 4;
constexpr size_t OFF_KVF   = OFF_KVP + SZ_KVP;
constexpr size_t SZ_KVF    = 65536;

constexpr unsigned KV_EMPTY = 0xFFFFFFFFu;
__device__ __forceinline__ float bf2f(u16 v) { return __uint_as_float(((unsigned)v) << 16); }
__device__ __forceinline__ u16 f2bf(float f) { return __builtin_bit_cast(u16, (__bf16)f); }
__device__ __forceinline__ float bfr(float f) { return bf2f(f2bf(f)); }
__device__ __forceinline__ unsigned pack2(float a, float b) { return (unsigned)f2bf(a) | ((unsigned)f2bf(b) << 16); }
__device__ __forceinline__ float phi_f(float t) {
  float p = t > 0.f ? bfr(t + 1.0f) : bfr(__expf(t));
  return bfr(p + 1e-6f);
}
typedef __attribute__((ext_vector_type(2))) float f32x2;
typedef __attribute__((ext_vector_type(2))) __bf16 bf16x2;
__device__ __forceinline__ unsigned cvt2(f32x2 v) { return __builtin_bit_cast(unsigned, __builtin_convertvector(v, bf16x2)); }
__device__ __forceinline__ f32x2 unp2(unsigned u) { return (f32x2){__uint_as_float(u << 16), __uint_as_float(u & 0xffff0000u)}; }
__device__ __forceinline__ f32x2 bfr2(f32x2 v) { return unp2(cvt2(v)); }
__device__ __forceinline__ f32x2 lo2(f32x4 v) { return __builtin_shufflevector(v, v, 0, 1); }
__device__ __forceinline__ f32x2 hi2(f32x4 v) { return __builtin_shufflevector(v, v, 2, 3); }
__device__ __forceinline__ unsigned phi2(f32x2 a) {
  const f32x2 t = bfr2(a), m = t * 1.44269504088896340736f, p = t + 1.0f;
  f32x2 sel;
  sel[0] = t[0] > 0.f ? p[0] : __builtin_amdgcn_exp2f(m[0]);
  sel[1] = t[1] > 0.f ? p[1] : __builtin_amdgcn_exp2f(m[1]);
  return cvt2(bfr2(sel) + 1e-6f);
}
__device__ __forceinline__ f32x2 sigm2(f32x2 g) {
  const f32x2 m = g * -1.44269504088896340736f;
  f32x2 e; e[0] = __builtin_amdgcn_exp2f(m[0]); e[1] = __builtin_amdgcn_exp2f(m[1]);
  e = e + 1.0f;
  f32x2 r; r[0] = __builtin_amdgcn_rcpf(e[0]); r[1] = __builtin_amdgcn_rcpf(e[1]);
  return r;
}
__device__ __forceinline__ int aswz(int row) { int x = (row >> 2) & 3; return (((x ^ (x >> 1)) & 1) << 1) | (x >> 1); }

#define WAIT_V(n) asm volatile("s_waitcnt vmcnt(" #n ")" ::: "memory")
#define STAMP(kid, idx)
#define STAMPW(idx)
#define WAIT_L0 asm volatile("s_waitcnt lgkmcnt(0)" ::: "memory")
#define BAR __builtin_amdgcn_s_barrier()
#define SCHED __builtin_amdgcn_sched_barrier(0)
__device__ __forceinline__ float frcp(float v) { return __builtin_amdgcn_rcpf(v); }

constexpr int RT_WD = 216 * 80, RT_ITEMS = RT_WD + 80 * 80, RT_R = 3;
__device__ __forceinline__ void retile_all(int t0, const u16* __restrict__ wo, const u16* __restrict__ wdown, char* __restrict__ ws) {
  u32x4 r[RT_R][8];
  char* dst[RT_R];
#pragma unroll
  for (int it = 0; it < RT_R; ++it) {
    int t = t0 + it * 8192; if (t >= RT_ITEMS) t = t0;
    const bool isd = t < RT_WD;
    const int t2 = isd ? t : t - RT_WD;
    const int ko = t2 / 80, noct = t2 - ko * 80;
    const int n0 = noct * 8, nt = n0 / 80, nn = n0 - nt * 80, nf = nn >> 4, c0 = nn & 15, gg = ko & 3, kq = ko >> 2;
    const int wq = kq / 5, ks = kq - wq * 5;
    const u16* src = (isd ? wdown : wo) + n0;
#pragma unroll
    for (int jj = 0; jj < 8; ++jj) {
      const int q = 8 * ks + jj, hh = q / 5, rr = q - hh * 5;
      const int row = isd ? ko * 8 + jj : (wq * 8 + hh) * 20 + (rr < 4 ? 4 * gg + rr : 16 + gg);
      r[it][jj] = *(const u32x4*)(src + (size_t)row * D_);
    }
    dst[it] = ws + (isd ? OFF_WC + (size_t)(nt * 54 + kq) * 5120 : OFF_WO + (size_t)(nt * 20 + kq) * 5120) + (size_t)(nf * 64 + gg * 16 + c0) * 16;
  }
#pragma unroll
  for (int it = 0; it < RT_R; ++it) {
#pragma unroll
    for (int i = 0; i < 4; ++i) {
      u32x4 lo, hi;
#pragma unroll
      for (int q = 0; q < 4; ++q) {
        lo[q] = (r[it][2 * q][i] & 0xffffu) | (r[it][2 * q + 1][i] << 16);
        hi[q] = (r[it][2 * q][i] >> 16) | (r[it][2 * q + 1][i] & 0xffff0000u);
      }
      *(u32x4*)(dst[it] + (2 * i) * 16) = lo;
      *(u32x4*)(dst[it] + (2 * i + 1) * 16) = hi;
    }
  }
}

__global__ __launch_bounds__(256) void k0_prep(const u16* __restrict__ x, char* __restrict__ ws) {
  const int tid = threadIdx.x;
  {
    { const int ci = blockIdx.x * 256 + tid;
      if (ci < (int)(SZ_KVP / 16)) *(u32x4*)(ws + OFF_KVP + (size_t)ci * 16) = (u32x4){KV_EMPTY, KV_EMPTY, KV_EMPTY, KV_EMPTY}; }
    const int row = blockIdx.x * 8 + (tid >> 5);
    const int l32 = tid & 31;
    const u16* xr = x + (size_t)row * D_;
    float v[20];
    float sum = 0.f;
#pragma unroll
    for (int i = 0; i < 5; ++i) {
      u32x2 w = *(const u32x2*)(xr + (l32 + 32 * i) * 4);
      v[4 * i + 0] = __uint_as_float(w[0] << 16);
      v[4 * i + 1] = __uint_as_float(w[0] & 0xffff0000u);
      v[4 * i + 2] = __uint_as_float(w[1] << 16);
      v[4 * i + 3] = __uint_as_float(w[1] & 0xffff0000u);
      sum += v[4 * i] + v[4 * i + 1] + v[4 * i + 2] + v[4 * i + 3];
    }
#pragma unroll
    for (int o = 16; o >= 1; o >>= 1) sum += __shfl_xor(sum, o);
    const float mean = sum * (1.0f / D_);
    float sq = 0.f;
#pragma unroll
    for (int i = 0; i < 20; ++i) { float d = v[i] - mean; sq += d * d; }
#pragma unroll
    for (int o = 16; o >= 1; o >>= 1) sq += __shfl_xor(sq, o);
    const float mb = bfr(mean);
    const float vb = bfr(sq * (1.0f / D_));
    const float sd = bfr(sqrtf(bfr(vb + 1e-5f)));
    u16* xn = (u16*)(ws + OFF_XN);
    const int mt = row >> 7, r = row & 127, sw = aswz(r);
#pragma unroll
    for (int i = 0; i < 5; ++i) {
      const int k = (l32 + 32 * i) * 4;
      float o0 = bfr(v[4 * i + 0] - mb) / sd, o1 = bfr(v[4 * i + 1] - mb) / sd;
      float o2 = bfr(v[4 * i + 2] - mb) / sd, o3 = bfr(v[4 * i + 3] - mb) / sd;
      u32x2 w; w[0] = pack2(o0, o1); w[1] = pack2(o2, o3);
      const int kt = k >> 5, ch = (k >> 3) & 3, hf = (k >> 2) & 1;
      size_t off = ((size_t)(mt * 20 + kt) * 128 + r) * 32 + ((ch ^ sw) << 3) + (hf << 2);
      *(u32x2*)(xn + off) = w;
    }
  }
}

constexpr int K1_STAGE = 32768;
#define K1NS 4
constexpr int K1_NS = K1NS;

template <int TYPE>
__device__ __forceinline__ void k1_body(const u16* __restrict__ wA, const u16* __restrict__ wB, char* __restrict__ ws,
                                        int mt, int j, char* smem) {
  constexpr int NCB = TYPE == 0 ? 6 : 5;
  constexpr int NFR = TYPE == 0 ? 6 : 5;
  constexpr int LDW = TYPE == 0 ? F_ : NQKV_;
  const int tid = threadIdx.x, lane = tid & 63;
  const int wave = __builtin_amdgcn_readfirstlane(tid >> 6);
  const int wm = wave >> 2, wn = (TYPE == 2 && wave >= 4) ? ((wave + 2) & 3) : (wave & 3);
  const int g = lane >> 4, c = lane & 15;

  const char* asrc = ws + OFF_XN + (size_t)mt * 20 * 8192 + wave * 1024 + lane * 16;
  const u16* bsrc[3];
#pragma unroll
  for (int t = 0; t < 3; ++t) {
    int ii = wave + 8 * t; if (ii >= NCB * 4) ii = wave;
    const int cb = ii >> 2, krow = (ii & 3) * 8 + (lane >> 3), u = lane & 7;
    const int sk = ((krow >> 1) & 1) | (((krow >> 3) & 1) << 1);
    const int ch = (u >> 1) ^ sk;
    const int lc = cb * 64 + ch * 16 + (u & 1) * 8;
    const u16* p;
    if (TYPE == 0) p = (lc < 192) ? (wA + j * 192 + lc) : (wB + j * 192 + (lc - 192));
    else if (TYPE == 1) p = wA + j * 320 + lc;
    else p = (lc < 160) ? (wA + 640 + j * 160 + lc) : (wA + 1280 + j * 160 + (lc - 160));
    bsrc[t] = p + (size_t)krow * LDW;
  }
  const bool has3 = (NCB == 6) || (wave < 4);

  auto issue = [&](int kt) {
    char* sb = smem + (kt % K1_NS) * K1_STAGE;
    __builtin_amdgcn_global_load_lds((const unsigned*)(asrc + (size_t)kt * 8192), (unsigned*)(sb + wave * 1024), 16, 0, 0);
    __builtin_amdgcn_global_load_lds((const unsigned*)(bsrc[0] + (size_t)kt * 32 * LDW), (unsigned*)(sb + 8192 + wave * 1024), 16, 0, 0);
    __builtin_amdgcn_global_load_lds((const unsigned*)(bsrc[1] + (size_t)kt * 32 * LDW), (unsigned*)(sb + 8192 + (wave + 8) * 1024), 16, 0, 0);
    if (has3) __builtin_amdgcn_global_load_lds((const unsigned*)(bsrc[2] + (size_t)kt * 32 * LDW), (unsigned*)(sb + 8192 + (wave + 16) * 1024), 16, 0, 0);
  };

  const int xoff = (wm * 64 + c) * 64 + ((g ^ aswz(c)) << 4);
  int woff[NFR];
  {
    const int q = (lane & 15) >> 2, p = lane & 3;
    const int s = (q >> 1) | ((g & 1) << 1);
    const int lo = (8 * g + q) * 128 + (s << 5) + p * 8;
#pragma unroll
    for (int i = 0; i < NFR; ++i) {
      const int nf = TYPE == 0 ? (i < 3 ? 3 * wn + i : 12 + 3 * wn + (i - 3)) : 5 * wn + i;
      woff[i] = 8192 + (nf >> 2) * 4096 + (lo ^ ((nf & 3) << 5));
    }
  }

  f32x4 acc[4][NFR];
#pragma unroll
  for (int a = 0; a < 4; ++a)
#pragma unroll
    for (int b = 0; b < NFR; ++b) acc[a][b] = (f32x4){0.f, 0.f, 0.f, 0.f};

  auto readfr = [&](int kt, s16x8 (&xf)[4], s16x8 (&wf)[NFR]) {
    const char* sb = smem + (kt % K1_NS) * K1_STAGE;
#pragma unroll
    for (int b = 0; b < NFR; ++b) {
      s16x4 lo = __builtin_amdgcn_ds_read_tr16_b64_v4i16((lds_s16x4_ptr)(sb + woff[b]));
      s16x4 hi = __builtin_amdgcn_ds_read_tr16_b64_v4i16((lds_s16x4_ptr)(sb + woff[b] + 512));
      wf[b] = __builtin_shufflevector(lo, hi, 0, 1, 2, 3, 4, 5, 6, 7);
    }
#pragma unroll
    for (int a = 0; a < 4; ++a) xf[a] = *(const s16x8*)(sb + xoff + a * 1024);
  };
  auto mma = [&](const s16x8 (&xf)[4], const s16x8 (&wf)[NFR]) {
#pragma unroll
    for (int a = 0; a < 4; ++a)
#pragma unroll
      for (int b = 0; b < NFR; ++b) {
        if (TYPE == 2) acc[a][b] = __builtin_amdgcn_mfma_f32_16x16x32_bf16(xf[a], wf[b], acc[a][b], 0, 0, 0);
        else           acc[a][b] = __builtin_amdgcn_mfma_f32_16x16x32_bf16(wf[b], xf[a], acc[a][b], 0, 0, 0);
      }
  };
#define WAITN(N) asm volatile("s_waitcnt vmcnt(%0)" :: "n"(N) : "memory")
#define K1_WAIT(n) do { if (has3) WAITN(4 * (n)); else WAITN(3 * (n)); } while (0)

  s16x8 xA[4], wAf[NFR], xB[4], wBf[NFR];
#define SGB(m, n) __builtin_amdgcn_sched_group_barrier(m, n, 0)
  auto pattern_full = [&]() {
    SGB(0x10, 1); SGB(0x8, 1); SGB(0x10, 1); SGB(0x8, 1); SGB(0x10, 1); SGB(0x8, 1); SGB(0x10, 1); SGB(0x8, 1);
#pragma unroll
    for (int i = 0; i < 4 + 2 * NFR; ++i) { SGB(0x100, 1); SGB(0x8, 1); }
    SGB(0x8, 4);
  };
  auto pattern_tail = [&]() {
#pragma unroll
    for (int i = 0; i < 4 + 2 * NFR; ++i) { SGB(0x100, 1); SGB(0x8, 1); }
    SGB(0x8, 8);
  };
  STAMP(1, 0);
#pragma unroll
  for (int i = 0; i < K1_NS; ++i) issue(i);
  K1_WAIT(K1_NS - 1);
  BAR;
  STAMP(1, 1);
  readfr(0, xA, wAf);
  SCHED;
#pragma unroll
  for (int kt = 0; kt < 19; ++kt) {
    if (kt + K1_NS <= 20) K1_WAIT(K1_NS - 2); else K1_WAIT(18 - kt);
    WAIT_L0; BAR; SCHED;
    if (kt + K1_NS < 20) issue(kt + K1_NS);
    if (kt & 1) { readfr(kt + 1, xA, wAf); mma(xB, wBf); }
    else        { readfr(kt + 1, xB, wBf); mma(xA, wAf); }
    if (kt + K1_NS < 20) pattern_full(); else pattern_tail();
    SCHED;
  }
  mma(xB, wBf);

  WAIT_L0; BAR;
  STAMP(1, 2);
  if (TYPE == 0) {
#pragma unroll
    for (int a = 0; a < 4; ++a) {
#pragma unroll
      for (int b = 0; b < 3; ++b) {
        u32x2 w;
        w[0] = cvt2(bfr2(sigm2(bfr2(lo2(acc[a][b])))) * bfr2(lo2(acc[a][b + 3])));
        w[1] = cvt2(bfr2(sigm2(bfr2(hi2(acc[a][b])))) * bfr2(hi2(acc[a][b + 3])));
        const int f0 = wn * 48 + b * 16;
        const int ktl = f0 >> 5, gk = ((f0 >> 4) & 1) * 2 + (g >> 1);
        *(u32x2*)(smem + (((wm * 4 + a) * 6 + ktl) * 64 + gk * 16 + c) * 16 + (g & 1) * 8) = w;
      }
    }
    __syncthreads();
    char* H = ws + OFF_H + ((size_t)(mt * 8) * 54 + j * 6) * 1024;
#pragma unroll
    for (int i = 0; i < 6; ++i) {
      const int q = tid + 512 * i;
      const int m16 = q / 384, rest = q - m16 * 384;
      const u32x4 v = *(const u32x4*)(smem + q * 16);
      *(u32x4*)(H + (size_t)m16 * 54 * 1024 + rest * 16) = v;
    }
  } else if (TYPE == 1) {
#pragma unroll
    for (int a = 0; a < 4; ++a) {
#pragma unroll
      for (int b = 0; b < 5; ++b) {
        u32x2 w; w[0] = phi2(lo2(acc[a][b])); w[1] = phi2(hi2(acc[a][b]));
        const int n = wn * 80 + b * 16 + 4 * g, hl = n / 20, d0 = n - hl * 20;
        const int ro = d0 < 16 ? (d0 >> 3) * 256 + c * 16 + (d0 & 7) * 2 : 512 + c * 8;
        *(u32x2*)(smem + ((wm * 4 + a) * 16 + hl) * 640 + ro) = w;
      }
    }
    __syncthreads();
    char* PQ = ws + OFF_PQ + ((size_t)(mt * 8) * 32 + j * 16) * 640;
#pragma unroll
    for (int i = 0; i < 10; ++i) {
      const int q = tid + 512 * i;
      const int m16 = q / 640, rest = q - m16 * 640;
      const u32x4 v = *(const u32x4*)(smem + q * 16);
      *(u32x4*)(PQ + (size_t)m16 * 20480 + rest * 16) = v;
    }
  } else {
#pragma unroll
    for (int a = 0; a < 4; ++a)
#pragma unroll
      for (int b = 0; b < 5; ++b) {
        const int col = wn * 80 + b * 16 + c;
        u32x2 w;
        if (wn < 2) { w[0] = phi2(lo2(acc[a][b])); w[1] = phi2(hi2(acc[a][b])); }
        else        { w[0] = cvt2(lo2(acc[a][b])); w[1] = cvt2(hi2(acc[a][b])); }
        *(u32x2*)(smem + col * 272 + (wm * 64 + a * 16 + 4 * g) * 2) = w;
      }
    __syncthreads();
    const int r32 = lane & 31, hh = lane >> 5;
    f32x16 akv, az;
#pragma unroll
    for (int i = 0; i < 16; ++i) { akv[i] = 0.f; az[i] = 0.f; }
    s16x8 ones;
#pragma unroll
    for (int i = 0; i < 8; ++i) ones[i] = (short)0x3F80;
#pragma unroll
    for (int s = 0; s < 8; ++s) {
      s16x8 af = *(const s16x8*)(smem + (20 * wave + r32) * 272 + (16 * s + 8 * hh) * 2);
      s16x8 bfv = *(const s16x8*)(smem + (160 + 20 * wave + r32) * 272 + (16 * s + 8 * hh) * 2);
      akv = __builtin_amdgcn_mfma_f32_32x32x16_bf16(af, bfv, akv, 0, 0, 0);
      az = __builtin_amdgcn_mfma_f32_32x32x16_bf16(af, ones, az, 0, 0, 0);
    }
    const int head = j * 8 + wave;
    float* P = (float*)(ws + OFF_KVP) + (size_t)(mt * 32 + head) * 504;
    if (r32 <= 20) {
#pragma unroll
      for (int q = 0; q < 3; ++q) {
        const int d0 = 8 * q + 4 * hh;
        if (d0 < 20) {
          f32x4 val;
#pragma unroll
          for (int r = 0; r < 4; ++r) {
            const unsigned u = __float_as_uint((r32 == 20) ? az[4 * q + r] : akv[4 * q + r]);
            val[r] = __uint_as_float(u == KV_EMPTY ? 0x7FC00000u : u);
          }
          asm volatile("global_store_dwordx4 %0, %1, off sc1" :: "v"(P + r32 * 24 + d0), "v"(val) : "memory");
        }
      }
    }
    if (mt < 8) {
      const int hd = j * 8 + mt, ci = tid & 127, pg = tid >> 7;
      const float* src = (const float*)(ws + OFF_KVP) + ((size_t)(4 * pg) * 32 + hd) * 504 + (ci < 126 ? ci : 0) * 4;
      const bool chk = ci < 126 && (ci % 6) != 5;
      u32x4 v0, v1, v2, v3;
      bool bad;
      do {
        asm volatile("global_load_dwordx4 %0, %4, off sc1\n\tglobal_load_dwordx4 %1, %5, off sc1\n\t"
                     "global_load_dwordx4 %2, %6, off sc1\n\tglobal_load_dwordx4 %3, %7, off sc1\n\ts_waitcnt vmcnt(0)"
                     : "=&v"(v0), "=&v"(v1), "=&v"(v2), "=&v"(v3)
                     : "v"(src), "v"(src + 32 * 504), "v"(src + 2 * 32 * 504), "v"(src + 3 * 32 * 504) : "memory");
        bad = false;
#pragma unroll
        for (int i = 0; i < 4; ++i) bad = bad || v0[i] == KV_EMPTY || v1[i] == KV_EMPTY || v2[i] == KV_EMPTY || v3[i] == KV_EMPTY;
      } while (__builtin_amdgcn_ballot_w64(bad && chk) != 0ull);
      f32x4* red = (f32x4*)(smem + 98304);
      red[pg * 128 + ci] = ((__builtin_bit_cast(f32x4, v0) + __builtin_bit_cast(f32x4, v1)) + __builtin_bit_cast(f32x4, v2)) + __builtin_bit_cast(f32x4, v3);
      __syncthreads();
      float* kvs = (float*)(smem + 106496);
      if (tid < 126) *(f32x4*)(kvs + tid * 4) = ((red[tid] + red[128 + tid]) + red[256 + tid]) + red[384 + tid];
      __syncthreads();
      if (wave < 2) {
        const int fr = wave;
        const int e = fr == 0 ? c : ((c & 3) == 0 ? 16 + (c >> 2) : ((c & 3) == 1 ? 20 : 99)), d0 = 8 * g;
        const bool act = (e <= 20 && d0 < 20), act1 = act && (d0 < 16);
        const float* sp = kvs + (act ? e * 24 + d0 : 0);
        f32x4 w0 = *(const f32x4*)sp, w1 = *(const f32x4*)(sp + (act1 ? 4 : 0));
        u32x4 w;
        w[0] = act ? pack2(w0[0], w0[1]) : 0u; w[1] = act ? pack2(w0[2], w0[3]) : 0u;
        w[2] = act1 ? pack2(w1[0], w1[1]) : 0u; w[3] = act1 ? pack2(w1[2], w1[3]) : 0u;
        *(u32x4*)(ws + OFF_KVF + ((size_t)(hd * 2 + fr) * 64 + lane) * 16) = w;
      }
    }
  }
}

__global__ __launch_bounds__(512, 2) void k1_gemm(const u16* __restrict__ wqkv, const u16* __restrict__ wg,
                                                   const u16* __restrict__ wu, const u16* __restrict__ wo,
                                                   const u16* __restrict__ wdown, char* __restrict__ ws) {
  extern __shared__ __attribute__((aligned(16))) char smem[];
  const int bid = blockIdx.x;
  if (bid >= 240) {
    retile_all((bid - 240) * 512 + threadIdx.x, wo, wdown, ws);
    return;
  }
  const int p = (bid & 7) * 30 + (bid >> 3);
  const int mh = p / 120, rem = p % 120, nt = rem >> 3, mt = mh * 8 + (rem & 7);
  if (nt < 4) k1_body<2>(wqkv, wqkv, ws, mt, nt, smem);
  else if (nt < 6) k1_body<1>(wqkv, wqkv, ws, mt, nt - 4, smem);
  else k1_body<0>(wg, wu, ws, mt, nt - 6, smem);
  STAMP(1, 3);
}

constexpr int K2_LDS = 163840;
#define K2DD 3
constexpr int K2_D = K2DD;
#define K2DW 3
constexpr int K2_DW = K2DW;

__global__ __launch_bounds__(512, 2) void k2_gemm(const u16* __restrict__ x, const char* __restrict__ ws, u16* __restrict__ out) {
  extern __shared__ __attribute__((aligned(16))) char smem[];
  const int tid = threadIdx.x, lane = tid & 63;
  const int wave = __builtin_amdgcn_readfirstlane(tid >> 6);
  const int wq = wave & 3;
  const int g = lane >> 4, c = lane & 15;
  const int bid = blockIdx.x;
  const int mt = (bid & 7) * 4 + (bid >> 6), nt = (bid >> 3) & 7;

  STAMP(2, 0);
  f32x4 acc[4][5];
#pragma unroll
  for (int a = 0; a < 4; ++a)
#pragma unroll
    for (int b = 0; b < 5; ++b) acc[a][b] = (f32x4){0.f, 0.f, 0.f, 0.f};
  const int b0 = wave < 4 ? 0 : 3, nb = wave < 4 ? 3 : 2;
  const int tok = mt * 64 + wq * 16 + c;
  u32x2 xw[3];
#pragma unroll
  for (int i = 0; i < 3; ++i) xw[i] = *(const u32x2*)(x + (size_t)tok * D_ + nt * 80 + (i < nb ? b0 + i : b0) * 16 + 4 * g);

  if (wave < 4) {
    const char* hsrc = ws + OFF_H + ((size_t)(mt * 4) * 54 + wq) * 1024 + lane * 16;
    const char* wsrc = ws + OFF_WC + ((size_t)nt * 54 + wq) * 5120 + lane * 16;
    s16x8 xr[K2_D][4], wr[K2_D][5];
    auto ld = [&](int slot, int i) {
#pragma unroll
      for (int b = 0; b < 5; ++b) wr[slot][b] = *(const s16x8*)(wsrc + (size_t)(20 * i + b) * 1024);
#pragma unroll
      for (int a = 0; a < 4; ++a) xr[slot][a] = *(const s16x8*)(hsrc + (size_t)(a * 54 + 4 * i) * 1024);
    };
    auto mma1 = [&](int slot) {
#pragma unroll
      for (int a = 0; a < 4; ++a)
#pragma unroll
        for (int b = 0; b < 5; ++b) acc[a][b] = __builtin_amdgcn_mfma_f32_16x16x32_bf16(wr[slot][b], xr[slot][a], acc[a][b], 0, 0, 0);
    };
#pragma unroll
    for (int i = 0; i < K2_D; ++i) ld(i, i);
    SCHED;
#pragma unroll
    for (int i = 0; i < 13; ++i) {
      mma1(i % K2_D);
      if (i + K2_D < 13) ld(i % K2_D, i + K2_D);
      else if (i + K2_D == 13) { if (wave < 2) ld(i % K2_D, 13); }
      SCHED;
    }
    if (wave < 2) mma1(13 % K2_D);
    STAMP(2, 1);
  } else {
    __builtin_amdgcn_s_setprio(3);
    char* reg = smem + wq * 36864;
    {
      const char* pqg = ws + OFF_PQ + ((size_t)(mt * 4) * 32 + wq * 8) * 640 + lane * 16;
      const char* kg = ws + OFF_KVF + (size_t)(wq * 8) * 2048 + lane * 16;
#pragma unroll
      for (int i = 0; i < 16; ++i) __builtin_amdgcn_global_load_lds((const unsigned*)(kg + i * 1024), (unsigned*)(reg + 20480 + i * 1024), 16, 0, 0);
#pragma unroll
      for (int tg = 0; tg < 4; ++tg)
#pragma unroll
        for (int i = 0; i < 5; ++i)
          __builtin_amdgcn_global_load_lds((const unsigned*)(pqg + tg * 20480 + i * 1024), (unsigned*)(reg + tg * 5120 + i * 1024), 16, 0, 0);
    }
    const char* wosrc = ws + OFF_WO + (size_t)(nt * 4 + wq) * 25600 + lane * 16;
    s16x8 w2[K2_DW][5];
    auto ldW = [&](int s) {
#pragma unroll
      for (int b = 0; b < 5; ++b) w2[s % K2_DW][b] = *(const s16x8*)(wosrc + (s * 5 + b) * 1024);
    };
#pragma unroll
    for (int s = 0; s < K2_DW; ++s) ldW(s);
    SCHED;
    __builtin_amdgcn_s_waitcnt(0x0F70);
    SCHED;
    STAMPW(4);
    const char* pql = reg + (g < 2 ? g * 256 + c * 16 : 512 + c * 8);
    const char* kvl = reg + 20480 + lane * 16;
    const f32x4 z4 = (f32x4){0.f, 0.f, 0.f, 0.f};
    unsigned ow[4][20];
    auto mma2 = [&](int s) {
#pragma unroll
      for (int a = 0; a < 4; ++a) {
        const u32x4 o4 = (u32x4){ow[a][4 * s], ow[a][4 * s + 1], ow[a][4 * s + 2], ow[a][4 * s + 3]};
        const s16x8 of = __builtin_bit_cast(s16x8, o4);
#pragma unroll
        for (int b = 0; b < 5; ++b) acc[a][b] = __builtin_amdgcn_mfma_f32_16x16x32_bf16(w2[s % K2_DW][b], of, acc[a][b], 0, 0, 0);
      }
      if (s + K2_DW < 5) ldW(s + K2_DW);
    };
#pragma unroll
    for (int m = 0; m < 4; ++m) {
      f32x4 ov[2][4]; float oe[2][4];
#pragma unroll
      for (int h2 = 0; h2 < 2; ++h2) {
        const int hh = 2 * m + h2;
        const s16x8 k0 = *(const s16x8*)(kvl + hh * 2048), k1 = *(const s16x8*)(kvl + hh * 2048 + 1024);
#pragma unroll
        for (int tg = 0; tg < 4; ++tg) {
          const u32x4 pq = *(const u32x4_a8*)(pql + tg * 5120 + hh * 640);
          u32x4 pk4;
          pk4[0] = g < 3 ? pq[0] : 0u; pk4[1] = g < 3 ? pq[1] : 0u;
          pk4[2] = g < 2 ? pq[2] : 0u; pk4[3] = g < 2 ? pq[3] : 0u;
          const s16x8 pf = __builtin_bit_cast(s16x8, pk4);
          const f32x4 d1 = __builtin_amdgcn_mfma_f32_16x16x32_bf16(k1, pf, z4, 0, 0, 0);
          const f32x4 d0 = __builtin_amdgcn_mfma_f32_16x16x32_bf16(k0, pf, z4, 0, 0, 0);
          const float rden = frcp(bfr(bfr(d1[1]) + 1e-6f));
          const f32x2 lo = bfr2(lo2(d0)) * rden, hi = bfr2(hi2(d0)) * rden;
          ov[h2][tg] = (f32x4){lo[0], lo[1], hi[0], hi[1]};
          oe[h2][tg] = bfr(d1[0]) * rden;
        }
      }
#pragma unroll
      for (int tg = 0; tg < 4; ++tg) {
        ow[tg][5 * m + 0] = cvt2(lo2(ov[0][tg]));
        ow[tg][5 * m + 1] = cvt2(hi2(ov[0][tg]));
        ow[tg][5 * m + 2] = cvt2((f32x2){oe[0][tg], ov[1][tg][0]});
        ow[tg][5 * m + 3] = cvt2((f32x2){ov[1][tg][1], ov[1][tg][2]});
        ow[tg][5 * m + 4] = cvt2((f32x2){ov[1][tg][3], oe[1][tg]});
      }
      mma2(m);
      if (m == 3) mma2(4);
      SCHED;
      if (m < 2) STAMPW(5 + m);
    }
  }
  __syncthreads();

  f32x4* red = (f32x4*)smem;
#pragma unroll
  for (int a = 0; a < 4; ++a)
#pragma unroll
    for (int b = 0; b < 5; ++b) red[(wave * 20 + a * 5 + b) * 64 + lane] = acc[a][b];
  __syncthreads();
#pragma unroll
  for (int i = 0; i < 3; ++i) {
    if (i < nb) {
      const int f = wq * 5 + b0 + i;
      f32x4 y1 = red[(0 * 20 + f) * 64 + lane], y2 = red[(4 * 20 + f) * 64 + lane];
#pragma unroll
      for (int sw = 1; sw < 4; ++sw) { y1 += red[(sw * 20 + f) * 64 + lane]; y2 += red[((4 + sw) * 20 + f) * 64 + lane]; }
      const int n = nt * 80 + (b0 + i) * 16 + 4 * g;
      u32x2 w;
      {
        const f32x2 y = bfr2(lo2(y1)), gs = bfr2(y * sigm2(y)), sv = bfr2(lo2(y2));
        w[0] = cvt2(unp2(xw[i][0]) + bfr2(sv * gs));
      }
      {
        const f32x2 y = bfr2(hi2(y1)), gs = bfr2(y * sigm2(y)), sv = bfr2(hi2(y2));
        w[1] = cvt2(unp2(xw[i][1]) + bfr2(sv * gs));
      }
      *(u32x2*)(out + (size_t)tok * D_ + n) = w;
    }
  }
  STAMP(2, 3);
}

extern "C" void kernel_launch(void* const* d_in, const int* in_sizes, int n_in,
                              void* d_out, int out_size, void* d_ws, size_t ws_size,
                              hipStream_t stream) {
  const u16* x = (const u16*)d_in[0];
  const u16* wqkv = (const u16*)d_in[1];
  const u16* wo = (const u16*)d_in[2];
  const u16* wg = (const u16*)d_in[4];
  const u16* wu = (const u16*)d_in[5];
  const u16* wd = (const u16*)d_in[6];
  char* ws = (char*)d_ws;
  u16* out = (u16*)d_out;

  hipFuncSetAttribute((const void*)k1_gemm, hipFuncAttributeMaxDynamicSharedMemorySize, K1_NS * K1_STAGE);
  hipFuncSetAttribute((const void*)k2_gemm, hipFuncAttributeMaxDynamicSharedMemorySize, K2_LDS);

  k0_prep<<<256, 256, 0, stream>>>(x, ws);
  k1_gemm<<<256, 512, K1_NS * K1_STAGE, stream>>>(wqkv, wg, wu, wo, wd, ws);
  k2_gemm<<<256, 512, K2_LDS, stream>>>(x, ws, out);
}
```

```cpp
#include <hip/hip_runtime.h>
#include <hip/hip_bf16.h>
#include <stdint.h>

typedef unsigned short u16;
typedef __attribute__((ext_vector_type(8))) short s16x8;
typedef __attribute__((ext_vector_type(4))) short s16x4;
typedef __attribute__((ext_vector_type(4))) float f32x4;
typedef __attribute__((ext_vector_type(16))) float f32x16;
typedef __attribute__((ext_vector_type(2))) unsigned u32x2;
typedef __attribute__((ext_vector_type(4))) unsigned u32x4;
typedef u32x4 u32x4_a8 __attribute__((aligned(8)));
typedef __attribute__((address_space(3))) s16x4* lds_s16x4_ptr;
typedef __attribute__((address_space(1))) unsigned gu32;
typedef __attribute__((address_space(1))) unsigned long long gu64;

#define RLX_AGENT __ATOMIC_RELAXED, __HIP_MEMORY_SCOPE_AGENT

constexpr int S_ = 2048, D_ = 640, F_ = 1728, NQKV_ = 1920;

constexpr size_t OFF_CNT   = 0;
constexpr size_t OFF_XN    = 8192;
constexpr size_t SZ_XN     = (size_t)S_ * D_ * 2;
constexpr size_t OFF_H     = OFF_XN + SZ_XN;
constexpr size_t SZ_H      = (size_t)S_ * F_ * 2;
constexpr size_t OFF_PQ    = OFF_H + SZ_H;
constexpr size_t SZ_PQ     = (size_t)S_ * D_ * 2;
constexpr size_t OFF_WC    = OFF_PQ + SZ_PQ;
constexpr size_t SZ_WC     = (size_t)8 * 54 * 5 * 1024;
constexpr size_t OFF_WO    = OFF_WC + SZ_WC;
constexpr size_t SZ_WO     = (size_t)8 * 4 * 5 * 5 * 1024;
constexpr size_t OFF_KVP   = OFF_WO + SZ_WO;
constexpr size_t SZ_KVP    = (size_t)16 * 32 * 504 * 4;
constexpr size_t OFF_KVF   = OFF_KVP + SZ_KVP;
constexpr size_t SZ_KVF    = 65536;

constexpr unsigned KV_EMPTY = 0xFFFFFFFFu;
__device__ __forceinline__ float bf2f(u16 v) { return __uint_as_float(((unsigned)v) << 16); }
__device__ __forceinline__ u16 f2bf(float f) { return __builtin_bit_cast(u16, (__bf16)f); }
__device__ __forceinline__ float bfr(float f) { return bf2f(f2bf(f)); }
__device__ __forceinline__ unsigned pack2(float a, float b) { return (unsigned)f2bf(a) | ((unsigned)f2bf(b) << 16); }
__device__ __forceinline__ float phi_f(float t) {
  float p = t > 0.f ? bfr(t + 1.0f) : bfr(__expf(t));
  return bfr(p + 1e-6f);
}
typedef __attribute__((ext_vector_type(2))) float f32x2;
typedef __attribute__((ext_vector_type(2))) __bf16 bf16x2;
__device__ __forceinline__ unsigned cvt2(f32x2 v) { return __builtin_bit_cast(unsigned, __builtin_convertvector(v, bf16x2)); }
__device__ __forceinline__ f32x2 unp2(unsigned u) { return (f32x2){__uint_as_float(u << 16), __uint_as_float(u & 0xffff0000u)}; }
__device__ __forceinline__ f32x2 bfr2(f32x2 v) { return unp2(cvt2(v)); }
__device__ __forceinline__ f32x2 lo2(f32x4 v) { return __builtin_shufflevector(v, v, 0, 1); }
__device__ __forceinline__ f32x2 hi2(f32x4 v) { return __builtin_shufflevector(v, v, 2, 3); }
__device__ __forceinline__ unsigned phi2(f32x2 a) {
  const f32x2 t = bfr2(a), m = t * 1.44269504088896340736f, p = t + 1.0f;
  f32x2 sel;
  sel[0] = t[0] > 0.f ? p[0] : __builtin_amdgcn_exp2f(m[0]);
  sel[1] = t[1] > 0.f ? p[1] : __builtin_amdgcn_exp2f(m[1]);
  return cvt2(bfr2(sel) + 1e-6f);
}
__device__ __forceinline__ f32x2 sigm2(f32x2 g) {
  const f32x2 m = g * -1.44269504088896340736f;
  f32x2 e; e[0] = __builtin_amdgcn_exp2f(m[0]); e[1] = __builtin_amdgcn_exp2f(m[1]);
  e = e + 1.0f;
  f32x2 r; r[0] = __builtin_amdgcn_rcpf(e[0]); r[1] = __builtin_amdgcn_rcpf(e[1]);
  return r;
}
__device__ __forceinline__ int aswz(int row) { int x = (row >> 2) & 3; return (((x ^ (x >> 1)) & 1) << 1) | (x >> 1); }

#define WAIT_V(n) asm volatile("s_waitcnt vmcnt(" #n ")" ::: "memory")
#define STAMP(kid, idx)
#define STAMPW(idx)
#define WAIT_L0 asm volatile("s_waitcnt lgkmcnt(0)" ::: "memory")
#define BAR __builtin_amdgcn_s_barrier()
#define SCHED __builtin_amdgcn_sched_barrier(0)
__device__ __forceinline__ float frcp(float v) { return __builtin_amdgcn_rcpf(v); }

__global__ __launch_bounds__(256) void k0_prep(const u16* __restrict__ x, char* __restrict__ ws) {
  const int tid = threadIdx.x;
  {
    { const int ci = blockIdx.x * 256 + tid;
      if (ci < (int)(SZ_KVP / 16)) *(u32x4*)(ws + OFF_KVP + (size_t)ci * 16) = (u32x4){KV_EMPTY, KV_EMPTY, KV_EMPTY, KV_EMPTY}; }
    const int row = blockIdx.x * 8 + (tid >> 5);
    const int l32 = tid & 31;
    const u16* xr = x + (size_t)row * D_;
    float v[20];
    float sum = 0.f;
#pragma unroll
    for (int i = 0; i < 5; ++i) {
      u32x2 w = *(const u32x2*)(xr + (l32 + 32 * i) * 4);
      v[4 * i + 0] = __uint_as_float(w[0] << 16);
      v[4 * i + 1] = __uint_as_float(w[0] & 0xffff0000u);
      v[4 * i + 2] = __uint_as_float(w[1] << 16);
      v[4 * i + 3] = __uint_as_float(w[1] & 0xffff0000u);
      sum += v[4 * i] + v[4 * i + 1] + v[4 * i + 2] + v[4 * i + 3];
    }
#pragma unroll
    for (int o = 16; o >= 1; o >>= 1) sum += __shfl_xor(sum, o);
    const float mean = sum * (1.0f / D_);
    float sq = 0.f;
#pragma unroll
    for (int i = 0; i < 20; ++i) { float d = v[i] - mean; sq += d * d; }
#pragma unroll
    for (int o = 16; o >= 1; o >>= 1) sq += __shfl_xor(sq, o);
    const float mb = bfr(mean);
    const float vb = bfr(sq * (1.0f / D_));
    const float sd = bfr(sqrtf(bfr(vb + 1e-5f)));
    u16* xn = (u16*)(ws + OFF_XN);
    const int mt = row >> 7, r = row & 127, sw = aswz(r);
#pragma unroll
    for (int i = 0; i < 5; ++i) {
      const int k = (l32 + 32 * i) * 4;
      float o0 = bfr(v[4 * i + 0] - mb) / sd, o1 = bfr(v[4 * i + 1] - mb) / sd;
      float o2 = bfr(v[4 * i + 2] - mb) / sd, o3 = bfr(v[4 * i + 3] - mb) / sd;
      u32x2 w; w[0] = pack2(o0, o1); w[1] = pack2(o2, o3);
      const int kt = k >> 5, ch = (k >> 3) & 3, hf = (k >> 2) & 1;
      size_t off = ((size_t)(mt * 20 + kt) * 128 + r) * 32 + ((ch ^ sw) << 3) + (hf << 2);
      *(u32x2*)(xn + off) = w;
    }
  }
}

#define P1NS 9
#define P2NS 5
constexpr int RT_WD = 216 * 80, RT_ITEMS = RT_WD + 80 * 80, RT_PB = 93;
constexpr int P1_STAGE = 13312, P1_NS = P1NS;
constexpr int P2_STAGE = 24576, P2_NS = P2NS;
constexpr int T_OFF = 122880;
constexpr int RED_OFF = T_OFF + 92 * 272;
constexpr int K1_LDS = RED_OFF + 8192 + 2048;

__global__ __launch_bounds__(512, 2) void k1_gemm(const u16* __restrict__ wqkv, const u16* __restrict__ wg,
                                                   const u16* __restrict__ wu, const u16* __restrict__ wo,
                                                   const u16* __restrict__ wdown, char* __restrict__ ws) {
  extern __shared__ __attribute__((aligned(16))) char smem[];
  const int bid = blockIdx.x;
  const int tid = threadIdx.x, lane = tid & 63;
  const int wave = __builtin_amdgcn_readfirstlane(tid >> 6);
  const int g = lane >> 4, c = lane & 15;
  const int mt = (bid & 1) * 8 + ((bid >> 3) & 7), nt = ((bid >> 1) & 3) * 4 + (bid >> 6);
#define WAITN(N) asm volatile("s_waitcnt vmcnt(%0)" :: "n"(N) : "memory")
#define SGB(m, n) __builtin_amdgcn_sched_group_barrier(m, n, 0)
  STAMP(1, 0);

  const char* asrc = ws + OFF_XN + (size_t)mt * 20 * 8192 + lane * 16;
  const int tq = (lane & 15) >> 2, tp = lane & 3;

  u32x4 rr[8]; char* rdst = ws; bool rdo = false;
  f32x4 acc1[2][5];
  if (wave >= 4) {
    const int l = tid - 256, t = bid * RT_PB + l;
    rdo = l < RT_PB && t < RT_ITEMS;
    if (rdo) {
      const bool isd = t < RT_WD;
      const int t2 = isd ? t : t - RT_WD;
      const int ko = t2 / 80, noct = t2 - ko * 80;
      const int n0 = noct * 8, ntt = n0 / 80, nn = n0 - ntt * 80, nf = nn >> 4, c0 = nn & 15, gg = ko & 3, kq = ko >> 2;
      const int wq = kq / 5, ks = kq - wq * 5;
      const u16* src = (isd ? wdown : wo) + n0;
#pragma unroll
      for (int jj = 0; jj < 8; ++jj) {
        const int q = 8 * ks + jj, hh = q / 5, rm = q - hh * 5;
        const int row = isd ? ko * 8 + jj : (wq * 8 + hh) * 20 + (rm < 4 ? 4 * gg + rm : 16 + gg);
        rr[jj] = *(const u32x4*)(src + (size_t)row * D_);
      }
      rdst = ws + (isd ? OFF_WC + (size_t)(ntt * 54 + kq) * 5120 : OFF_WO + (size_t)(ntt * 20 + kq) * 5120) + (size_t)(nf * 64 + gg * 16 + c0) * 16;
    }
#pragma unroll
    for (int kt = 0; kt < 20; ++kt) {
      BAR;
      if (kt == 12 && rdo) {
#pragma unroll
        for (int i = 0; i < 4; ++i) {
          u32x4 lo, hi;
#pragma unroll
          for (int q = 0; q < 4; ++q) {
            lo[q] = (rr[2 * q][i] & 0xffffu) | (rr[2 * q + 1][i] << 16);
            hi[q] = (rr[2 * q][i] >> 16) | (rr[2 * q + 1][i] & 0xffff0000u);
          }
          *(u32x4*)(rdst + (2 * i) * 16) = lo;
          *(u32x4*)(rdst + (2 * i + 1) * 16) = hi;
        }
      }
    }
  } else {
    const u16* b1[2];
#pragma unroll
    for (int t = 0; t < 2; ++t) {
      const int L = (t == 0 ? wave : 4) * 64 + lane, row = L / 10, col = (L - row * 10) * 8;
      b1[t] = wqkv + (size_t)row * NQKV_ + (col < 40 ? 640 + nt * 40 + col : 1280 + nt * 40 + (col - 40));
    }
    const bool has4 = wave == 0;
    auto issue1 = [&](int kt) {
      char* sb = smem + (kt % P1_NS) * P1_STAGE;
      __builtin_amdgcn_global_load_lds((const unsigned*)(asrc + (size_t)kt * 8192 + wave * 1024), (unsigned*)(sb + wave * 1024), 16, 0, 0);
      __builtin_amdgcn_global_load_lds((const unsigned*)(asrc + (size_t)kt * 8192 + (wave + 4) * 1024), (unsigned*)(sb + (wave + 4) * 1024), 16, 0, 0);
      __builtin_amdgcn_global_load_lds((const unsigned*)(b1[0] + (size_t)kt * 32 * NQKV_), (unsigned*)(sb + 8192 + wave * 1024), 16, 0, 0);
      if (has4) __builtin_amdgcn_global_load_lds((const unsigned*)(b1[1] + (size_t)kt * 32 * NQKV_), (unsigned*)(sb + 8192 + 4096), 16, 0, 0);
    };
    const int xo1 = (wave * 32 + c) * 64 + ((g ^ aswz(c)) << 4);
    const int wo1 = 8192 + (8 * g + tq) * 160 + tp * 8;
    auto rd1 = [&](int kt, s16x8 (&xf)[2], s16x8 (&wf)[5]) {
      const char* sb = smem + (kt % P1_NS) * P1_STAGE;
#pragma unroll
      for (int b = 0; b < 5; ++b) {
        s16x4 lo = __builtin_amdgcn_ds_read_tr16_b64_v4i16((lds_s16x4_ptr)(sb + wo1 + b * 32));
        s16x4 hi = __builtin_amdgcn_ds_read_tr16_b64_v4i16((lds_s16x4_ptr)(sb + wo1 + b * 32 + 640));
        wf[b] = __builtin_shufflevector(lo, hi, 0, 1, 2, 3, 4, 5, 6, 7);
      }
#pragma unroll
      for (int a = 0; a < 2; ++a) xf[a] = *(const s16x8*)(sb + xo1 + a * 1024);
    };
    auto mm1 = [&](const s16x8 (&xf)[2], const s16x8 (&wf)[5]) {
#pragma unroll
      for (int a = 0; a < 2; ++a)
#pragma unroll
        for (int b = 0; b < 5; ++b) acc1[a][b] = __builtin_amdgcn_mfma_f32_16x16x32_bf16(xf[a], wf[b], acc1[a][b], 0, 0, 0);
    };
#define P1_WAIT(n) do { if (has4) WAITN(4 * (n)); else WAITN(3 * (n)); } while (0)
#pragma unroll
    for (int a = 0; a < 2; ++a)
#pragma unroll
      for (int b = 0; b < 5; ++b) acc1[a][b] = (f32x4){0.f, 0.f, 0.f, 0.f};
    s16x8 xA[2], wAf[5], xB[2], wBf[5];
#pragma unroll
    for (int i = 0; i < P1_NS; ++i) issue1(i);
    P1_WAIT(P1_NS - 1);
    BAR;
    rd1(0, xA, wAf);
    SCHED;
#pragma unroll
    for (int kt = 0; kt < 19; ++kt) {
      if (kt + P1_NS <= 20) P1_WAIT(P1_NS - 2); else P1_WAIT(18 - kt);
      WAIT_L0; BAR; SCHED;
      if (kt + P1_NS < 20) issue1(kt + P1_NS);
      if (kt & 1) { rd1(kt + 1, xA, wAf); mm1(xB, wBf); }
      else        { rd1(kt + 1, xB, wBf); mm1(xA, wAf); }
      if (kt + P1_NS < 20) { SGB(0x10, 1); SGB(0x8, 1); SGB(0x10, 1); SGB(0x8, 1); SGB(0x10, 2); SGB(0x8, 1); }
#pragma unroll
      for (int i = 0; i < 6; ++i) { SGB(0x100, 2); SGB(0x8, 1); }
      SGB(0x8, 4);
      SCHED;
    }
    mm1(xB, wBf);
  }
  WAIT_L0; BAR;
  STAMP(1, 1);

  const int wm = wave >> 2, wn = wave & 3;
  const u16* bsrc[2]; unsigned bstr[2];
#pragma unroll
  for (int t = 0; t < 2; ++t) {
    const int ii = wave + 8 * t, cb = ii >> 2, krow = (ii & 3) * 8 + (lane >> 3), u = lane & 7;
    const int sk = ((krow >> 1) & 1) | (((krow >> 3) & 1) << 1);
    const int lc = ((u >> 1) ^ sk) * 16 + (u & 1) * 8, unit = nt * 4 + cb;
    const bool isq = unit < 10;
    const int ldw = isq ? NQKV_ : F_;
    const u16* p = isq ? wqkv + unit * 64 + lc : (lc < 32 ? wg + (unit - 10) * 32 + lc : wu + (unit - 10) * 32 + (lc - 32));
    bsrc[t] = p + (size_t)krow * ldw;
    bstr[t] = 32u * (unsigned)ldw;
  }
  auto issue2 = [&](int kt) {
    char* sb = smem + (kt % P2_NS) * P2_STAGE;
    __builtin_amdgcn_global_load_lds((const unsigned*)(asrc + (size_t)kt * 8192 + wave * 1024), (unsigned*)(sb + wave * 1024), 16, 0, 0);
    __builtin_amdgcn_global_load_lds((const unsigned*)(bsrc[0] + (size_t)kt * bstr[0]), (unsigned*)(sb + 8192 + wave * 1024), 16, 0, 0);
    __builtin_amdgcn_global_load_lds((const unsigned*)(bsrc[1] + (size_t)kt * bstr[1]), (unsigned*)(sb + 8192 + (wave + 8) * 1024), 16, 0, 0);
  };
#pragma unroll
  for (int i = 0; i < P2_NS; ++i) issue2(i);
  SCHED;

  if (wave < 4) {
#pragma unroll
    for (int a = 0; a < 2; ++a)
#pragma unroll
      for (int b = 0; b < 5; ++b) {
        u32x2 w;
        if (b < 2) { w[0] = phi2(lo2(acc1[a][b])); w[1] = phi2(hi2(acc1[a][b])); }
        else if (b == 2) {
          w[0] = c < 8 ? phi2(lo2(acc1[a][b])) : cvt2(lo2(acc1[a][b]));
          w[1] = c < 8 ? phi2(hi2(acc1[a][b])) : cvt2(hi2(acc1[a][b]));
        } else { w[0] = cvt2(lo2(acc1[a][b])); w[1] = cvt2(hi2(acc1[a][b])); }
        *(u32x2*)(smem + T_OFF + (b * 16 + c) * 272 + (wave * 32 + a * 16 + 4 * g) * 2) = w;
      }
  }
  WAIT_L0; BAR;
  if (wave < 2) {
    const int r32 = lane & 31, hh = lane >> 5;
    f32x16 akv, az;
#pragma unroll
    for (int i = 0; i < 16; ++i) { akv[i] = 0.f; az[i] = 0.f; }
    s16x8 ones;
#pragma unroll
    for (int i = 0; i < 8; ++i) ones[i] = (short)0x3F80;
#pragma unroll
    for (int s = 0; s < 8; ++s) {
      s16x8 af = *(const s16x8*)(smem + T_OFF + (20 * wave + r32) * 272 + (16 * s + 8 * hh) * 2);
      s16x8 bfv = *(const s16x8*)(smem + T_OFF + (40 + 20 * wave + r32) * 272 + (16 * s + 8 * hh) * 2);
      akv = __builtin_amdgcn_mfma_f32_32x32x16_bf16(af, bfv, akv, 0, 0, 0);
      az = __builtin_amdgcn_mfma_f32_32x32x16_bf16(af, ones, az, 0, 0, 0);
    }
    float* P = (float*)(ws + OFF_KVP) + (size_t)(mt * 32 + nt * 2 + wave) * 504;
    if (r32 <= 20) {
#pragma unroll
      for (int q = 0; q < 3; ++q) {
        const int d0 = 8 * q + 4 * hh;
        if (d0 < 20) {
          f32x4 val;
#pragma unroll
          for (int r = 0; r < 4; ++r) {
            const unsigned u = __float_as_uint((r32 == 20) ? az[4 * q + r] : akv[4 * q + r]);
            val[r] = __uint_as_float(u == KV_EMPTY ? 0x7FC00000u : u);
          }
          asm volatile("global_store_dwordx4 %0, %1, off sc1" :: "v"(P + r32 * 24 + d0), "v"(val) : "memory");
        }
      }
    }
  }
  SCHED;

  const int xoff = (wm * 64 + c) * 64 + ((g ^ aswz(c)) << 4);
  int woff[4];
  {
    const int s = (tq >> 1) | ((g & 1) << 1);
    const int lo = (8 * g + tq) * 128 + (s << 5) + tp * 8;
#pragma unroll
    for (int i = 0; i < 4; ++i) woff[i] = 8192 + wn * 4096 + (lo ^ (i << 5));
  }
  f32x4 acc[4][4];
#pragma unroll
  for (int a = 0; a < 4; ++a)
#pragma unroll
    for (int b = 0; b < 4; ++b) acc[a][b] = (f32x4){0.f, 0.f, 0.f, 0.f};
  auto readfr = [&](int kt, s16x8 (&xf)[4], s16x8 (&wf)[4]) {
    const char* sb = smem + (kt % P2_NS) * P2_STAGE;
#pragma unroll
    for (int b = 0; b < 4; ++b) {
      s16x4 lo = __builtin_amdgcn_ds_read_tr16_b64_v4i16((lds_s16x4_ptr)(sb + woff[b]));
      s16x4 hi = __builtin_amdgcn_ds_read_tr16_b64_v4i16((lds_s16x4_ptr)(sb + woff[b] + 512));
      wf[b] = __builtin_shufflevector(lo, hi, 0, 1, 2, 3, 4, 5, 6, 7);
    }
#pragma unroll
    for (int a = 0; a < 4; ++a) xf[a] = *(const s16x8*)(sb + xoff + a * 1024);
  };
  auto mma = [&](const s16x8 (&xf)[4], const s16x8 (&wf)[4]) {
#pragma unroll
    for (int a = 0; a < 4; ++a)
#pragma unroll
      for (int b = 0; b < 4; ++b) acc[a][b] = __builtin_amdgcn_mfma_f32_16x16x32_bf16(wf[b], xf[a], acc[a][b], 0, 0, 0);
  };
  {
    s16x8 xA[4], wAf[4], xB[4], wBf[4];
    WAITN(3 * (P2_NS - 1));
    BAR;
    readfr(0, xA, wAf);
    SCHED;
#pragma unroll
    for (int kt = 0; kt < 19; ++kt) {
      if (kt + P2_NS <= 20) WAITN(3 * (P2_NS - 2)); else WAITN(3 * (18 - kt));
      WAIT_L0; BAR; SCHED;
      if (kt + P2_NS < 20) issue2(kt + P2_NS);
      if (kt & 1) { readfr(kt + 1, xA, wAf); mma(xB, wBf); }
      else        { readfr(kt + 1, xB, wBf); mma(xA, wAf); }
      if (kt + P2_NS < 20) { SGB(0x10, 1); SGB(0x8, 1); SGB(0x10, 1); SGB(0x8, 1); SGB(0x10, 1); SGB(0x8, 1); }
#pragma unroll
      for (int i = 0; i < 12; ++i) { SGB(0x100, 1); SGB(0x8, 1); }
      SGB(0x8, 4);
      SCHED;
    }
    mma(xB, wBf);
  }
  STAMP(1, 2);

  const bool red_blk = nt < 2;
  const int hd = mt * 2 + nt, ci = tid & 127, pg = tid >> 7;
  const bool chk = ci < 126 && (ci % 6) != 5;
  const unsigned* psrc = (const unsigned*)(ws + OFF_KVP) + ((size_t)(4 * pg) * 32 + hd) * 504 + (ci < 126 ? ci : 0) * 4;
  u32x4 pv[4];
  if (red_blk) {
#pragma unroll
    for (int m = 0; m < 4; ++m)
#pragma unroll
      for (int i = 0; i < 4; ++i) pv[m][i] = __hip_atomic_load((const gu32*)(psrc + m * 32 * 504 + i), RLX_AGENT);
  }

  {
    const int unit = nt * 4 + wn;
    if (unit >= 10) {
      char* H = ws + OFF_H + ((size_t)(mt * 8 + wm * 4) * 54 + (unit - 10)) * 1024 + c * 16 + (g >> 1) * 256 + (g & 1) * 8;
#pragma unroll
      for (int a = 0; a < 4; ++a)
#pragma unroll
        for (int b = 0; b < 2; ++b) {
          u32x2 w;
          w[0] = cvt2(bfr2(sigm2(bfr2(lo2(acc[a][b])))) * bfr2(lo2(acc[a][b + 2])));
          w[1] = cvt2(bfr2(sigm2(bfr2(hi2(acc[a][b])))) * bfr2(hi2(acc[a][b + 2])));
          *(u32x2*)(H + (size_t)a * 54 * 1024 + b * 512) = w;
        }
    } else {
      char* PQ = ws + OFF_PQ + (size_t)(mt * 8 + wm * 4) * 32 * 640;
#pragma unroll
      for (int b = 0; b < 4; ++b) {
        const int n = unit * 64 + b * 16 + 4 * g, hl = n / 20, d0 = n - hl * 20;
        const int ro = hl * 640 + (d0 < 16 ? (d0 >> 3) * 256 + c * 16 + (d0 & 7) * 2 : 512 + c * 8);
#pragma unroll
        for (int a = 0; a < 4; ++a) {
          u32x2 w; w[0] = phi2(lo2(acc[a][b])); w[1] = phi2(hi2(acc[a][b]));
          *(u32x2*)(PQ + (size_t)a * 20480 + ro) = w;
        }
      }
    }
  }

  if (red_blk) {
    bool bad = false;
#pragma unroll
    for (int m = 0; m < 4; ++m)
#pragma unroll
      for (int i = 0; i < 4; ++i) bad = bad || pv[m][i] == KV_EMPTY;
    while (__builtin_amdgcn_ballot_w64(bad && chk) != 0ull) {
      bad = false;
#pragma unroll
      for (int m = 0; m < 4; ++m)
#pragma unroll
        for (int i = 0; i < 4; ++i) { pv[m][i] = __hip_atomic_load((const gu32*)(psrc + m * 32 * 504 + i), RLX_AGENT); bad = bad || pv[m][i] == KV_EMPTY; }
    }
    f32x4* red = (f32x4*)(smem + RED_OFF);
    red[pg * 128 + ci] = ((__builtin_bit_cast(f32x4, pv[0]) + __builtin_bit_cast(f32x4, pv[1])) + __builtin_bit_cast(f32x4, pv[2])) + __builtin_bit_cast(f32x4, pv[3]);
    WAIT_L0; BAR;
    float* kvs = (float*)(smem + RED_OFF + 8192);
    if (tid < 126) *(f32x4*)(kvs + tid * 4) = ((red[tid] + red[128 + tid]) + red[256 + tid]) + red[384 + tid];
    WAIT_L0; BAR;
    if (wave < 2) {
      const int fr = wave;
      const int e = fr == 0 ? c : ((c & 3) == 0 ? 16 + (c >> 2) : ((c & 3) == 1 ? 20 : 99)), d0 = 8 * g;
      const bool act = (e <= 20 && d0 < 20), act1 = act && (d0 < 16);
      const float* sp = kvs + (act ? e * 24 + d0 : 0);
      f32x4 w0 = *(const f32x4*)sp, w1 = *(const f32x4*)(sp + (act1 ? 4 : 0));
      u32x4 w;
      w[0] = act ? pack2(w0[0], w0[1]) : 0u; w[1] = act ? pack2(w0[2], w0[3]) : 0u;
      w[2] = act1 ? pack2(w1[0], w1[1]) : 0u; w[3] = act1 ? pack2(w1[2], w1[3]) : 0u;
      *(u32x4*)(ws + OFF_KVF + ((size_t)(hd * 2 + fr) * 64 + lane) * 16) = w;
    }
  }
  STAMP(1, 3);
}

constexpr int K2_LDS = 163840;
#define K2DD 3
constexpr int K2_D = K2DD;
#define K2DW 3
constexpr int K2_DW = K2DW;

__global__ __launch_bounds__(512, 2) void k2_gemm(const u16* __restrict__ x, const char* __restrict__ ws, u16* __restrict__ out) {
  extern __shared__ __attribute__((aligned(16))) char smem[];
  const int tid = threadIdx.x, lane = tid & 63;
  const int wave = __builtin_amdgcn_readfirstlane(tid >> 6);
  const int wq = wave & 3;
  const int g = lane >> 4, c = lane & 15;
  const int bid = blockIdx.x;
  const int mt = (bid & 7) * 4 + (bid >> 6), nt = (bid >> 3) & 7;

  STAMP(2, 0);
  f32x4 acc[4][5];
#pragma unroll
  for (int a = 0; a < 4; ++a)
#pragma unroll
    for (int b = 0; b < 5; ++b) acc[a][b] = (f32x4){0.f, 0.f, 0.f, 0.f};
  const int b0 = wave < 4 ? 0 : 3, nb = wave < 4 ? 3 : 2;
  const int tok = mt * 64 + wq * 16 + c;
  u32x2 xw[3];
#pragma unroll
  for (int i = 0; i < 3; ++i) xw[i] = *(const u32x2*)(x + (size_t)tok * D_ + nt * 80 + (i < nb ? b0 + i : b0) * 16 + 4 * g);

  if (wave < 4) {
    const char* hsrc = ws + OFF_H + ((size_t)(mt * 4) * 54 + wq) * 1024 + lane * 16;
    const char* wsrc = ws + OFF_WC + ((size_t)nt * 54 + wq) * 5120 + lane * 16;
    s16x8 xr[K2_D][4], wr[K2_D][5];
    auto ld = [&](int slot, int i) {
#pragma unroll
      for (int b = 0; b < 5; ++b) wr[slot][b] = *(const s16x8*)(wsrc + (size_t)(20 * i + b) * 1024);
#pragma unroll
      for (int a = 0; a < 4; ++a) xr[slot][a] = *(const s16x8*)(hsrc + (size_t)(a * 54 + 4 * i) * 1024);
    };
    auto mma1 = [&](int slot) {
#pragma unroll
      for (int a = 0; a < 4; ++a)
#pragma unroll
        for (int b = 0; b < 5; ++b) acc[a][b] = __builtin_amdgcn_mfma_f32_16x16x32_bf16(wr[slot][b], xr[slot][a], acc[a][b], 0, 0, 0);
    };
#pragma unroll
    for (int i = 0; i < K2_D; ++i) ld(i, i);
    SCHED;
#pragma unroll
    for (int i = 0; i < 13; ++i) {
      mma1(i % K2_D);
      if (i + K2_D < 13) ld(i % K2_D, i + K2_D);
      else if (i + K2_D == 13) { if (wave < 2) ld(i % K2_D, 13); }
      SCHED;
    }
    if (wave < 2) mma1(13 % K2_D);
    STAMP(2, 1);
  } else {
    __builtin_amdgcn_s_setprio(3);
    char* reg = smem + wq * 36864;
    {
      const char* pqg = ws + OFF_PQ + ((size_t)(mt * 4) * 32 + wq * 8) * 640 + lane * 16;
      const char* kg = ws + OFF_KVF + (size_t)(wq * 8) * 2048 + lane * 16;
#pragma unroll
      for (int i = 0; i < 16; ++i) __builtin_amdgcn_global_load_lds((const unsigned*)(kg + i * 1024), (unsigned*)(reg + 20480 + i * 1024), 16, 0, 0);
#pragma unroll
      for (int tg = 0; tg < 4; ++tg)
#pragma unroll
        for (int i = 0; i < 5; ++i)
          __builtin_amdgcn_global_load_lds((const unsigned*)(pqg + tg * 20480 + i * 1024), (unsigned*)(reg + tg * 5120 + i * 1024), 16, 0, 0);
    }
    const char* wosrc = ws + OFF_WO + (size_t)(nt * 4 + wq) * 25600 + lane * 16;
    s16x8 w2[K2_DW][5];
    auto ldW = [&](int s) {
#pragma unroll
      for (int b = 0; b < 5; ++b) w2[s % K2_DW][b] = *(const s16x8*)(wosrc + (s * 5 + b) * 1024);
    };
#pragma unroll
    for (int s = 0; s < K2_DW; ++s) ldW(s);
    SCHED;
    __builtin_amdgcn_s_waitcnt(0x0F70);
    SCHED;
    STAMPW(4);
    const char* pql = reg + (g < 2 ? g * 256 + c * 16 : 512 + c * 8);
    const char* kvl = reg + 20480 + lane * 16;
    const f32x4 z4 = (f32x4){0.f, 0.f, 0.f, 0.f};
    unsigned ow[4][20];
    auto mma2 = [&](int s) {
#pragma unroll
      for (int a = 0; a < 4; ++a) {
        const u32x4 o4 = (u32x4){ow[a][4 * s], ow[a][4 * s + 1], ow[a][4 * s + 2], ow[a][4 * s + 3]};
        const s16x8 of = __builtin_bit_cast(s16x8, o4);
#pragma unroll
        for (int b = 0; b < 5; ++b) acc[a][b] = __builtin_amdgcn_mfma_f32_16x16x32_bf16(w2[s % K2_DW][b], of, acc[a][b], 0, 0, 0);
      }
      if (s + K2_DW < 5) ldW(s + K2_DW);
    };
#pragma unroll
    for (int m = 0; m < 4; ++m) {
      f32x4 ov[2][4]; float oe[2][4];
#pragma unroll
      for (int h2 = 0; h2 < 2; ++h2) {
        const int hh = 2 * m + h2;
        const s16x8 k0 = *(const s16x8*)(kvl + hh * 2048), k1 = *(const s16x8*)(kvl + hh * 2048 + 1024);
#pragma unroll
        for (int tg = 0; tg < 4; ++tg) {
          const u32x4 pq = *(const u32x4_a8*)(pql + tg * 5120 + hh * 640);
          u32x4 pk4;
          pk4[0] = g < 3 ? pq[0] : 0u; pk4[1] = g < 3 ? pq[1] : 0u;
          pk4[2] = g < 2 ? pq[2] : 0u; pk4[3] = g < 2 ? pq[3] : 0u;
          const s16x8 pf = __builtin_bit_cast(s16x8, pk4);
          const f32x4 d1 = __builtin_amdgcn_mfma_f32_16x16x32_bf16(k1, pf, z4, 0, 0, 0);
          const f32x4 d0 = __builtin_amdgcn_mfma_f32_16x16x32_bf16(k0, pf, z4, 0, 0, 0);
          const float rden = frcp(bfr(bfr(d1[1]) + 1e-6f));
          const f32x2 lo = bfr2(lo2(d0)) * rden, hi = bfr2(hi2(d0)) * rden;
          ov[h2][tg] = (f32x4){lo[0], lo[1], hi[0], hi[1]};
          oe[h2][tg] = bfr(d1[0]) * rden;
        }
      }
#pragma unroll
      for (int tg = 0; tg < 4; ++tg) {
        ow[tg][5 * m + 0] = cvt2(lo2(ov[0][tg]));
        ow[tg][5 * m + 1] = cvt2(hi2(ov[0][tg]));
        ow[tg][5 * m + 2] = cvt2((f32x2){oe[0][tg], ov[1][tg][0]});
        ow[tg][5 * m + 3] = cvt2((f32x2){ov[1][tg][1], ov[1][tg][2]});
        ow[tg][5 * m + 4] = cvt2((f32x2){ov[1][tg][3], oe[1][tg]});
      }
      mma2(m);
      if (m == 3) mma2(4);
      SCHED;
      if (m < 2) STAMPW(5 + m);
    }
  }
  __syncthreads();

  f32x4* red = (f32x4*)smem;
#pragma unroll
  for (int a = 0; a < 4; ++a)
#pragma unroll
    for (int b = 0; b < 5; ++b) red[(wave * 20 + a * 5 + b) * 64 + lane] = acc[a][b];
  __syncthreads();
#pragma unroll
  for (int i = 0; i < 3; ++i) {
    if (i < nb) {
      const int f = wq * 5 + b0 + i;
      f32x4 y1 = red[(0 * 20 + f) * 64 + lane], y2 = red[(4 * 20 + f) * 64 + lane];
#pragma unroll
      for (int sw = 1; sw < 4; ++sw) { y1 += red[(sw * 20 + f) * 64 + lane]; y2 += red[((4 + sw) * 20 + f) * 64 + lane]; }
      const int n = nt * 80 + (b0 + i) * 16 + 4 * g;
      u32x2 w;
      {
        const f32x2 y = bfr2(lo2(y1)), gs = bfr2(y * sigm2(y)), sv = bfr2(lo2(y2));
        w[0] = cvt2(unp2(xw[i][0]) + bfr2(sv * gs));
      }
      {
        const f32x2 y = bfr2(hi2(y1)), gs = bfr2(y * sigm2(y)), sv = bfr2(hi2(y2));
        w[1] = cvt2(unp2(xw[i][1]) + bfr2(sv * gs));
      }
      *(u32x2*)(out + (size_t)tok * D_ + n) = w;
    }
  }
  STAMP(2, 3);
}

extern "C" void kernel_launch(void* const* d_in, const int* in_sizes, int n_in,
                              void* d_out, int out_size, void* d_ws, size_t ws_size,
                              hipStream_t stream) {
  const u16* x = (const u16*)d_in[0];
  const u16* wqkv = (const u16*)d_in[1];
  const u16* wo = (const u16*)d_in[2];
  const u16* wg = (const u16*)d_in[4];
  const u16* wu = (const u16*)d_in[5];
  const u16* wd = (const u16*)d_in[6];
  char* ws = (char*)d_ws;
  u16* out = (u16*)d_out;

  hipFuncSetAttribute((const void*)k1_gemm, hipFuncAttributeMaxDynamicSharedMemorySize, K1_LDS);
  hipFuncSetAttribute((const void*)k2_gemm, hipFuncAttributeMaxDynamicSharedMemorySize, K2_LDS);

  k0_prep<<<256, 256, 0, stream>>>(x, ws);
  k1_gemm<<<256, 512, K1_LDS, stream>>>(wqkv, wg, wu, wo, wd, ws);
  k2_gemm<<<256, 512, K2_LDS, stream>>>(x, ws, out);
}
```

```cpp
#include <hip/hip_runtime.h>
#include <hip/hip_bf16.h>
#include <stdint.h>

typedef unsigned short u16;
typedef __attribute__((ext_vector_type(8))) short s16x8;
typedef __attribute__((ext_vector_type(4))) short s16x4;
typedef __attribute__((ext_vector_type(4))) float f32x4;
typedef __attribute__((ext_vector_type(16))) float f32x16;
typedef __attribute__((ext_vector_type(2))) unsigned u32x2;
typedef __attribute__((ext_vector_type(4))) unsigned u32x4;
typedef u32x4 u32x4_a8 __attribute__((aligned(8)));
typedef __attribute__((address_space(3))) s16x4* lds_s16x4_ptr;
typedef __attribute__((address_space(1))) unsigned gu32;
typedef __attribute__((address_space(1))) unsigned long long gu64;

#define RLX_AGENT __ATOMIC_RELAXED, __HIP_MEMORY_SCOPE_AGENT

constexpr int S_ = 2048, D_ = 640, F_ = 1728, NQKV_ = 1920;

constexpr size_t OFF_CNT   = 0;
constexpr size_t OFF_XN    = 8192;
constexpr size_t SZ_XN     = (size_t)S_ * D_ * 2;
constexpr size_t OFF_H     = OFF_XN + SZ_XN;
constexpr size_t SZ_H      = (size_t)S_ * F_ * 2;
constexpr size_t OFF_PQ    = OFF_H + SZ_H;
constexpr size_t SZ_PQ     = (size_t)S_ * D_ * 2;
constexpr size_t OFF_WC    = OFF_PQ + SZ_PQ;
constexpr size_t SZ_WC     = (size_t)8 * 54 * 5 * 1024;
constexpr size_t OFF_WO    = OFF_WC + SZ_WC;
constexpr size_t SZ_WO     = (size_t)8 * 4 * 5 * 5 * 1024;
constexpr size_t OFF_KVP   = OFF_WO + SZ_WO;
constexpr size_t SZ_KVP    = (size_t)16 * 32 * 504 * 4;
constexpr size_t OFF_KVF   = OFF_KVP + SZ_KVP;
constexpr size_t SZ_KVF    = 65536;

constexpr unsigned KV_EMPTY = 0xFFFFFFFFu;
__device__ __forceinline__ float bf2f(u16 v) { return __uint_as_float(((unsigned)v) << 16); }
__device__ __forceinline__ u16 f2bf(float f) { return __builtin_bit_cast(u16, (__bf16)f); }
__device__ __forceinline__ float bfr(float f) { return bf2f(f2bf(f)); }
__device__ __forceinline__ unsigned pack2(float a, float b) { return (unsigned)f2bf(a) | ((unsigned)f2bf(b) << 16); }
__device__ __forceinline__ float phi_f(float t) {
  float p = t > 0.f ? bfr(t + 1.0f) : bfr(__expf(t));
  return bfr(p + 1e-6f);
}
typedef __attribute__((ext_vector_type(2))) float f32x2;
typedef __attribute__((ext_vector_type(2))) __bf16 bf16x2;
__device__ __forceinline__ unsigned cvt2(f32x2 v) { return __builtin_bit_cast(unsigned, __builtin_convertvector(v, bf16x2)); }
__device__ __forceinline__ f32x2 unp2(unsigned u) { return (f32x2){__uint_as_float(u << 16), __uint_as_float(u & 0xffff0000u)}; }
__device__ __forceinline__ f32x2 bfr2(f32x2 v) { return unp2(cvt2(v)); }
__device__ __forceinline__ f32x2 lo2(f32x4 v) { return __builtin_shufflevector(v, v, 0, 1); }
__device__ __forceinline__ f32x2 hi2(f32x4 v) { return __builtin_shufflevector(v, v, 2, 3); }
__device__ __forceinline__ unsigned phi2(f32x2 a) {
  const f32x2 t = bfr2(a), m = t * 1.44269504088896340736f, p = t + 1.0f;
  f32x2 sel;
  sel[0] = t[0] > 0.f ? p[0] : __builtin_amdgcn_exp2f(m[0]);
  sel[1] = t[1] > 0.f ? p[1] : __builtin_amdgcn_exp2f(m[1]);
  return cvt2(bfr2(sel) + 1e-6f);
}
__device__ __forceinline__ f32x2 sigm2(f32x2 g) {
  const f32x2 m = g * -1.44269504088896340736f;
  f32x2 e; e[0] = __builtin_amdgcn_exp2f(m[0]); e[1] = __builtin_amdgcn_exp2f(m[1]);
  e = e + 1.0f;
  f32x2 r; r[0] = __builtin_amdgcn_rcpf(e[0]); r[1] = __builtin_amdgcn_rcpf(e[1]);
  return r;
}
__device__ __forceinline__ int aswz(int row) { int x = (row >> 2) & 3; return (((x ^ (x >> 1)) & 1) << 1) | (x >> 1); }

#define WAIT_V(n) asm volatile("s_waitcnt vmcnt(" #n ")" ::: "memory")
#define STAMP(kid, idx)
#define STAMPW(idx)
#define WAIT_L0 asm volatile("s_waitcnt lgkmcnt(0)" ::: "memory")
#define BAR __builtin_amdgcn_s_barrier()
#define SCHED __builtin_amdgcn_sched_barrier(0)
__device__ __forceinline__ float frcp(float v) { return __builtin_amdgcn_rcpf(v); }

constexpr int RT_WD = 216 * 80, RT_ITEMS = RT_WD + 80 * 80, RT_R = 3;
__device__ __forceinline__ void retile_all(int t0, const u16* __restrict__ wo, const u16* __restrict__ wdown, char* __restrict__ ws) {
  u32x4 r[RT_R][8];
  char* dst[RT_R];
#pragma unroll
  for (int it = 0; it < RT_R; ++it) {
    int t = t0 + it * 8192; if (t >= RT_ITEMS) t = t0;
    const bool isd = t < RT_WD;
    const int t2 = isd ? t : t - RT_WD;
    const int ko = t2 / 80, noct = t2 - ko * 80;
    const int n0 = noct * 8, nt = n0 / 80, nn = n0 - nt * 80, nf = nn >> 4, c0 = nn & 15, gg = ko & 3, kq = ko >> 2;
    const int wq = kq / 5, ks = kq - wq * 5;
    const u16* src = (isd ? wdown : wo) + n0;
#pragma unroll
    for (int jj = 0; jj < 8; ++jj) {
      const int q = 8 * ks + jj, hh = q / 5, rr = q - hh * 5;
      const int row = isd ? ko * 8 + jj : (wq * 8 + hh) * 20 + (rr < 4 ? 4 * gg + rr : 16 + gg);
      r[it][jj] = *(const u32x4*)(src + (size_t)row * D_);
    }
    dst[it] = ws + (isd ? OFF_WC + (size_t)(nt * 54 + kq) * 5120 : OFF_WO + (size_t)(nt * 20 + kq) * 5120) + (size_t)(nf * 64 + gg * 16 + c0) * 16;
  }
#pragma unroll
  for (int it = 0; it < RT_R; ++it) {
#pragma unroll
    for (int i = 0; i < 4; ++i) {
      u32x4 lo, hi;
#pragma unroll
      for (int q = 0; q < 4; ++q) {
        lo[q] = (r[it][2 * q][i] & 0xffffu) | (r[it][2 * q + 1][i] << 16);
        hi[q] = (r[it][2 * q][i] >> 16) | (r[it][2 * q + 1][i] & 0xffff0000u);
      }
      *(u32x4*)(dst[it] + (2 * i) * 16) = lo;
      *(u32x4*)(dst[it] + (2 * i + 1) * 16) = hi;
    }
  }
}

__global__ __launch_bounds__(256) void k0_prep(const u16* __restrict__ x, char* __restrict__ ws, const u16* __restrict__ wqkv,
                                               const u16* __restrict__ wg, const u16* __restrict__ wu) {
  const int tid = threadIdx.x;
  if (blockIdx.x >= 256) {
    const int t = (blockIdx.x - 256) * 256 + tid;
    unsigned v0, v1;
    const int l1 = t + 65536 < 107520 ? t + 65536 : t;
    const char* p0 = t < 38400 ? (const char*)wqkv + (size_t)t * 64 : (const char*)wg + (size_t)(t - 38400) * 64;
    const char* p1 = l1 < 38400 ? (const char*)wqkv + (size_t)l1 * 64 : (l1 < 72960 ? (const char*)wg + (size_t)(l1 - 38400) * 64 : (const char*)wu + (size_t)(l1 - 72960) * 64);
    asm volatile("global_load_dword %0, %2, off\n\tglobal_load_dword %1, %3, off\n\ts_waitcnt vmcnt(0)" : "=&v"(v0), "=&v"(v1) : "v"(p0), "v"(p1) : "memory");
    return;
  }
  {
    { const int ci = blockIdx.x * 256 + tid;
      if (ci < (int)(SZ_KVP / 16)) *(u32x4*)(ws + OFF_KVP + (size_t)ci * 16) = (u32x4){KV_EMPTY, KV_EMPTY, KV_EMPTY, KV_EMPTY}; }
    const int row = blockIdx.x * 8 + (tid >> 5);
    const int l32 = tid & 31;
    const u16* xr = x + (size_t)row * D_;
    float v[20];
    float sum = 0.f;
#pragma unroll
    for (int i = 0; i < 5; ++i) {
      u32x2 w = *(const u32x2*)(xr + (l32 + 32 * i) * 4);
      v[4 * i + 0] = __uint_as_float(w[0] << 16);
      v[4 * i + 1] = __uint_as_float(w[0] & 0xffff0000u);
      v[4 * i + 2] = __uint_as_float(w[1] << 16);
      v[4 * i + 3] = __uint_as_float(w[1] & 0xffff0000u);
      sum += v[4 * i] + v[4 * i + 1] + v[4 * i + 2] + v[4 * i + 3];
    }
#pragma unroll
    for (int o = 16; o >= 1; o >>= 1) sum += __shfl_xor(sum, o);
    const float mean = sum * (1.0f / D_);
    float sq = 0.f;
#pragma unroll
    for (int i = 0; i < 20; ++i) { float d = v[i] - mean; sq += d * d; }
#pragma unroll
    for (int o = 16; o >= 1; o >>= 1) sq += __shfl_xor(sq, o);
    const float mb = bfr(mean);
    const float vb = bfr(sq * (1.0f / D_));
    const float sd = bfr(sqrtf(bfr(vb + 1e-5f)));
    u16* xn = (u16*)(ws + OFF_XN);
    const int mt = row >> 7, r = row & 127, sw = aswz(r);
#pragma unroll
    for (int i = 0; i < 5; ++i) {
      const int k = (l32 + 32 * i) * 4;
      float o0 = bfr(v[4 * i + 0] - mb) / sd, o1 = bfr(v[4 * i + 1] - mb) / sd;
      float o2 = bfr(v[4 * i + 2] - mb) / sd, o3 = bfr(v[4 * i + 3] - mb) / sd;
      u32x2 w; w[0] = pack2(o0, o1); w[1] = pack2(o2, o3);
      const int kt = k >> 5, ch = (k >> 3) & 3, hf = (k >> 2) & 1;
      size_t off = ((size_t)(mt * 20 + kt) * 128 + r) * 32 + ((ch ^ sw) << 3) + (hf << 2);
      *(u32x2*)(xn + off) = w;
    }
  }
}

constexpr int K1_STAGE = 32768;
#define K1NS 4
constexpr int K1_NS = K1NS;

template <int TYPE>
__device__ __forceinline__ void k1_body(const u16* __restrict__ wA, const u16* __restrict__ wB, char* __restrict__ ws,
                                        int mt, int j, char* smem) {
  constexpr int NCB = TYPE == 0 ? 6 : 5;
  constexpr int NFR = TYPE == 0 ? 6 : 5;
  constexpr int LDW = TYPE == 0 ? F_ : NQKV_;
  const int tid = threadIdx.x, lane = tid & 63;
  const int wave = __builtin_amdgcn_readfirstlane(tid >> 6);
  const int wm = wave >> 2, wn = (TYPE == 2 && wave >= 4) ? ((wave + 2) & 3) : (wave & 3);
  const int g = lane >> 4, c = lane & 15;

  const char* asrc = ws + OFF_XN + (size_t)mt * 20 * 8192 + wave * 1024 + lane * 16;
  const u16* bsrc[3];
#pragma unroll
  for (int t = 0; t < 3; ++t) {
    int ii = wave + 8 * t; if (ii >= NCB * 4) ii = wave;
    const int cb = ii >> 2, krow = (ii & 3) * 8 + (lane >> 3), u = lane & 7;
    const int sk = ((krow >> 1) & 1) | (((krow >> 3) & 1) << 1);
    const int ch = (u >> 1) ^ sk;
    const int lc = cb * 64 + ch * 16 + (u & 1) * 8;
    const u16* p;
    if (TYPE == 0) p = (lc < 192) ? (wA + j * 192 + lc) : (wB + j * 192 + (lc - 192));
    else if (TYPE == 1) p = wA + j * 320 + lc;
    else p = (lc < 160) ? (wA + 640 + j * 160 + lc) : (wA + 1280 + j * 160 + (lc - 160));
    bsrc[t] = p + (size_t)krow * LDW;
  }
  const bool has3 = (NCB == 6) || (wave < 4);

  auto issue = [&](int kt) {
    char* sb = smem + (kt % K1_NS) * K1_STAGE;
    __builtin_amdgcn_global_load_lds((const unsigned*)(asrc + (size_t)kt * 8192), (unsigned*)(sb + wave * 1024), 16, 0, 0);
    __builtin_amdgcn_global_load_lds((const unsigned*)(bsrc[0] + (size_t)kt * 32 * LDW), (unsigned*)(sb + 8192 + wave * 1024), 16, 0, 0);
    __builtin_amdgcn_global_load_lds((const unsigned*)(bsrc[1] + (size_t)kt * 32 * LDW), (unsigned*)(sb + 8192 + (wave + 8) * 1024), 16, 0, 0);
    if (has3) __builtin_amdgcn_global_load_lds((const unsigned*)(bsrc[2] + (size_t)kt * 32 * LDW), (unsigned*)(sb + 8192 + (wave + 16) * 1024), 16, 0, 0);
  };

  const int xoff = (wm * 64 + c) * 64 + ((g ^ aswz(c)) << 4);
  int woff[NFR];
  {
    const int q = (lane & 15) >> 2, p = lane & 3;
    const int s = (q >> 1) | ((g & 1) << 1);
    const int lo = (8 * g + q) * 128 + (s << 5) + p * 8;
#pragma unroll
    for (int i = 0; i < NFR; ++i) {
      const int nf = TYPE == 0 ? (i < 3 ? 3 * wn + i : 12 + 3 * wn + (i - 3)) : 5 * wn + i;
      woff[i] = 8192 + (nf >> 2) * 4096 + (lo ^ ((nf & 3) << 5));
    }
  }

  f32x4 acc[4][NFR];
#pragma unroll
  for (int a = 0; a < 4; ++a)
#pragma unroll
    for (int b = 0; b < NFR; ++b) acc[a][b] = (f32x4){0.f, 0.f, 0.f, 0.f};

  auto readfr = [&](int kt, s16x8 (&xf)[4], s16x8 (&wf)[NFR]) {
    const char* sb = smem + (kt % K1_NS) * K1_STAGE;
#pragma unroll
    for (int b = 0; b < NFR; ++b) {
      s16x4 lo = __builtin_amdgcn_ds_read_tr16_b64_v4i16((lds_s16x4_ptr)(sb + woff[b]));
      s16x4 hi = __builtin_amdgcn_ds_read_tr16_b64_v4i16((lds_s16x4_ptr)(sb + woff[b] + 512));
      wf[b] = __builtin_shufflevector(lo, hi, 0, 1, 2, 3, 4, 5, 6, 7);
    }
#pragma unroll
    for (int a = 0; a < 4; ++a) xf[a] = *(const s16x8*)(sb + xoff + a * 1024);
  };
  auto mma = [&](const s16x8 (&xf)[4], const s16x8 (&wf)[NFR]) {
#pragma unroll
    for (int a = 0; a < 4; ++a)
#pragma unroll
      for (int b = 0; b < NFR; ++b) {
        if (TYPE == 2) acc[a][b] = __builtin_amdgcn_mfma_f32_16x16x32_bf16(xf[a], wf[b], acc[a][b], 0, 0, 0);
        else           acc[a][b] = __builtin_amdgcn_mfma_f32_16x16x32_bf16(wf[b], xf[a], acc[a][b], 0, 0, 0);
      }
  };
#define WAITN(N) asm volatile("s_waitcnt vmcnt(%0)" :: "n"(N) : "memory")
#define K1_WAIT(n) do { if (has3) WAITN(4 * (n)); else WAITN(3 * (n)); } while (0)

  s16x8 xA[4], wAf[NFR], xB[4], wBf[NFR];
#define SGB(m, n) __builtin_amdgcn_sched_group_barrier(m, n, 0)
  auto pattern_full = [&]() {
    SGB(0x10, 1); SGB(0x8, 1); SGB(0x10, 1); SGB(0x8, 1); SGB(0x10, 1); SGB(0x8, 1); SGB(0x10, 1); SGB(0x8, 1);
#pragma unroll
    for (int i = 0; i < 4 + 2 * NFR; ++i) { SGB(0x100, 1); SGB(0x8, 1); }
    SGB(0x8, 4);
  };
  auto pattern_tail = [&]() {
#pragma unroll
    for (int i = 0; i < 4 + 2 * NFR; ++i) { SGB(0x100, 1); SGB(0x8, 1); }
    SGB(0x8, 8);
  };
  STAMP(1, 0);
#pragma unroll
  for (int i = 0; i < K1_NS; ++i) issue(i);
  K1_WAIT(K1_NS - 1);
  BAR;
  STAMP(1, 1);
  readfr(0, xA, wAf);
  SCHED;
#pragma unroll
  for (int kt = 0; kt < 19; ++kt) {
    if (kt + K1_NS <= 20) K1_WAIT(K1_NS - 2); else K1_WAIT(18 - kt);
    WAIT_L0; BAR; SCHED;
    if (kt + K1_NS < 20) issue(kt + K1_NS);
    if (kt & 1) { readfr(kt + 1, xA, wAf); mma(xB, wBf); }
    else        { readfr(kt + 1, xB, wBf); mma(xA, wAf); }
    if (kt + K1_NS < 20) pattern_full(); else pattern_tail();
    SCHED;
  }
  mma(xB, wBf);

  WAIT_L0; BAR;
  STAMP(1, 2);
  if (TYPE == 0) {
#pragma unroll
    for (int a = 0; a < 4; ++a) {
#pragma unroll
      for (int b = 0; b < 3; ++b) {
        u32x2 w;
        w[0] = cvt2(bfr2(sigm2(bfr2(lo2(acc[a][b])))) * bfr2(lo2(acc[a][b + 3])));
        w[1] = cvt2(bfr2(sigm2(bfr2(hi2(acc[a][b])))) * bfr2(hi2(acc[a][b + 3])));
        const int f0 = wn * 48 + b * 16;
        const int ktl = f0 >> 5, gk = ((f0 >> 4) & 1) * 2 + (g >> 1);
        *(u32x2*)(smem + (((wm * 4 + a) * 6 + ktl) * 64 + gk * 16 + c) * 16 + (g & 1) * 8) = w;
      }
    }
    __syncthreads();
    char* H = ws + OFF_H + ((size_t)(mt * 8) * 54 + j * 6) * 1024;
#pragma unroll
    for (int i = 0; i < 6; ++i) {
      const int q = tid + 512 * i;
      const int m16 = q / 384, rest = q - m16 * 384;
      const u32x4 v = *(const u32x4*)(smem + q * 16);
      *(u32x4*)(H + (size_t)m16 * 54 * 1024 + rest * 16) = v;
    }
  } else if (TYPE == 1) {
#pragma unroll
    for (int a = 0; a < 4; ++a) {
#pragma unroll
      for (int b = 0; b < 5; ++b) {
        u32x2 w; w[0] = phi2(lo2(acc[a][b])); w[1] = phi2(hi2(acc[a][b]));
        const int n = wn * 80 + b * 16 + 4 * g, hl = n / 20, d0 = n - hl * 20;
        const int ro = d0 < 16 ? (d0 >> 3) * 256 + c * 16 + (d0 & 7) * 2 : 512 + c * 8;
        *(u32x2*)(smem + ((wm * 4 + a) * 16 + hl) * 640 + ro) = w;
      }
    }
    __syncthreads();
    char* PQ = ws + OFF_PQ + ((size_t)(mt * 8) * 32 + j * 16) * 640;
#pragma unroll
    for (int i = 0; i < 10; ++i) {
      const int q = tid + 512 * i;
      const int m16 = q / 640, rest = q - m16 * 640;
      const u32x4 v = *(const u32x4*)(smem + q * 16);
      *(u32x4*)(PQ + (size_t)m16 * 20480 + rest * 16) = v;
    }
  } else {
#pragma unroll
    for (int a = 0; a < 4; ++a)
#pragma unroll
      for (int b = 0; b < 5; ++b) {
        const int col = wn * 80 + b * 16 + c;
        u32x2 w;
        if (wn < 2) { w[0] = phi2(lo2(acc[a][b])); w[1] = phi2(hi2(acc[a][b])); }
        else        { w[0] = cvt2(lo2(acc[a][b])); w[1] = cvt2(hi2(acc[a][b])); }
        *(u32x2*)(smem + col * 272 + (wm * 64 + a * 16 + 4 * g) * 2) = w;
      }
    __syncthreads();
    const int r32 = lane & 31, hh = lane >> 5;
    f32x16 akv, az;
#pragma unroll
    for (int i = 0; i < 16; ++i) { akv[i] = 0.f; az[i] = 0.f; }
    s16x8 ones;
#pragma unroll
    for (int i = 0; i < 8; ++i) ones[i] = (short)0x3F80;
#pragma unroll
    for (int s = 0; s < 8; ++s) {
      s16x8 af = *(const s16x8*)(smem + (20 * wave + r32) * 272 + (16 * s + 8 * hh) * 2);
      s16x8 bfv = *(const s16x8*)(smem + (160 + 20 * wave + r32) * 272 + (16 * s + 8 * hh) * 2);
      akv = __builtin_amdgcn_mfma_f32_32x32x16_bf16(af, bfv, akv, 0, 0, 0);
      az = __builtin_amdgcn_mfma_f32_32x32x16_bf16(af, ones, az, 0, 0, 0);
    }
    const int head = j * 8 + wave;
    float* P = (float*)(ws + OFF_KVP) + (size_t)(mt * 32 + head) * 504;
    if (r32 <= 20) {
#pragma unroll
      for (int q = 0; q < 3; ++q) {
        const int d0 = 8 * q + 4 * hh;
        if (d0 < 20) {
          f32x4 val;
#pragma unroll
          for (int r = 0; r < 4; ++r) {
            const unsigned u = __float_as_uint((r32 == 20) ? az[4 * q + r] : akv[4 * q + r]);
            val[r] = __uint_as_float(u == KV_EMPTY ? 0x7FC00000u : u);
          }
          asm volatile("global_store_dwordx4 %0, %1, off sc1" :: "v"(P + r32 * 24 + d0), "v"(val) : "memory");
        }
      }
    }
    if (mt < 8) {
      const int hd = j * 8 + mt, ci = tid & 127, pg = tid >> 7;
      const float* src = (const float*)(ws + OFF_KVP) + ((size_t)(4 * pg) * 32 + hd) * 504 + (ci < 126 ? ci : 0) * 4;
      const bool chk = ci < 126 && (ci % 6) != 5;
      u32x4 v0, v1, v2, v3;
      bool bad;
      do {
        asm volatile("global_load_dwordx4 %0, %4, off sc1\n\tglobal_load_dwordx4 %1, %5, off sc1\n\t"
                     "global_load_dwordx4 %2, %6, off sc1\n\tglobal_load_dwordx4 %3, %7, off sc1\n\ts_waitcnt vmcnt(0)"
                     : "=&v"(v0), "=&v"(v1), "=&v"(v2), "=&v"(v3)
                     : "v"(src), "v"(src + 32 * 504), "v"(src + 2 * 32 * 504), "v"(src + 3 * 32 * 504) : "memory");
        bad = false;
#pragma unroll
        for (int i = 0; i < 4; ++i) bad = bad || v0[i] == KV_EMPTY || v1[i] == KV_EMPTY || v2[i] == KV_EMPTY || v3[i] == KV_EMPTY;
      } while (__builtin_amdgcn_ballot_w64(bad && chk) != 0ull);
      f32x4* red = (f32x4*)(smem + 98304);
      red[pg * 128 + ci] = ((__builtin_bit_cast(f32x4, v0) + __builtin_bit_cast(f32x4, v1)) + __builtin_bit_cast(f32x4, v2)) + __builtin_bit_cast(f32x4, v3);
      __syncthreads();
      float* kvs = (float*)(smem + 106496);
      if (tid < 126) *(f32x4*)(kvs + tid * 4) = ((red[tid] + red[128 + tid]) + red[256 + tid]) + red[384 + tid];
      __syncthreads();
      if (wave < 2) {
        const int fr = wave;
        const int e = fr == 0 ? c : ((c & 3) == 0 ? 16 + (c >> 2) : ((c & 3) == 1 ? 20 : 99)), d0 = 8 * g;
        const bool act = (e <= 20 && d0 < 20), act1 = act && (d0 < 16);
        const float* sp = kvs + (act ? e * 24 + d0 : 0);
        f32x4 w0 = *(const f32x4*)sp, w1 = *(const f32x4*)(sp + (act1 ? 4 : 0));
        u32x4 w;
        w[0] = act ? pack2(w0[0], w0[1]) : 0u; w[1] = act ? pack2(w0[2], w0[3]) : 0u;
        w[2] = act1 ? pack2(w1[0], w1[1]) : 0u; w[3] = act1 ? pack2(w1[2], w1[3]) : 0u;
        *(u32x4*)(ws + OFF_KVF + ((size_t)(hd * 2 + fr) * 64 + lane) * 16) = w;
      }
    }
  }
}

__global__ __launch_bounds__(512, 2) void k1_gemm(const u16* __restrict__ wqkv, const u16* __restrict__ wg,
                                                   const u16* __restrict__ wu, const u16* __restrict__ wo,
                                                   const u16* __restrict__ wdown, char* __restrict__ ws) {
  extern __shared__ __attribute__((aligned(16))) char smem[];
  const int bid = blockIdx.x;
  if (bid >= 240) {
    retile_all((bid - 240) * 512 + threadIdx.x, wo, wdown, ws);
    return;
  }
  const int p = (bid & 7) * 30 + (bid >> 3);
  const int mh = p / 120, rem = p % 120, nt = rem >> 3, mt = mh * 8 + (rem & 7);
  if (nt < 4) k1_body<2>(wqkv, wqkv, ws, mt, nt, smem);
  else if (nt < 6) k1_body<1>(wqkv, wqkv, ws, mt, nt - 4, smem);
  else k1_body<0>(wg, wu, ws, mt, nt - 6, smem);
  STAMP(1, 3);
}

constexpr int K2_LDS = 163840;
#define K2DD 3
constexpr int K2_D = K2DD;
#define K2DW 3
constexpr int K2_DW = K2DW;

__global__ __launch_bounds__(512, 2) void k2_gemm(const u16* __restrict__ x, const char* __restrict__ ws, u16* __restrict__ out) {
  extern __shared__ __attribute__((aligned(16))) char smem[];
  const int tid = threadIdx.x, lane = tid & 63;
  const int wave = __builtin_amdgcn_readfirstlane(tid >> 6);
  const int wq = wave & 3;
  const int g = lane >> 4, c = lane & 15;
  const int bid = blockIdx.x;
  const int mt = (bid & 7) * 4 + (bid >> 6), nt = (bid >> 3) & 7;

  STAMP(2, 0);
  f32x4 acc[4][5];
#pragma unroll
  for (int a = 0; a < 4; ++a)
#pragma unroll
    for (int b = 0; b < 5; ++b) acc[a][b] = (f32x4){0.f, 0.f, 0.f, 0.f};
  const int b0 = wave < 4 ? 0 : 3, nb = wave < 4 ? 3 : 2;
  const int tok = mt * 64 + wq * 16 + c;
  u32x2 xw[3];
#pragma unroll
  for (int i = 0; i < 3; ++i) xw[i] = *(const u32x2*)(x + (size_t)tok * D_ + nt * 80 + (i < nb ? b0 + i : b0) * 16 + 4 * g);

  if (wave < 4) {
    const char* hsrc = ws + OFF_H + ((size_t)(mt * 4) * 54 + wq) * 1024 + lane * 16;
    const char* wsrc = ws + OFF_WC + ((size_t)nt * 54 + wq) * 5120 + lane * 16;
    s16x8 xr[K2_D][4], wr[K2_D][5];
    auto ld = [&](int slot, int i) {
#pragma unroll
      for (int b = 0; b < 5; ++b) wr[slot][b] = *(const s16x8*)(wsrc + (size_t)(20 * i + b) * 1024);
#pragma unroll
      for (int a = 0; a < 4; ++a) xr[slot][a] = *(const s16x8*)(hsrc + (size_t)(a * 54 + 4 * i) * 1024);
    };
    auto mma1 = [&](int slot) {
#pragma unroll
      for (int a = 0; a < 4; ++a)
#pragma unroll
        for (int b = 0; b < 5; ++b) acc[a][b] = __builtin_amdgcn_mfma_f32_16x16x32_bf16(wr[slot][b], xr[slot][a], acc[a][b], 0, 0, 0);
    };
#pragma unroll
    for (int i = 0; i < K2_D; ++i) ld(i, i);
    SCHED;
#pragma unroll
    for (int i = 0; i < 13; ++i) {
      mma1(i % K2_D);
      if (i + K2_D < 13) ld(i % K2_D, i + K2_D);
      else if (i + K2_D == 13) { if (wave < 2) ld(i % K2_D, 13); }
      SCHED;
    }
    if (wave < 2) mma1(13 % K2_D);
    STAMP(2, 1);
  } else {
    __builtin_amdgcn_s_setprio(3);
    char* reg = smem + wq * 36864;
    {
      const char* pqg = ws + OFF_PQ + ((size_t)(mt * 4) * 32 + wq * 8) * 640 + lane * 16;
      const char* kg = ws + OFF_KVF + (size_t)(wq * 8) * 2048 + lane * 16;
#pragma unroll
      for (int i = 0; i < 16; ++i) __builtin_amdgcn_global_load_lds((const unsigned*)(kg + i * 1024), (unsigned*)(reg + 20480 + i * 1024), 16, 0, 0);
#pragma unroll
      for (int tg = 0; tg < 4; ++tg)
#pragma unroll
        for (int i = 0; i < 5; ++i)
          __builtin_amdgcn_global_load_lds((const unsigned*)(pqg + tg * 20480 + i * 1024), (unsigned*)(reg + tg * 5120 + i * 1024), 16, 0, 0);
    }
    const char* wosrc = ws + OFF_WO + (size_t)(nt * 4 + wq) * 25600 + lane * 16;
    s16x8 w2[K2_DW][5];
    auto ldW = [&](int s) {
#pragma unroll
      for (int b = 0; b < 5; ++b) w2[s % K2_DW][b] = *(const s16x8*)(wosrc + (s * 5 + b) * 1024);
    };
#pragma unroll
    for (int s = 0; s < K2_DW; ++s) ldW(s);
    SCHED;
    __builtin_amdgcn_s_waitcnt(0x0F70);
    SCHED;
    STAMPW(4);
    const char* pql = reg + (g < 2 ? g * 256 + c * 16 : 512 + c * 8);
    const char* kvl = reg + 20480 + lane * 16;
    const f32x4 z4 = (f32x4){0.f, 0.f, 0.f, 0.f};
    unsigned ow[4][20];
    auto mma2 = [&](int s) {
#pragma unroll
      for (int a = 0; a < 4; ++a) {
        const u32x4 o4 = (u32x4){ow[a][4 * s], ow[a][4 * s + 1], ow[a][4 * s + 2], ow[a][4 * s + 3]};
        const s16x8 of = __builtin_bit_cast(s16x8, o4);
#pragma unroll
        for (int b = 0; b < 5; ++b) acc[a][b] = __builtin_amdgcn_mfma_f32_16x16x32_bf16(w2[s % K2_DW][b], of, acc[a][b], 0, 0, 0);
      }
      if (s + K2_DW < 5) ldW(s + K2_DW);
    };
#pragma unroll
    for (int m = 0; m < 4; ++m) {
      f32x4 ov[2][4]; float oe[2][4];
#pragma unroll
      for (int h2 = 0; h2 < 2; ++h2) {
        const int hh = 2 * m + h2;
        const s16x8 k0 = *(const s16x8*)(kvl + hh * 2048), k1 = *(const s16x8*)(kvl + hh * 2048 + 1024);
#pragma unroll
        for (int tg = 0; tg < 4; ++tg) {
          const u32x4 pq = *(const u32x4_a8*)(pql + tg * 5120 + hh * 640);
          u32x4 pk4;
          pk4[0] = g < 3 ? pq[0] : 0u; pk4[1] = g < 3 ? pq[1] : 0u;
          pk4[2] = g < 2 ? pq[2] : 0u; pk4[3] = g < 2 ? pq[3] : 0u;
          const s16x8 pf = __builtin_bit_cast(s16x8, pk4);
          const f32x4 d1 = __builtin_amdgcn_mfma_f32_16x16x32_bf16(k1, pf, z4, 0, 0, 0);
          const f32x4 d0 = __builtin_amdgcn_mfma_f32_16x16x32_bf16(k0, pf, z4, 0, 0, 0);
          const float rden = frcp(bfr(bfr(d1[1]) + 1e-6f));
          const f32x2 lo = bfr2(lo2(d0)) * rden, hi = bfr2(hi2(d0)) * rden;
          ov[h2][tg] = (f32x4){lo[0], lo[1], hi[0], hi[1]};
          oe[h2][tg] = bfr(d1[0]) * rden;
        }
      }
#pragma unroll
      for (int tg = 0; tg < 4; ++tg) {
        ow[tg][5 * m + 0] = cvt2(lo2(ov[0][tg]));
        ow[tg][5 * m + 1] = cvt2(hi2(ov[0][tg]));
        ow[tg][5 * m + 2] = cvt2((f32x2){oe[0][tg], ov[1][tg][0]});
        ow[tg][5 * m + 3] = cvt2((f32x2){ov[1][tg][1], ov[1][tg][2]});
        ow[tg][5 * m + 4] = cvt2((f32x2){ov[1][tg][3], oe[1][tg]});
      }
      mma2(m);
      if (m == 3) mma2(4);
      SCHED;
      if (m < 2) STAMPW(5 + m);
    }
  }
  __syncthreads();

  f32x4* red = (f32x4*)smem;
#pragma unroll
  for (int a = 0; a < 4; ++a)
#pragma unroll
    for (int b = 0; b < 5; ++b) red[(wave * 20 + a * 5 + b) * 64 + lane] = acc[a][b];
  __syncthreads();
#pragma unroll
  for (int i = 0; i < 3; ++i) {
    if (i < nb) {
      const int f = wq * 5 + b0 + i;
      f32x4 y1 = red[(0 * 20 + f) * 64 + lane], y2 = red[(4 * 20 + f) * 64 + lane];
#pragma unroll
      for (int sw = 1; sw < 4; ++sw) { y1 += red[(sw * 20 + f) * 64 + lane]; y2 += red[((4 + sw) * 20 + f) * 64 + lane]; }
      const int n = nt * 80 + (b0 + i) * 16 + 4 * g;
      u32x2 w;
      {
        const f32x2 y = bfr2(lo2(y1)), gs = bfr2(y * sigm2(y)), sv = bfr2(lo2(y2));
        w[0] = cvt2(unp2(xw[i][0]) + bfr2(sv * gs));
      }
      {
        const f32x2 y = bfr2(hi2(y1)), gs = bfr2(y * sigm2(y)), sv = bfr2(hi2(y2));
        w[1] = cvt2(unp2(xw[i][1]) + bfr2(sv * gs));
      }
      *(u32x2*)(out + (size_t)tok * D_ + n) = w;
    }
  }
  STAMP(2, 3);
}

extern "C" void kernel_launch(void* const* d_in, const int* in_sizes, int n_in,
                              void* d_out, int out_size, void* d_ws, size_t ws_size,
                              hipStream_t stream) {
  const u16* x = (const u16*)d_in[0];
  const u16* wqkv = (const u16*)d_in[1];
  const u16* wo = (const u16*)d_in[2];
  const u16* wg = (const u16*)d_in[4];
  const u16* wu = (const u16*)d_in[5];
  const u16* wd = (const u16*)d_in[6];
  char* ws = (char*)d_ws;
  u16* out = (u16*)d_out;

  hipFuncSetAttribute((const void*)k1_gemm, hipFuncAttributeMaxDynamicSharedMemorySize, K1_NS * K1_STAGE);
  hipFuncSetAttribute((const void*)k2_gemm, hipFuncAttributeMaxDynamicSharedMemorySize, K2_LDS);

  k0_prep<<<512, 256, 0, stream>>>(x, ws, wqkv, wg, wu);
  k1_gemm<<<256, 512, K1_NS * K1_STAGE, stream>>>(wqkv, wg, wu, wo, wd, ws);
  k2_gemm<<<256, 512, K2_LDS, stream>>>(x, ws, out);
}
```

```cpp
#include <hip/hip_runtime.h>
#include <hip/hip_bf16.h>
#include <stdint.h>

typedef unsigned short u16;
typedef __attribute__((ext_vector_type(8))) short s16x8;
typedef __attribute__((ext_vector_type(4))) short s16x4;
typedef __attribute__((ext_vector_type(4))) float f32x4;
typedef __attribute__((ext_vector_type(16))) float f32x16;
typedef __attribute__((ext_vector_type(2))) unsigned u32x2;
typedef __attribute__((ext_vector_type(4))) unsigned u32x4;
typedef u32x4 u32x4_a8 __attribute__((aligned(8)));
typedef __attribute__((address_space(3))) s16x4* lds_s16x4_ptr;
typedef __attribute__((address_space(1))) unsigned gu32;
typedef __attribute__((address_space(1))) unsigned long long gu64;

#define RLX_AGENT __ATOMIC_RELAXED, __HIP_MEMORY_SCOPE_AGENT

constexpr int S_ = 2048, D_ = 640, F_ = 1728, NQKV_ = 1920;

constexpr size_t OFF_CNT   = 0;
constexpr size_t OFF_XN    = 8192;
constexpr size_t SZ_XN     = (size_t)S_ * D_ * 2;
constexpr size_t OFF_H     = OFF_XN + SZ_XN;
constexpr size_t SZ_H      = (size_t)S_ * F_ * 2;
constexpr size_t OFF_PQ    = OFF_H + SZ_H;
constexpr size_t SZ_PQ     = (size_t)S_ * D_ * 2;
constexpr size_t OFF_WC    = OFF_PQ + SZ_PQ;
constexpr size_t SZ_WC     = (size_t)8 * 54 * 5 * 1024;
constexpr size_t OFF_WO    = OFF_WC + SZ_WC;
constexpr size_t SZ_WO     = (size_t)8 * 4 * 5 * 5 * 1024;
constexpr size_t OFF_KVP   = OFF_WO + SZ_WO;
constexpr size_t SZ_KVP    = (size_t)16 * 32 * 504 * 4;
constexpr size_t OFF_KVF   = OFF_KVP + SZ_KVP;
constexpr size_t SZ_KVF    = 65536;

#define KVAUX 16
constexpr unsigned KV_EMPTY = 0xFFFFFFFFu;
__device__ __forceinline__ float bf2f(u16 v) { return __uint_as_float(((unsigned)v) << 16); }
__device__ __forceinline__ u16 f2bf(float f) { return __builtin_bit_cast(u16, (__bf16)f); }
__device__ __forceinline__ float bfr(float f) { return bf2f(f2bf(f)); }
__device__ __forceinline__ unsigned pack2(float a, float b) { return (unsigned)f2bf(a) | ((unsigned)f2bf(b) << 16); }
__device__ __forceinline__ float phi_f(float t) {
  float p = t > 0.f ? bfr(t + 1.0f) : bfr(__expf(t));
  return bfr(p + 1e-6f);
}
typedef __attribute__((ext_vector_type(2))) float f32x2;
typedef __attribute__((ext_vector_type(2))) __bf16 bf16x2;
__device__ __forceinline__ unsigned cvt2(f32x2 v) { return __builtin_bit_cast(unsigned, __builtin_convertvector(v, bf16x2)); }
__device__ __forceinline__ f32x2 unp2(unsigned u) { return (f32x2){__uint_as_float(u << 16), __uint_as_float(u & 0xffff0000u)}; }
__device__ __forceinline__ f32x2 bfr2(f32x2 v) { return unp2(cvt2(v)); }
__device__ __forceinline__ f32x2 lo2(f32x4 v) { return __builtin_shufflevector(v, v, 0, 1); }
__device__ __forceinline__ f32x2 hi2(f32x4 v) { return __builtin_shufflevector(v, v, 2, 3); }
__device__ __forceinline__ unsigned phi2(f32x2 a) {
  const f32x2 t = bfr2(a), m = t * 1.44269504088896340736f, p = t + 1.0f;
  f32x2 sel;
  sel[0] = t[0] > 0.f ? p[0] : __builtin_amdgcn_exp2f(m[0]);
  sel[1] = t[1] > 0.f ? p[1] : __builtin_amdgcn_exp2f(m[1]);
  return cvt2(bfr2(sel) + 1e-6f);
}
__device__ __forceinline__ f32x2 sigm2(f32x2 g) {
  const f32x2 m = g * -1.44269504088896340736f;
  f32x2 e; e[0] = __builtin_amdgcn_exp2f(m[0]); e[1] = __builtin_amdgcn_exp2f(m[1]);
  e = e + 1.0f;
  f32x2 r; r[0] = __builtin_amdgcn_rcpf(e[0]); r[1] = __builtin_amdgcn_rcpf(e[1]);
  return r;
}
__device__ __forceinline__ int aswz(int row) { int x = (row >> 2) & 3; return (((x ^ (x >> 1)) & 1) << 1) | (x >> 1); }

#define WAIT_V(n) asm volatile("s_waitcnt vmcnt(" #n ")" ::: "memory")
#define STAMP(kid, idx)
#define STAMPW(idx)
#define WAIT_L0 asm volatile("s_waitcnt lgkmcnt(0)" ::: "memory")
#define BAR __builtin_amdgcn_s_barrier()
#define SCHED __builtin_amdgcn_sched_barrier(0)
__device__ __forceinline__ float frcp(float v) { return __builtin_amdgcn_rcpf(v); }

constexpr int RT_WD = 216 * 80, RT_ITEMS = RT_WD + 80 * 80, RT_R = 3;
__device__ __forceinline__ void retile_all(int t0, const u16* __restrict__ wo, const u16* __restrict__ wdown, char* __restrict__ ws) {
  u32x4 r[RT_R][8];
  char* dst[RT_R];
#pragma unroll
  for (int it = 0; it < RT_R; ++it) {
    int t = t0 + it * 8192; if (t >= RT_ITEMS) t = t0;
    const bool isd = t < RT_WD;
    const int t2 = isd ? t : t - RT_WD;
    const int ko = t2 / 80, noct = t2 - ko * 80;
    const int n0 = noct * 8, nt = n0 / 80, nn = n0 - nt * 80, nf = nn >> 4, c0 = nn & 15, gg = ko & 3, kq = ko >> 2;
    const int wq = kq / 5, ks = kq - wq * 5;
    const u16* src = (isd ? wdown : wo) + n0;
#pragma unroll
    for (int jj = 0; jj < 8; ++jj) {
      const int q = 8 * ks + jj, hh = q / 5, rr = q - hh * 5;
      const int row = isd ? ko * 8 + jj : (wq * 8 + hh) * 20 + (rr < 4 ? 4 * gg + rr : 16 + gg);
      r[it][jj] = *(const u32x4*)(src + (size_t)row * D_);
    }
    dst[it] = ws + (isd ? OFF_WC + (size_t)(nt * 54 + kq) * 5120 : OFF_WO + (size_t)(nt * 20 + kq) * 5120) + (size_t)(nf * 64 + gg * 16 + c0) * 16;
  }
#pragma unroll
  for (int it = 0; it < RT_R; ++it) {
#pragma unroll
    for (int i = 0; i < 4; ++i) {
      u32x4 lo, hi;
#pragma unroll
      for (int q = 0; q < 4; ++q) {
        lo[q] = (r[it][2 * q][i] & 0xffffu) | (r[it][2 * q + 1][i] << 16);
        hi[q] = (r[it][2 * q][i] >> 16) | (r[it][2 * q + 1][i] & 0xffff0000u);
      }
      *(u32x4*)(dst[it] + (2 * i) * 16) = lo;
      *(u32x4*)(dst[it] + (2 * i + 1) * 16) = hi;
    }
  }
}

__global__ __launch_bounds__(256) void k0_prep(const u16* __restrict__ x, char* __restrict__ ws, const u16* __restrict__ wqkv,
                                               const u16* __restrict__ wg, const u16* __restrict__ wu) {
  const int tid = threadIdx.x;
  if (blockIdx.x >= 256) {
    const int t = (blockIdx.x - 256) * 256 + tid;
    unsigned v0, v1;
    const int l1 = t + 65536 < 107520 ? t + 65536 : t;
    const char* p0 = t < 38400 ? (const char*)wqkv + (size_t)t * 64 : (const char*)wg + (size_t)(t - 38400) * 64;
    const char* p1 = l1 < 38400 ? (const char*)wqkv + (size_t)l1 * 64 : (l1 < 72960 ? (const char*)wg + (size_t)(l1 - 38400) * 64 : (const char*)wu + (size_t)(l1 - 72960) * 64);
    asm volatile("global_load_dword %0, %2, off\n\tglobal_load_dword %1, %3, off\n\ts_waitcnt vmcnt(0)" : "=&v"(v0), "=&v"(v1) : "v"(p0), "v"(p1) : "memory");
    return;
  }
  {
    if (blockIdx.x < 16) {
      const int ci = blockIdx.x * 256 + tid, fr = (ci >> 6) & 1, gg = (ci >> 4) & 3, cc = ci & 15;
      const unsigned v = (gg < 3 && (fr == 0 || (cc & 3) < 2)) ? KV_EMPTY : 0u;
      *(u32x4*)(ws + OFF_KVF + (size_t)ci * 16) = (u32x4){v, v, v, v};
    }
    const int row = blockIdx.x * 8 + (tid >> 5);
    const int l32 = tid & 31;
    const u16* xr = x + (size_t)row * D_;
    float v[20];
    float sum = 0.f;
#pragma unroll
    for (int i = 0; i < 5; ++i) {
      u32x2 w = *(const u32x2*)(xr + (l32 + 32 * i) * 4);
      v[4 * i + 0] = __uint_as_float(w[0] << 16);
      v[4 * i + 1] = __uint_as_float(w[0] & 0xffff0000u);
      v[4 * i + 2] = __uint_as_float(w[1] << 16);
      v[4 * i + 3] = __uint_as_float(w[1] & 0xffff0000u);
      sum += v[4 * i] + v[4 * i + 1] + v[4 * i + 2] + v[4 * i + 3];
    }
#pragma unroll
    for (int o = 16; o >= 1; o >>= 1) sum += __shfl_xor(sum, o);
    const float mean = sum * (1.0f / D_);
    float sq = 0.f;
#pragma unroll
    for (int i = 0; i < 20; ++i) { float d = v[i] - mean; sq += d * d; }
#pragma unroll
    for (int o = 16; o >= 1; o >>= 1) sq += __shfl_xor(sq, o);
    const float mb = bfr(mean);
    const float vb = bfr(sq * (1.0f / D_));
    const float sd = bfr(sqrtf(bfr(vb + 1e-5f)));
    u16* xn = (u16*)(ws + OFF_XN);
    const int mt = row >> 7, r = row & 127, sw = aswz(r);
#pragma unroll
    for (int i = 0; i < 5; ++i) {
      const int k = (l32 + 32 * i) * 4;
      float o0 = bfr(v[4 * i + 0] - mb) / sd, o1 = bfr(v[4 * i + 1] - mb) / sd;
      float o2 = bfr(v[4 * i + 2] - mb) / sd, o3 = bfr(v[4 * i + 3] - mb) / sd;
      u32x2 w; w[0] = pack2(o0, o1); w[1] = pack2(o2, o3);
      const int kt = k >> 5, ch = (k >> 3) & 3, hf = (k >> 2) & 1;
      size_t off = ((size_t)(mt * 20 + kt) * 128 + r) * 32 + ((ch ^ sw) << 3) + (hf << 2);
      *(u32x2*)(xn + off) = w;
    }
  }
}

constexpr int K1_STAGE = 32768;
#define K1NS 4
constexpr int K1_NS = K1NS;

template <int TYPE>
__device__ __forceinline__ void k1_body(const u16* __restrict__ wA, const u16* __restrict__ wB, char* __restrict__ ws,
                                        int mt, int j, char* smem) {
  constexpr int NCB = TYPE == 0 ? 6 : 5;
  constexpr int NFR = TYPE == 0 ? 6 : 5;
  constexpr int LDW = TYPE == 0 ? F_ : NQKV_;
  const int tid = threadIdx.x, lane = tid & 63;
  const int wave = __builtin_amdgcn_readfirstlane(tid >> 6);
  const int wm = wave >> 2, wn = (TYPE == 2 && wave >= 4) ? ((wave + 2) & 3) : (wave & 3);
  const int g = lane >> 4, c = lane & 15;

  const char* asrc = ws + OFF_XN + (size_t)mt * 20 * 8192 + wave * 1024 + lane * 16;
  const u16* bsrc[3];
#pragma unroll
  for (int t = 0; t < 3; ++t) {
    int ii = wave + 8 * t; if (ii >= NCB * 4) ii = wave;
    const int cb = ii >> 2, krow = (ii & 3) * 8 + (lane >> 3), u = lane & 7;
    const int sk = ((krow >> 1) & 1) | (((krow >> 3) & 1) << 1);
    const int ch = (u >> 1) ^ sk;
    const int lc = cb * 64 + ch * 16 + (u & 1) * 8;
    const u16* p;
    if (TYPE == 0) p = (lc < 192) ? (wA + j * 192 + lc) : (wB + j * 192 + (lc - 192));
    else if (TYPE == 1) p = wA + j * 320 + lc;
    else p = (lc < 160) ? (wA + 640 + j * 160 + lc) : (wA + 1280 + j * 160 + (lc - 160));
    bsrc[t] = p + (size_t)krow * LDW;
  }
  const bool has3 = (NCB == 6) || (wave < 4);

  auto issue = [&](int kt) {
    char* sb = smem + (kt % K1_NS) * K1_STAGE;
    __builtin_amdgcn_global_load_lds((const unsigned*)(asrc + (size_t)kt * 8192), (unsigned*)(sb + wave * 1024), 16, 0, 0);
    __builtin_amdgcn_global_load_lds((const unsigned*)(bsrc[0] + (size_t)kt * 32 * LDW), (unsigned*)(sb + 8192 + wave * 1024), 16, 0, 0);
    __builtin_amdgcn_global_load_lds((const unsigned*)(bsrc[1] + (size_t)kt * 32 * LDW), (unsigned*)(sb + 8192 + (wave + 8) * 1024), 16, 0, 0);
    if (has3) __builtin_amdgcn_global_load_lds((const unsigned*)(bsrc[2] + (size_t)kt * 32 * LDW), (unsigned*)(sb + 8192 + (wave + 16) * 1024), 16, 0, 0);
  };

  const int xoff = (wm * 64 + c) * 64 + ((g ^ aswz(c)) << 4);
  int woff[NFR];
  {
    const int q = (lane & 15) >> 2, p = lane & 3;
    const int s = (q >> 1) | ((g & 1) << 1);
    const int lo = (8 * g + q) * 128 + (s << 5) + p * 8;
#pragma unroll
    for (int i = 0; i < NFR; ++i) {
      const int nf = TYPE == 0 ? (i < 3 ? 3 * wn + i : 12 + 3 * wn + (i - 3)) : 5 * wn + i;
      woff[i] = 8192 + (nf >> 2) * 4096 + (lo ^ ((nf & 3) << 5));
    }
  }

  f32x4 acc[4][NFR];
#pragma unroll
  for (int a = 0; a < 4; ++a)
#pragma unroll
    for (int b = 0; b < NFR; ++b) acc[a][b] = (f32x4){0.f, 0.f, 0.f, 0.f};

  auto readfr = [&](int kt, s16x8 (&xf)[4], s16x8 (&wf)[NFR]) {
    const char* sb = smem + (kt % K1_NS) * K1_STAGE;
#pragma unroll
    for (int b = 0; b < NFR; ++b) {
      s16x4 lo = __builtin_amdgcn_ds_read_tr16_b64_v4i16((lds_s16x4_ptr)(sb + woff[b]));
      s16x4 hi = __builtin_amdgcn_ds_read_tr16_b64_v4i16((lds_s16x4_ptr)(sb + woff[b] + 512));
      wf[b] = __builtin_shufflevector(lo, hi, 0, 1, 2, 3, 4, 5, 6, 7);
    }
#pragma unroll
    for (int a = 0; a < 4; ++a) xf[a] = *(const s16x8*)(sb + xoff + a * 1024);
  };
  auto mma = [&](const s16x8 (&xf)[4], const s16x8 (&wf)[NFR]) {
#pragma unroll
    for (int a = 0; a < 4; ++a)
#pragma unroll
      for (int b = 0; b < NFR; ++b) {
        if (TYPE == 2) acc[a][b] = __builtin_amdgcn_mfma_f32_16x16x32_bf16(xf[a], wf[b], acc[a][b], 0, 0, 0);
        else           acc[a][b] = __builtin_amdgcn_mfma_f32_16x16x32_bf16(wf[b], xf[a], acc[a][b], 0, 0, 0);
      }
  };
#define WAITN(N) asm volatile("s_waitcnt vmcnt(%0)" :: "n"(N) : "memory")
#define K1_WAIT(n) do { if (has3) WAITN(4 * (n)); else WAITN(3 * (n)); } while (0)

  s16x8 xA[4], wAf[NFR], xB[4], wBf[NFR];
#define SGB(m, n) __builtin_amdgcn_sched_group_barrier(m, n, 0)
  auto pattern_full = [&]() {
    SGB(0x10, 1); SGB(0x8, 1); SGB(0x10, 1); SGB(0x8, 1); SGB(0x10, 1); SGB(0x8, 1); SGB(0x10, 1); SGB(0x8, 1);
#pragma unroll
    for (int i = 0; i < 4 + 2 * NFR; ++i) { SGB(0x100, 1); SGB(0x8, 1); }
    SGB(0x8, 4);
  };
  auto pattern_tail = [&]() {
#pragma unroll
    for (int i = 0; i < 4 + 2 * NFR; ++i) { SGB(0x100, 1); SGB(0x8, 1); }
    SGB(0x8, 8);
  };
  STAMP(1, 0);
#pragma unroll
  for (int i = 0; i < K1_NS; ++i) issue(i);
  K1_WAIT(K1_NS - 1);
  BAR;
  STAMP(1, 1);
  readfr(0, xA, wAf);
  SCHED;
#pragma unroll
  for (int kt = 0; kt < 19; ++kt) {
    if (kt + K1_NS <= 20) K1_WAIT(K1_NS - 2); else K1_WAIT(18 - kt);
    WAIT_L0; BAR; SCHED;
    if (kt + K1_NS < 20) issue(kt + K1_NS);
    if (kt & 1) { readfr(kt + 1, xA, wAf); mma(xB, wBf); }
    else        { readfr(kt + 1, xB, wBf); mma(xA, wAf); }
    if (kt + K1_NS < 20) pattern_full(); else pattern_tail();
    SCHED;
  }
  mma(xB, wBf);

  WAIT_L0; BAR;
  STAMP(1, 2);
  if (TYPE == 0) {
#pragma unroll
    for (int a = 0; a < 4; ++a) {
#pragma unroll
      for (int b = 0; b < 3; ++b) {
        u32x2 w;
        w[0] = cvt2(bfr2(sigm2(bfr2(lo2(acc[a][b])))) * bfr2(lo2(acc[a][b + 3])));
        w[1] = cvt2(bfr2(sigm2(bfr2(hi2(acc[a][b])))) * bfr2(hi2(acc[a][b + 3])));
        const int f0 = wn * 48 + b * 16;
        const int ktl = f0 >> 5, gk = ((f0 >> 4) & 1) * 2 + (g >> 1);
        *(u32x2*)(smem + (((wm * 4 + a) * 6 + ktl) * 64 + gk * 16 + c) * 16 + (g & 1) * 8) = w;
      }
    }
    __syncthreads();
    char* H = ws + OFF_H + ((size_t)(mt * 8) * 54 + j * 6) * 1024;
#pragma unroll
    for (int i = 0; i < 6; ++i) {
      const int q = tid + 512 * i;
      const int m16 = q / 384, rest = q - m16 * 384;
      const u32x4 v = *(const u32x4*)(smem + q * 16);
      *(u32x4*)(H + (size_t)m16 * 54 * 1024 + rest * 16) = v;
    }
  } else if (TYPE == 1) {
#pragma unroll
    for (int a = 0; a < 4; ++a) {
#pragma unroll
      for (int b = 0; b < 5; ++b) {
        u32x2 w; w[0] = phi2(lo2(acc[a][b])); w[1] = phi2(hi2(acc[a][b]));
        const int n = wn * 80 + b * 16 + 4 * g, hl = n / 20, d0 = n - hl * 20;
        const int ro = d0 < 16 ? (d0 >> 3) * 256 + c * 16 + (d0 & 7) * 2 : 512 + c * 8;
        *(u32x2*)(smem + ((wm * 4 + a) * 16 + hl) * 640 + ro) = w;
      }
    }
    __syncthreads();
    char* PQ = ws + OFF_PQ + ((size_t)(mt * 8) * 32 + j * 16) * 640;
#pragma unroll
    for (int i = 0; i < 10; ++i) {
      const int q = tid + 512 * i;
      const int m16 = q / 640, rest = q - m16 * 640;
      const u32x4 v = *(const u32x4*)(smem + q * 16);
      *(u32x4*)(PQ + (size_t)m16 * 20480 + rest * 16) = v;
    }
  } else {
#pragma unroll
    for (int a = 0; a < 4; ++a)
#pragma unroll
      for (int b = 0; b < 5; ++b) {
        const int col = wn * 80 + b * 16 + c;
        u32x2 w;
        if (wn < 2) { w[0] = phi2(lo2(acc[a][b])); w[1] = phi2(hi2(acc[a][b])); }
        else        { w[0] = cvt2(lo2(acc[a][b])); w[1] = cvt2(hi2(acc[a][b])); }
        *(u32x2*)(smem + col * 272 + (wm * 64 + a * 16 + 4 * g) * 2) = w;
      }
    __syncthreads();
    const int r32 = lane & 31, hh = lane >> 5;
    f32x16 akv, az;
#pragma unroll
    for (int i = 0; i < 16; ++i) { akv[i] = 0.f; az[i] = 0.f; }
    s16x8 ones;
#pragma unroll
    for (int i = 0; i < 8; ++i) ones[i] = (short)0x3F80;
#pragma unroll
    for (int s = 0; s < 8; ++s) {
      s16x8 af = *(const s16x8*)(smem + (20 * wave + r32) * 272 + (16 * s + 8 * hh) * 2);
      s16x8 bfv = *(const s16x8*)(smem + (160 + 20 * wave + r32) * 272 + (16 * s + 8 * hh) * 2);
      akv = __builtin_amdgcn_mfma_f32_32x32x16_bf16(af, bfv, akv, 0, 0, 0);
      az = __builtin_amdgcn_mfma_f32_32x32x16_bf16(af, ones, az, 0, 0, 0);
    }
    const int head = j * 8 + wave;
    float* P = (float*)(ws + OFF_KVP) + (size_t)(mt * 32 + head) * 504;
    if (r32 <= 20) {
#pragma unroll
      for (int q = 0; q < 3; ++q) {
        const int d0 = 8 * q + 4 * hh;
        if (d0 < 20) {
          f32x4 val;
#pragma unroll
          for (int r = 0; r < 4; ++r) val[r] = (r32 == 20) ? az[4 * q + r] : akv[4 * q + r];
          *(f32x4*)(P + r32 * 24 + d0) = val;
        }
      }
    }
  }
}

__global__ __launch_bounds__(512, 2) void k1_gemm(const u16* __restrict__ wqkv, const u16* __restrict__ wg,
                                                   const u16* __restrict__ wu, const u16* __restrict__ wo,
                                                   const u16* __restrict__ wdown, char* __restrict__ ws) {
  extern __shared__ __attribute__((aligned(16))) char smem[];
  const int bid = blockIdx.x;
  if (bid >= 240) {
    retile_all((bid - 240) * 512 + threadIdx.x, wo, wdown, ws);
    return;
  }
  const int p = (bid & 7) * 30 + (bid >> 3);
  const int mh = p / 120, rem = p % 120, nt = rem >> 3, mt = mh * 8 + (rem & 7);
  if (nt < 4) k1_body<2>(wqkv, wqkv, ws, mt, nt, smem);
  else if (nt < 6) k1_body<1>(wqkv, wqkv, ws, mt, nt - 4, smem);
  else k1_body<0>(wg, wu, ws, mt, nt - 6, smem);
  STAMP(1, 3);
}

constexpr int K2_LDS = 163840;
#define K2DD 3
constexpr int K2_D = K2DD;
#define K2DW 3
constexpr int K2_DW = K2DW;

__global__ __launch_bounds__(512, 2) void k2_gemm(const u16* __restrict__ x, const char* __restrict__ ws, u16* __restrict__ out) {
  extern __shared__ __attribute__((aligned(16))) char smem[];
  const int tid = threadIdx.x, lane = tid & 63;
  const int wave = __builtin_amdgcn_readfirstlane(tid >> 6);
  const int wq = wave & 3;
  const int g = lane >> 4, c = lane & 15;
  const int bid = blockIdx.x;
  const int mt = (bid & 7) * 4 + (bid >> 6), nt = (bid >> 3) & 7;

  STAMP(2, 0);
  f32x4 acc[4][5];
#pragma unroll
  for (int a = 0; a < 4; ++a)
#pragma unroll
    for (int b = 0; b < 5; ++b) acc[a][b] = (f32x4){0.f, 0.f, 0.f, 0.f};
  const int b0 = wave < 4 ? 0 : 3, nb = wave < 4 ? 3 : 2;
  const int tok = mt * 64 + wq * 16 + c;
  u32x2 xw[3];
#pragma unroll
  for (int i = 0; i < 3; ++i) xw[i] = *(const u32x2*)(x + (size_t)tok * D_ + nt * 80 + (i < nb ? b0 + i : b0) * 16 + 4 * g);

  if (wave < 4) {
    const char* hsrc = ws + OFF_H + ((size_t)(mt * 4) * 54 + wq) * 1024 + lane * 16;
    const char* wsrc = ws + OFF_WC + ((size_t)nt * 54 + wq) * 5120 + lane * 16;
    s16x8 xr[K2_D][4], wr[K2_D][5];
    auto ld = [&](int slot, int i) {
#pragma unroll
      for (int b = 0; b < 5; ++b) wr[slot][b] = *(const s16x8*)(wsrc + (size_t)(20 * i + b) * 1024);
#pragma unroll
      for (int a = 0; a < 4; ++a) xr[slot][a] = *(const s16x8*)(hsrc + (size_t)(a * 54 + 4 * i) * 1024);
    };
    auto mma1 = [&](int slot) {
#pragma unroll
      for (int a = 0; a < 4; ++a)
#pragma unroll
        for (int b = 0; b < 5; ++b) acc[a][b] = __builtin_amdgcn_mfma_f32_16x16x32_bf16(wr[slot][b], xr[slot][a], acc[a][b], 0, 0, 0);
    };
#pragma unroll
    for (int i = 0; i < K2_D; ++i) ld(i, i);
    SCHED;
#pragma unroll
    for (int i = 0; i < 13; ++i) {
      mma1(i % K2_D);
      if (i + K2_D < 13) ld(i % K2_D, i + K2_D);
      else if (i + K2_D == 13) { if (wave < 2) ld(i % K2_D, 13); }
      SCHED;
    }
    if (wave < 2) mma1(13 % K2_D);
    STAMP(2, 1);
  } else {
    __builtin_amdgcn_s_setprio(3);
    char* reg = smem + wq * 36864;
    if (bid < 32) {
      const int t = tid - 256, u = t >> 2, q = t & 3, uu = u < 63 ? u : 62, e = uu / 3, g3 = uu - e * 3;
      const float* src = (const float*)(ws + OFF_KVP) + ((size_t)(4 * q) * 32 + bid) * 504 + e * 24 + 8 * g3;
      f32x4 lo[4], hi[4];
#pragma unroll
      for (int i = 0; i < 4; ++i) { lo[i] = *(const f32x4*)(src + i * 32 * 504); hi[i] = *(const f32x4*)(src + i * 32 * 504 + (g3 < 2 ? 4 : 0)); }
      f32x4 s0 = (lo[0] + lo[1]) + (lo[2] + lo[3]), s1 = (hi[0] + hi[1]) + (hi[2] + hi[3]);
#pragma unroll
      for (int o = 1; o <= 2; o <<= 1)
#pragma unroll
        for (int r = 0; r < 4; ++r) { s0[r] += __shfl_xor(s0[r], o); s1[r] += __shfl_xor(s1[r], o); }
      u32x4 w;
      w[0] = pack2(s0[0], s0[1]); w[1] = pack2(s0[2], s0[3]);
      w[2] = g3 < 2 ? pack2(s1[0], s1[1]) : 0u; w[3] = g3 < 2 ? pack2(s1[2], s1[3]) : 0u;
#pragma unroll
      for (int r = 0; r < 4; ++r) w[r] = w[r] == KV_EMPTY ? 0x7FC07FC0u : w[r];
      const int slot = e < 16 ? g3 * 16 + e : 64 + g3 * 16 + (e < 20 ? (e - 16) * 4 : 4 * q + 1);
      if ((q == 0 || e == 20) && u < 63)
        asm volatile("global_store_dwordx4 %0, %1, off sc1" :: "v"(ws + OFF_KVF + ((size_t)bid * 128 + slot) * 16), "v"(w) : "memory");
    }
    {
      const char* pqg = ws + OFF_PQ + ((size_t)(mt * 4) * 32 + wq * 8) * 640 + lane * 16;
#pragma unroll
      for (int tg = 0; tg < 4; ++tg)
#pragma unroll
        for (int i = 0; i < 5; ++i)
          __builtin_amdgcn_global_load_lds((const unsigned*)(pqg + tg * 20480 + i * 1024), (unsigned*)(reg + tg * 5120 + i * 1024), 16, 0, 0);
    }
    const char* wosrc = ws + OFF_WO + (size_t)(nt * 4 + wq) * 25600 + lane * 16;
    s16x8 w2[K2_DW][5];
    auto ldW = [&](int s) {
#pragma unroll
      for (int b = 0; b < 5; ++b) w2[s % K2_DW][b] = *(const s16x8*)(wosrc + (s * 5 + b) * 1024);
    };
#pragma unroll
    for (int s = 0; s < K2_DW; ++s) ldW(s);
    SCHED;
    const char* kvl = reg + 20480 + lane * 16;
    {
      const int hp = lane & 3;
      const char* hint = ws + OFF_KVF + ((size_t)(wq * 8 + ((lane >> 2) & 7)) * 128 + (hp == 3 ? 64 : hp * 21)) * 16;
      unsigned hv;
      while (true) {
        asm volatile("global_load_dword %0, %1, off sc1\n\ts_waitcnt vmcnt(0)" : "=v"(hv) : "v"(hint) : "memory");
        if (__builtin_amdgcn_ballot_w64(hv == KV_EMPTY) == 0ull) break;
        __builtin_amdgcn_s_sleep(4);
      }
      const char* kg = ws + OFF_KVF + (size_t)(wq * 8) * 2048 + lane * 16;
      bool bad;
      do {
#pragma unroll
        for (int i = 0; i < 16; ++i) __builtin_amdgcn_global_load_lds((const unsigned*)(kg + i * 1024), (unsigned*)(reg + 20480 + i * 1024), 16, 0, KVAUX);
        asm volatile("s_waitcnt vmcnt(0)" ::: "memory");
        bad = false;
#pragma unroll
        for (int i = 0; i < 16; ++i) {
          const u32x4 v = *(const __attribute__((address_space(3))) u32x4*)(kvl + i * 1024);
          bad = bad || v[0] == KV_EMPTY || v[1] == KV_EMPTY || v[2] == KV_EMPTY || v[3] == KV_EMPTY;
        }
      } while (__builtin_amdgcn_ballot_w64(bad) != 0ull);
    }
    __builtin_amdgcn_s_waitcnt(0x0F70);
    SCHED;
    STAMPW(4);
    const char* pql = reg + (g < 2 ? g * 256 + c * 16 : 512 + c * 8);
    const f32x4 z4 = (f32x4){0.f, 0.f, 0.f, 0.f};
    unsigned ow[4][20];
    auto mma2 = [&](int s) {
#pragma unroll
      for (int a = 0; a < 4; ++a) {
        const u32x4 o4 = (u32x4){ow[a][4 * s], ow[a][4 * s + 1], ow[a][4 * s + 2], ow[a][4 * s + 3]};
        const s16x8 of = __builtin_bit_cast(s16x8, o4);
#pragma unroll
        for (int b = 0; b < 5; ++b) acc[a][b] = __builtin_amdgcn_mfma_f32_16x16x32_bf16(w2[s % K2_DW][b], of, acc[a][b], 0, 0, 0);
      }
      if (s + K2_DW < 5) ldW(s + K2_DW);
    };
#pragma unroll
    for (int m = 0; m < 4; ++m) {
      f32x4 ov[2][4]; float oe[2][4];
#pragma unroll
      for (int h2 = 0; h2 < 2; ++h2) {
        const int hh = 2 * m + h2;
        const s16x8 k0 = *(const s16x8*)(kvl + hh * 2048), k1 = *(const s16x8*)(kvl + hh * 2048 + 1024);
#pragma unroll
        for (int tg = 0; tg < 4; ++tg) {
          const u32x4 pq = *(const u32x4_a8*)(pql + tg * 5120 + hh * 640);
          u32x4 pk4;
          pk4[0] = g < 3 ? pq[0] : 0u; pk4[1] = g < 3 ? pq[1] : 0u;
          pk4[2] = g < 2 ? pq[2] : 0u; pk4[3] = g < 2 ? pq[3] : 0u;
          const s16x8 pf = __builtin_bit_cast(s16x8, pk4);
          const f32x4 d1 = __builtin_amdgcn_mfma_f32_16x16x32_bf16(k1, pf, z4, 0, 0, 0);
          const f32x4 d0 = __builtin_amdgcn_mfma_f32_16x16x32_bf16(k0, pf, z4, 0, 0, 0);
          const float rden = frcp(bfr(bfr(d1[1]) + 1e-6f));
          const f32x2 lo = bfr2(lo2(d0)) * rden, hi = bfr2(hi2(d0)) * rden;
          ov[h2][tg] = (f32x4){lo[0], lo[1], hi[0], hi[1]};
          oe[h2][tg] = bfr(d1[0]) * rden;
        }
      }
#pragma unroll
      for (int tg = 0; tg < 4; ++tg) {
        ow[tg][5 * m + 0] = cvt2(lo2(ov[0][tg]));
        ow[tg][5 * m + 1] = cvt2(hi2(ov[0][tg]));
        ow[tg][5 * m + 2] = cvt2((f32x2){oe[0][tg], ov[1][tg][0]});
        ow[tg][5 * m + 3] = cvt2((f32x2){ov[1][tg][1], ov[1][tg][2]});
        ow[tg][5 * m + 4] = cvt2((f32x2){ov[1][tg][3], oe[1][tg]});
      }
      mma2(m);
      if (m == 3) mma2(4);
      SCHED;
      if (m < 2) STAMPW(5 + m);
    }
  }
  __syncthreads();

  f32x4* red = (f32x4*)smem;
#pragma unroll
  for (int a = 0; a < 4; ++a)
#pragma unroll
    for (int b = 0; b < 5; ++b) red[(wave * 20 + a * 5 + b) * 64 + lane] = acc[a][b];
  __syncthreads();
#pragma unroll
  for (int i = 0; i < 3; ++i) {
    if (i < nb) {
      const int f = wq * 5 + b0 + i;
      f32x4 y1 = red[(0 * 20 + f) * 64 + lane], y2 = red[(4 * 20 + f) * 64 + lane];
#pragma unroll
      for (int sw = 1; sw < 4; ++sw) { y1 += red[(sw * 20 + f) * 64 + lane]; y2 += red[((4 + sw) * 20 + f) * 64 + lane]; }
      const int n = nt * 80 + (b0 + i) * 16 + 4 * g;
      u32x2 w;
      {
        const f32x2 y = bfr2(lo2(y1)), gs = bfr2(y * sigm2(y)), sv = bfr2(lo2(y2));
        w[0] = cvt2(unp2(xw[i][0]) + bfr2(sv * gs));
      }
      {
        const f32x2 y = bfr2(hi2(y1)), gs = bfr2(y * sigm2(y)), sv = bfr2(hi2(y2));
        w[1] = cvt2(unp2(xw[i][1]) + bfr2(sv * gs));
      }
      *(u32x2*)(out + (size_t)tok * D_ + n) = w;
    }
  }
  STAMP(2, 3);
}

extern "C" void kernel_launch(void* const* d_in, const int* in_sizes, int n_in,
                              void* d_out, int out_size, void* d_ws, size_t ws_size,
                              hipStream_t stream) {
  const u16* x = (const u16*)d_in[0];
  const u16* wqkv = (const u16*)d_in[1];
  const u16* wo = (const u16*)d_in[2];
  const u16* wg = (const u16*)d_in[4];
  const u16* wu = (const u16*)d_in[5];
  const u16* wd = (const u16*)d_in[6];
  char* ws = (char*)d_ws;
  u16* out = (u16*)d_out;

  hipFuncSetAttribute((const void*)k1_gemm, hipFuncAttributeMaxDynamicSharedMemorySize, K1_NS * K1_STAGE);
  hipFuncSetAttribute((const void*)k2_gemm, hipFuncAttributeMaxDynamicSharedMemorySize, K2_LDS);

  k0_prep<<<512, 256, 0, stream>>>(x, ws, wqkv, wg, wu);
  k1_gemm<<<256, 512, K1_NS * K1_STAGE, stream>>>(wqkv, wg, wu, wo, wd, ws);
  k2_gemm<<<256, 512, K2_LDS, stream>>>(x, ws, out);
}
```

```cpp
#define STM 1
#include <hip/hip_runtime.h>
#include <hip/hip_bf16.h>
#include <stdint.h>

typedef unsigned short u16;
typedef __attribute__((ext_vector_type(8))) short s16x8;
typedef __attribute__((ext_vector_type(4))) short s16x4;
typedef __attribute__((ext_vector_type(4))) float f32x4;
typedef __attribute__((ext_vector_type(16))) float f32x16;
typedef __attribute__((ext_vector_type(2))) unsigned u32x2;
typedef __attribute__((ext_vector_type(4))) unsigned u32x4;
typedef u32x4 u32x4_a8 __attribute__((aligned(8)));
typedef __attribute__((address_space(3))) s16x4* lds_s16x4_ptr;
typedef __attribute__((address_space(1))) unsigned gu32;
typedef __attribute__((address_space(1))) unsigned long long gu64;

#define RLX_AGENT __ATOMIC_RELAXED, __HIP_MEMORY_SCOPE_AGENT

constexpr int S_ = 2048, D_ = 640, F_ = 1728, NQKV_ = 1920;

constexpr size_t OFF_CNT   = 0;
constexpr size_t OFF_XN    = 8192;
constexpr size_t SZ_XN     = (size_t)S_ * D_ * 2;
constexpr size_t OFF_H     = OFF_XN + SZ_XN;
constexpr size_t SZ_H      = (size_t)S_ * F_ * 2;
constexpr size_t OFF_PQ    = OFF_H + SZ_H;
constexpr size_t SZ_PQ     = (size_t)S_ * D_ * 2;
constexpr size_t OFF_WC    = OFF_PQ + SZ_PQ;
constexpr size_t SZ_WC     = (size_t)8 * 54 * 5 * 1024;
constexpr size_t OFF_WO    = OFF_WC + SZ_WC;
constexpr size_t SZ_WO     = (size_t)8 * 4 * 5 * 5 * 1024;
constexpr size_t OFF_KVP   = OFF_WO + SZ_WO;
constexpr size_t SZ_KVP    = (size_t)16 * 32 * 504 * 4;
constexpr size_t OFF_KVF   = OFF_KVP + SZ_KVP;
constexpr size_t SZ_KVF    = 65536;

#define KVAUX 16
constexpr unsigned KV_EMPTY = 0xFFFFFFFFu;
__device__ __forceinline__ float bf2f(u16 v) { return __uint_as_float(((unsigned)v) << 16); }
__device__ __forceinline__ u16 f2bf(float f) { return __builtin_bit_cast(u16, (__bf16)f); }
__device__ __forceinline__ float bfr(float f) { return bf2f(f2bf(f)); }
__device__ __forceinline__ unsigned pack2(float a, float b) { return (unsigned)f2bf(a) | ((unsigned)f2bf(b) << 16); }
__device__ __forceinline__ float phi_f(float t) {
  float p = t > 0.f ? bfr(t + 1.0f) : bfr(__expf(t));
  return bfr(p + 1e-6f);
}
typedef __attribute__((ext_vector_type(2))) float f32x2;
typedef __attribute__((ext_vector_type(2))) __bf16 bf16x2;
__device__ __forceinline__ unsigned cvt2(f32x2 v) { return __builtin_bit_cast(unsigned, __builtin_convertvector(v, bf16x2)); }
__device__ __forceinline__ f32x2 unp2(unsigned u) { return (f32x2){__uint_as_float(u << 16), __uint_as_float(u & 0xffff0000u)}; }
__device__ __forceinline__ f32x2 bfr2(f32x2 v) { return unp2(cvt2(v)); }
__device__ __forceinline__ f32x2 lo2(f32x4 v) { return __builtin_shufflevector(v, v, 0, 1); }
__device__ __forceinline__ f32x2 hi2(f32x4 v) { return __builtin_shufflevector(v, v, 2, 3); }
__device__ __forceinline__ unsigned phi2(f32x2 a) {
  const f32x2 t = bfr2(a), m = t * 1.44269504088896340736f, p = t + 1.0f;
  f32x2 sel;
  sel[0] = t[0] > 0.f ? p[0] : __builtin_amdgcn_exp2f(m[0]);
  sel[1] = t[1] > 0.f ? p[1] : __builtin_amdgcn_exp2f(m[1]);
  return cvt2(bfr2(sel) + 1e-6f);
}
__device__ __forceinline__ f32x2 sigm2(f32x2 g) {
  const f32x2 m = g * -1.44269504088896340736f;
  f32x2 e; e[0] = __builtin_amdgcn_exp2f(m[0]); e[1] = __builtin_amdgcn_exp2f(m[1]);
  e = e + 1.0f;
  f32x2 r; r[0] = __builtin_amdgcn_rcpf(e[0]); r[1] = __builtin_amdgcn_rcpf(e[1]);
  return r;
}
__device__ __forceinline__ int aswz(int row) { int x = (row >> 2) & 3; return (((x ^ (x >> 1)) & 1) << 1) | (x >> 1); }

#define GST16(p, v) __builtin_nontemporal_store((u32x4)(v), (u32x4*)(p))
#define GST8(p, v) __builtin_nontemporal_store((u32x2)(v), (u32x2*)(p))
#define WAIT_V(n) asm volatile("s_waitcnt vmcnt(" #n ")" ::: "memory")
#define STAMP(kid, idx)
#define STAMPW(idx)
#define WAIT_L0 asm volatile("s_waitcnt lgkmcnt(0)" ::: "memory")
#define BAR __builtin_amdgcn_s_barrier()
#define SCHED __builtin_amdgcn_sched_barrier(0)
__device__ __forceinline__ float frcp(float v) { return __builtin_amdgcn_rcpf(v); }

constexpr int RT_WD = 216 * 80, RT_ITEMS = RT_WD + 80 * 80, RT_R = 3;
__device__ __forceinline__ void retile_all(int t0, const u16* __restrict__ wo, const u16* __restrict__ wdown, char* __restrict__ ws) {
  u32x4 r[RT_R][8];
  char* dst[RT_R];
#pragma unroll
  for (int it = 0; it < RT_R; ++it) {
    int t = t0 + it * 8192; if (t >= RT_ITEMS) t = t0;
    const bool isd = t < RT_WD;
    const int t2 = isd ? t : t - RT_WD;
    const int ko = t2 / 80, noct = t2 - ko * 80;
    const int n0 = noct * 8, nt = n0 / 80, nn = n0 - nt * 80, nf = nn >> 4, c0 = nn & 15, gg = ko & 3, kq = ko >> 2;
    const int wq = kq / 5, ks = kq - wq * 5;
    const u16* src = (isd ? wdown : wo) + n0;
#pragma unroll
    for (int jj = 0; jj < 8; ++jj) {
      const int q = 8 * ks + jj, hh = q / 5, rr = q - hh * 5;
      const int row = isd ? ko * 8 + jj : (wq * 8 + hh) * 20 + (rr < 4 ? 4 * gg + rr : 16 + gg);
      r[it][jj] = *(const u32x4*)(src + (size_t)row * D_);
    }
    dst[it] = ws + (isd ? OFF_WC + (size_t)(nt * 54 + kq) * 5120 : OFF_WO + (size_t)(nt * 20 + kq) * 5120) + (size_t)(nf * 64 + gg * 16 + c0) * 16;
  }
#pragma unroll
  for (int it = 0; it < RT_R; ++it) {
#pragma unroll
    for (int i = 0; i < 4; ++i) {
      u32x4 lo, hi;
#pragma unroll
      for (int q = 0; q < 4; ++q) {
        lo[q] = (r[it][2 * q][i] & 0xffffu) | (r[it][2 * q + 1][i] << 16);
        hi[q] = (r[it][2 * q][i] >> 16) | (r[it][2 * q + 1][i] & 0xffff0000u);
      }
      GST16(dst[it] + (2 * i) * 16, lo);
      GST16(dst[it] + (2 * i + 1) * 16, hi);
    }
  }
}

__global__ __launch_bounds__(256) void k0_prep(const u16* __restrict__ x, char* __restrict__ ws, const u16* __restrict__ wqkv,
                                               const u16* __restrict__ wg, const u16* __restrict__ wu) {
  const int tid = threadIdx.x;
  if (blockIdx.x >= 256) {
    const int t = (blockIdx.x - 256) * 256 + tid;
    unsigned v0, v1;
    const int l1 = t + 65536 < 107520 ? t + 65536 : t;
    const char* p0 = t < 38400 ? (const char*)wqkv + (size_t)t * 64 : (const char*)wg + (size_t)(t - 38400) * 64;
    const char* p1 = l1 < 38400 ? (const char*)wqkv + (size_t)l1 * 64 : (l1 < 72960 ? (const char*)wg + (size_t)(l1 - 38400) * 64 : (const char*)wu + (size_t)(l1 - 72960) * 64);
    asm volatile("global_load_dword %0, %2, off\n\tglobal_load_dword %1, %3, off\n\ts_waitcnt vmcnt(0)" : "=&v"(v0), "=&v"(v1) : "v"(p0), "v"(p1) : "memory");
    return;
  }
  {
    if (blockIdx.x < 16) {
      const int ci = blockIdx.x * 256 + tid, fr = (ci >> 6) & 1, gg = (ci >> 4) & 3, cc = ci & 15;
      const unsigned v = (gg < 3 && (fr == 0 || (cc & 3) < 2)) ? KV_EMPTY : 0u;
      *(u32x4*)(ws + OFF_KVF + (size_t)ci * 16) = (u32x4){v, v, v, v};
    }
    const int row = blockIdx.x * 8 + (tid >> 5);
    const int l32 = tid & 31;
    const u16* xr = x + (size_t)row * D_;
    float v[20];
    float sum = 0.f;
#pragma unroll
    for (int i = 0; i < 5; ++i) {
      u32x2 w = *(const u32x2*)(xr + (l32 + 32 * i) * 4);
      v[4 * i + 0] = __uint_as_float(w[0] << 16);
      v[4 * i + 1] = __uint_as_float(w[0] & 0xffff0000u);
      v[4 * i + 2] = __uint_as_float(w[1] << 16);
      v[4 * i + 3] = __uint_as_float(w[1] & 0xffff0000u);
      sum += v[4 * i] + v[4 * i + 1] + v[4 * i + 2] + v[4 * i + 3];
    }
#pragma unroll
    for (int o = 16; o >= 1; o >>= 1) sum += __shfl_xor(sum, o);
    const float mean = sum * (1.0f / D_);
    float sq = 0.f;
#pragma unroll
    for (int i = 0; i < 20; ++i) { float d = v[i] - mean; sq += d * d; }
#pragma unroll
    for (int o = 16; o >= 1; o >>= 1) sq += __shfl_xor(sq, o);
    const float mb = bfr(mean);
    const float vb = bfr(sq * (1.0f / D_));
    const float sd = bfr(sqrtf(bfr(vb + 1e-5f)));
    u16* xn = (u16*)(ws + OFF_XN);
    const int mt = row >> 7, r = row & 127, sw = aswz(r);
#pragma unroll
    for (int i = 0; i < 5; ++i) {
      const int k = (l32 + 32 * i) * 4;
      float o0 = bfr(v[4 * i + 0] - mb) / sd, o1 = bfr(v[4 * i + 1] - mb) / sd;
      float o2 = bfr(v[4 * i + 2] - mb) / sd, o3 = bfr(v[4 * i + 3] - mb) / sd;
      u32x2 w; w[0] = pack2(o0, o1); w[1] = pack2(o2, o3);
      const int kt = k >> 5, ch = (k >> 3) & 3, hf = (k >> 2) & 1;
      size_t off = ((size_t)(mt * 20 + kt) * 128 + r) * 32 + ((ch ^ sw) << 3) + (hf << 2);
      GST8(xn + off, w);
    }
  }
}

constexpr int K1_STAGE = 32768;
#define K1NS 4
constexpr int K1_NS = K1NS;

template <int TYPE>
__device__ __forceinline__ void k1_body(const u16* __restrict__ wA, const u16* __restrict__ wB, char* __restrict__ ws,
                                        int mt, int j, char* smem) {
  constexpr int NCB = TYPE == 0 ? 6 : 5;
  constexpr int NFR = TYPE == 0 ? 6 : 5;
  constexpr int LDW = TYPE == 0 ? F_ : NQKV_;
  const int tid = threadIdx.x, lane = tid & 63;
  const int wave = __builtin_amdgcn_readfirstlane(tid >> 6);
  const int wm = wave >> 2, wn = (TYPE == 2 && wave >= 4) ? ((wave + 2) & 3) : (wave & 3);
  const int g = lane >> 4, c = lane & 15;

  const char* asrc = ws + OFF_XN + (size_t)mt * 20 * 8192 + wave * 1024 + lane * 16;
  const u16* bsrc[3];
#pragma unroll
  for (int t = 0; t < 3; ++t) {
    int ii = wave + 8 * t; if (ii >= NCB * 4) ii = wave;
    const int cb = ii >> 2, krow = (ii & 3) * 8 + (lane >> 3), u = lane & 7;
    const int sk = ((krow >> 1) & 1) | (((krow >> 3) & 1) << 1);
    const int ch = (u >> 1) ^ sk;
    const int lc = cb * 64 + ch * 16 + (u & 1) * 8;
    const u16* p;
    if (TYPE == 0) p = (lc < 192) ? (wA + j * 192 + lc) : (wB + j * 192 + (lc - 192));
    else if (TYPE == 1) p = wA + j * 320 + lc;
    else p = (lc < 160) ? (wA + 640 + j * 160 + lc) : (wA + 1280 + j * 160 + (lc - 160));
    bsrc[t] = p + (size_t)krow * LDW;
  }
  const bool has3 = (NCB == 6) || (wave < 4);

  auto issue = [&](int kt) {
    char* sb = smem + (kt % K1_NS) * K1_STAGE;
    __builtin_amdgcn_global_load_lds((const unsigned*)(asrc + (size_t)kt * 8192), (unsigned*)(sb + wave * 1024), 16, 0, 0);
    __builtin_amdgcn_global_load_lds((const unsigned*)(bsrc[0] + (size_t)kt * 32 * LDW), (unsigned*)(sb + 8192 + wave * 1024), 16, 0, 0);
    __builtin_amdgcn_global_load_lds((const unsigned*)(bsrc[1] + (size_t)kt * 32 * LDW), (unsigned*)(sb + 8192 + (wave + 8) * 1024), 16, 0, 0);
    if (has3) __builtin_amdgcn_global_load_lds((const unsigned*)(bsrc[2] + (size_t)kt * 32 * LDW), (unsigned*)(sb + 8192 + (wave + 16) * 1024), 16, 0, 0);
  };

  const int xoff = (wm * 64 + c) * 64 + ((g ^ aswz(c)) << 4);
  int woff[NFR];
  {
    const int q = (lane & 15) >> 2, p = lane & 3;
    const int s = (q >> 1) | ((g & 1) << 1);
    const int lo = (8 * g + q) * 128 + (s << 5) + p * 8;
#pragma unroll
    for (int i = 0; i < NFR; ++i) {
      const int nf = TYPE == 0 ? (i < 3 ? 3 * wn + i : 12 + 3 * wn + (i - 3)) : 5 * wn + i;
      woff[i] = 8192 + (nf >> 2) * 4096 + (lo ^ ((nf & 3) << 5));
    }
  }

  f32x4 acc[4][NFR];
#pragma unroll
  for (int a = 0; a < 4; ++a)
#pragma unroll
    for (int b = 0; b < NFR; ++b) acc[a][b] = (f32x4){0.f, 0.f, 0.f, 0.f};

  auto readfr = [&](int kt, s16x8 (&xf)[4], s16x8 (&wf)[NFR]) {
    const char* sb = smem + (kt % K1_NS) * K1_STAGE;
#pragma unroll
    for (int b = 0; b < NFR; ++b) {
      s16x4 lo = __builtin_amdgcn_ds_read_tr16_b64_v4i16((lds_s16x4_ptr)(sb + woff[b]));
      s16x4 hi = __builtin_amdgcn_ds_read_tr16_b64_v4i16((lds_s16x4_ptr)(sb + woff[b] + 512));
      wf[b] = __builtin_shufflevector(lo, hi, 0, 1, 2, 3, 4, 5, 6, 7);
    }
#pragma unroll
    for (int a = 0; a < 4; ++a) xf[a] = *(const s16x8*)(sb + xoff + a * 1024);
  };
  auto mma = [&](const s16x8 (&xf)[4], const s16x8 (&wf)[NFR]) {
#pragma unroll
    for (int a = 0; a < 4; ++a)
#pragma unroll
      for (int b = 0; b < NFR; ++b) {
        if (TYPE == 2) acc[a][b] = __builtin_amdgcn_mfma_f32_16x16x32_bf16(xf[a], wf[b], acc[a][b], 0, 0, 0);
        else           acc[a][b] = __builtin_amdgcn_mfma_f32_16x16x32_bf16(wf[b], xf[a], acc[a][b], 0, 0, 0);
      }
  };
#define WAITN(N) asm volatile("s_waitcnt vmcnt(%0)" :: "n"(N) : "memory")
#define K1_WAIT(n) do { if (has3) WAITN(4 * (n)); else WAITN(3 * (n)); } while (0)

  s16x8 xA[4], wAf[NFR], xB[4], wBf[NFR];
#define SGB(m, n) __builtin_amdgcn_sched_group_barrier(m, n, 0)
  auto pattern_full = [&]() {
    SGB(0x10, 1); SGB(0x8, 1); SGB(0x10, 1); SGB(0x8, 1); SGB(0x10, 1); SGB(0x8, 1); SGB(0x10, 1); SGB(0x8, 1);
#pragma unroll
    for (int i = 0; i < 4 + 2 * NFR; ++i) { SGB(0x100, 1); SGB(0x8, 1); }
    SGB(0x8, 4);
  };
  auto pattern_tail = [&]() {
#pragma unroll
    for (int i = 0; i < 4 + 2 * NFR; ++i) { SGB(0x100, 1); SGB(0x8, 1); }
    SGB(0x8, 8);
  };
  STAMP(1, 0);
#pragma unroll
  for (int i = 0; i < K1_NS; ++i) issue(i);
  K1_WAIT(K1_NS - 1);
  BAR;
  STAMP(1, 1);
  readfr(0, xA, wAf);
  SCHED;
#pragma unroll
  for (int kt = 0; kt < 19; ++kt) {
    if (kt + K1_NS <= 20) K1_WAIT(K1_NS - 2); else K1_WAIT(18 - kt);
    WAIT_L0; BAR; SCHED;
    if (kt + K1_NS < 20) issue(kt + K1_NS);
    if (kt & 1) { readfr(kt + 1, xA, wAf); mma(xB, wBf); }
    else        { readfr(kt + 1, xB, wBf); mma(xA, wAf); }
    if (kt + K1_NS < 20) pattern_full(); else pattern_tail();
    SCHED;
  }
  mma(xB, wBf);

  WAIT_L0; BAR;
  STAMP(1, 2);
  if (TYPE == 0) {
#pragma unroll
    for (int a = 0; a < 4; ++a) {
#pragma unroll
      for (int b = 0; b < 3; ++b) {
        u32x2 w;
        w[0] = cvt2(bfr2(sigm2(bfr2(lo2(acc[a][b])))) * bfr2(lo2(acc[a][b + 3])));
        w[1] = cvt2(bfr2(sigm2(bfr2(hi2(acc[a][b])))) * bfr2(hi2(acc[a][b + 3])));
        const int f0 = wn * 48 + b * 16;
        const int ktl = f0 >> 5, gk = ((f0 >> 4) & 1) * 2 + (g >> 1);
        *(u32x2*)(smem + (((wm * 4 + a) * 6 + ktl) * 64 + gk * 16 + c) * 16 + (g & 1) * 8) = w;
      }
    }
    __syncthreads();
    char* H = ws + OFF_H + ((size_t)(mt * 8) * 54 + j * 6) * 1024;
#pragma unroll
    for (int i = 0; i < 6; ++i) {
      const int q = tid + 512 * i;
      const int m16 = q / 384, rest = q - m16 * 384;
      const u32x4 v = *(const u32x4*)(smem + q * 16);
      GST16(H + (size_t)m16 * 54 * 1024 + rest * 16, v);
    }
  } else if (TYPE == 1) {
#pragma unroll
    for (int a = 0; a < 4; ++a) {
#pragma unroll
      for (int b = 0; b < 5; ++b) {
        u32x2 w; w[0] = phi2(lo2(acc[a][b])); w[1] = phi2(hi2(acc[a][b]));
        const int n = wn * 80 + b * 16 + 4 * g, hl = n / 20, d0 = n - hl * 20;
        const int ro = d0 < 16 ? (d0 >> 3) * 256 + c * 16 + (d0 & 7) * 2 : 512 + c * 8;
        *(u32x2*)(smem + ((wm * 4 + a) * 16 + hl) * 640 + ro) = w;
      }
    }
    __syncthreads();
    char* PQ = ws + OFF_PQ + ((size_t)(mt * 8) * 32 + j * 16) * 640;
#pragma unroll
    for (int i = 0; i < 10; ++i) {
      const int q = tid + 512 * i;
      const int m16 = q / 640, rest = q - m16 * 640;
      const u32x4 v = *(const u32x4*)(smem + q * 16);
      GST16(PQ + (size_t)m16 * 20480 + rest * 16, v);
    }
  } else {
#pragma unroll
    for (int a = 0; a < 4; ++a)
#pragma unroll
      for (int b = 0; b < 5; ++b) {
        const int col = wn * 80 + b * 16 + c;
        u32x2 w;
        if (wn < 2) { w[0] = phi2(lo2(acc[a][b])); w[1] = phi2(hi2(acc[a][b])); }
        else        { w[0] = cvt2(lo2(acc[a][b])); w[1] = cvt2(hi2(acc[a][b])); }
        *(u32x2*)(smem + col * 272 + (wm * 64 + a * 16 + 4 * g) * 2) = w;
      }
    __syncthreads();
    const int r32 = lane & 31, hh = lane >> 5;
    f32x16 akv, az;
#pragma unroll
    for (int i = 0; i < 16; ++i) { akv[i] = 0.f; az[i] = 0.f; }
    s16x8 ones;
#pragma unroll
    for (int i = 0; i < 8; ++i) ones[i] = (short)0x3F80;
#pragma unroll
    for (int s = 0; s < 8; ++s) {
      s16x8 af = *(const s16x8*)(smem + (20 * wave + r32) * 272 + (16 * s + 8 * hh) * 2);
      s16x8 bfv = *(const s16x8*)(smem + (160 + 20 * wave + r32) * 272 + (16 * s + 8 * hh) * 2);
      akv = __builtin_amdgcn_mfma_f32_32x32x16_bf16(af, bfv, akv, 0, 0, 0);
      az = __builtin_amdgcn_mfma_f32_32x32x16_bf16(af, ones, az, 0, 0, 0);
    }
    const int head = j * 8 + wave;
    float* P = (float*)(ws + OFF_KVP) + (size_t)(mt * 32 + head) * 504;
    if (r32 <= 20) {
#pragma unroll
      for (int q = 0; q < 3; ++q) {
        const int d0 = 8 * q + 4 * hh;
        if (d0 < 20) {
          f32x4 val;
#pragma unroll
          for (int r = 0; r < 4; ++r) val[r] = (r32 == 20) ? az[4 * q + r] : akv[4 * q + r];
          *(f32x4*)(P + r32 * 24 + d0) = val;
        }
      }
    }
  }
}

__global__ __launch_bounds__(512, 2) void k1_gemm(const u16* __restrict__ wqkv, const u16* __restrict__ wg,
                                                   const u16* __restrict__ wu, const u16* __restrict__ wo,
                                                   const u16* __restrict__ wdown, char* __restrict__ ws) {
  extern __shared__ __attribute__((aligned(16))) char smem[];
  const int bid = blockIdx.x;
  if (bid >= 240) {
    retile_all((bid - 240) * 512 + threadIdx.x, wo, wdown, ws);
    return;
  }
  const int p = (bid & 7) * 30 + (bid >> 3);
  const int mh = p / 120, rem = p % 120, nt = rem >> 3, mt = mh * 8 + (rem & 7);
  if (nt < 4) k1_body<2>(wqkv, wqkv, ws, mt, nt, smem);
  else if (nt < 6) k1_body<1>(wqkv, wqkv, ws, mt, nt - 4, smem);
  else k1_body<0>(wg, wu, ws, mt, nt - 6, smem);
  STAMP(1, 3);
}

constexpr int K2_LDS = 163840;
#define K2DD 3
constexpr int K2_D = K2DD;
#define K2DW 3
constexpr int K2_DW = K2DW;

__global__ __launch_bounds__(512, 2) void k2_gemm(const u16* __restrict__ x, const char* __restrict__ ws, u16* __restrict__ out) {
  extern __shared__ __attribute__((aligned(16))) char smem[];
  const int tid = threadIdx.x, lane = tid & 63;
  const int wave = __builtin_amdgcn_readfirstlane(tid >> 6);
  const int wq = wave & 3;
  const int g = lane >> 4, c = lane & 15;
  const int bid = blockIdx.x;
  const int mt = (bid & 7) * 4 + (bid >> 6), nt = (bid >> 3) & 7;

  STAMP(2, 0);
  f32x4 acc[4][5];
#pragma unroll
  for (int a = 0; a < 4; ++a)
#pragma unroll
    for (int b = 0; b < 5; ++b) acc[a][b] = (f32x4){0.f, 0.f, 0.f, 0.f};
  const int b0 = wave < 4 ? 0 : 3, nb = wave < 4 ? 3 : 2;
  const int tok = mt * 64 + wq * 16 + c;
  u32x2 xw[3];
#pragma unroll
  for (int i = 0; i < 3; ++i) xw[i] = *(const u32x2*)(x + (size_t)tok * D_ + nt * 80 + (i < nb ? b0 + i : b0) * 16 + 4 * g);

  if (wave < 4) {
    const char* hsrc = ws + OFF_H + ((size_t)(mt * 4) * 54 + wq) * 1024 + lane * 16;
    const char* wsrc = ws + OFF_WC + ((size_t)nt * 54 + wq) * 5120 + lane * 16;
    s16x8 xr[K2_D][4], wr[K2_D][5];
    auto ld = [&](int slot, int i) {
#pragma unroll
      for (int b = 0; b < 5; ++b) wr[slot][b] = *(const s16x8*)(wsrc + (size_t)(20 * i + b) * 1024);
#pragma unroll
      for (int a = 0; a < 4; ++a) xr[slot][a] = *(const s16x8*)(hsrc + (size_t)(a * 54 + 4 * i) * 1024);
    };
    auto mma1 = [&](int slot) {
#pragma unroll
      for (int a = 0; a < 4; ++a)
#pragma unroll
        for (int b = 0; b < 5; ++b) acc[a][b] = __builtin_amdgcn_mfma_f32_16x16x32_bf16(wr[slot][b], xr[slot][a], acc[a][b], 0, 0, 0);
    };
#pragma unroll
    for (int i = 0; i < K2_D; ++i) ld(i, i);
    SCHED;
#pragma unroll
    for (int i = 0; i < 13; ++i) {
      mma1(i % K2_D);
      if (i + K2_D < 13) ld(i % K2_D, i + K2_D);
      else if (i + K2_D == 13) { if (wave < 2) ld(i % K2_D, 13); }
      SCHED;
    }
    if (wave < 2) mma1(13 % K2_D);
    STAMP(2, 1);
  } else {
    __builtin_amdgcn_s_setprio(3);
    char* reg = smem + wq * 36864;
    if (bid < 32) {
      const int t = tid - 256, u = t >> 2, q = t & 3, uu = u < 63 ? u : 62, e = uu / 3, g3 = uu - e * 3;
      const float* src = (const float*)(ws + OFF_KVP) + ((size_t)(4 * q) * 32 + bid) * 504 + e * 24 + 8 * g3;
      f32x4 lo[4], hi[4];
#pragma unroll
      for (int i = 0; i < 4; ++i) { lo[i] = *(const f32x4*)(src + i * 32 * 504); hi[i] = *(const f32x4*)(src + i * 32 * 504 + (g3 < 2 ? 4 : 0)); }
      f32x4 s0 = (lo[0] + lo[1]) + (lo[2] + lo[3]), s1 = (hi[0] + hi[1]) + (hi[2] + hi[3]);
#pragma unroll
      for (int o = 1; o <= 2; o <<= 1)
#pragma unroll
        for (int r = 0; r < 4; ++r) { s0[r] += __shfl_xor(s0[r], o); s1[r] += __shfl_xor(s1[r], o); }
      u32x4 w;
      w[0] = pack2(s0[0], s0[1]); w[1] = pack2(s0[2], s0[3]);
      w[2] = g3 < 2 ? pack2(s1[0], s1[1]) : 0u; w[3] = g3 < 2 ? pack2(s1[2], s1[3]) : 0u;
#pragma unroll
      for (int r = 0; r < 4; ++r) w[r] = w[r] == KV_EMPTY ? 0x7FC07FC0u : w[r];
      const int slot = e < 16 ? g3 * 16 + e : 64 + g3 * 16 + (e < 20 ? (e - 16) * 4 : 4 * q + 1);
      if ((q == 0 || e == 20) && u < 63)
        asm volatile("global_store_dwordx4 %0, %1, off sc1" :: "v"(ws + OFF_KVF + ((size_t)bid * 128 + slot) * 16), "v"(w) : "memory");
    }
    {
      const char* pqg = ws + OFF_PQ + ((size_t)(mt * 4) * 32 + wq * 8) * 640 + lane * 16;
#pragma unroll
      for (int tg = 0; tg < 4; ++tg)
#pragma unroll
        for (int i = 0; i < 5; ++i)
          __builtin_amdgcn_global_load_lds((const unsigned*)(pqg + tg * 20480 + i * 1024), (unsigned*)(reg + tg * 5120 + i * 1024), 16, 0, 0);
    }
    const char* wosrc = ws + OFF_WO + (size_t)(nt * 4 + wq) * 25600 + lane * 16;
    s16x8 w2[K2_DW][5];
    auto ldW = [&](int s) {
#pragma unroll
      for (int b = 0; b < 5; ++b) w2[s % K2_DW][b] = *(const s16x8*)(wosrc + (s * 5 + b) * 1024);
    };
#pragma unroll
    for (int s = 0; s < K2_DW; ++s) ldW(s);
    SCHED;
    const char* kvl = reg + 20480 + lane * 16;
    {
      const int hp = lane & 3;
      const char* hint = ws + OFF_KVF + ((size_t)(wq * 8 + ((lane >> 2) & 7)) * 128 + (hp == 3 ? 64 : hp * 21)) * 16;
      unsigned hv;
      while (true) {
        asm volatile("global_load_dword %0, %1, off sc1\n\ts_waitcnt vmcnt(0)" : "=v"(hv) : "v"(hint) : "memory");
        if (__builtin_amdgcn_ballot_w64(hv == KV_EMPTY) == 0ull) break;
        __builtin_amdgcn_s_sleep(4);
      }
      const char* kg = ws + OFF_KVF + (size_t)(wq * 8) * 2048 + lane * 16;
      bool bad;
      do {
#pragma unroll
        for (int i = 0; i < 16; ++i) __builtin_amdgcn_global_load_lds((const unsigned*)(kg + i * 1024), (unsigned*)(reg + 20480 + i * 1024), 16, 0, KVAUX);
        asm volatile("s_waitcnt vmcnt(0)" ::: "memory");
        bad = false;
#pragma unroll
        for (int i = 0; i < 16; ++i) {
          const u32x4 v = *(const __attribute__((address_space(3))) u32x4*)(kvl + i * 1024);
          bad = bad || v[0] == KV_EMPTY || v[1] == KV_EMPTY || v[2] == KV_EMPTY || v[3] == KV_EMPTY;
        }
      } while (__builtin_amdgcn_ballot_w64(bad) != 0ull);
    }
    __builtin_amdgcn_s_waitcnt(0x0F70);
    SCHED;
    STAMPW(4);
    const char* pql = reg + (g < 2 ? g * 256 + c * 16 : 512 + c * 8);
    const f32x4 z4 = (f32x4){0.f, 0.f, 0.f, 0.f};
    unsigned ow[4][20];
    auto mma2 = [&](int s) {
#pragma unroll
      for (int a = 0; a < 4; ++a) {
        const u32x4 o4 = (u32x4){ow[a][4 * s], ow[a][4 * s + 1], ow[a][4 * s + 2], ow[a][4 * s + 3]};
        const s16x8 of = __builtin_bit_cast(s16x8, o4);
#pragma unroll
        for (int b = 0; b < 5; ++b) acc[a][b] = __builtin_amdgcn_mfma_f32_16x16x32_bf16(w2[s % K2_DW][b], of, acc[a][b], 0, 0, 0);
      }
      if (s + K2_DW < 5) ldW(s + K2_DW);
    };
#pragma unroll
    for (int m = 0; m < 4; ++m) {
      f32x4 ov[2][4]; float oe[2][4];
#pragma unroll
      for (int h2 = 0; h2 < 2; ++h2) {
        const int hh = 2 * m + h2;
        const s16x8 k0 = *(const s16x8*)(kvl + hh * 2048), k1 = *(const s16x8*)(kvl + hh * 2048 + 1024);
#pragma unroll
        for (int tg = 0; tg < 4; ++tg) {
          const u32x4 pq = *(const u32x4_a8*)(pql + tg * 5120 + hh * 640);
          u32x4 pk4;
          pk4[0] = g < 3 ? pq[0] : 0u; pk4[1] = g < 3 ? pq[1] : 0u;
          pk4[2] = g < 2 ? pq[2] : 0u; pk4[3] = g < 2 ? pq[3] : 0u;
          const s16x8 pf = __builtin_bit_cast(s16x8, pk4);
          const f32x4 d1 = __builtin_amdgcn_mfma_f32_16x16x32_bf16(k1, pf, z4, 0, 0, 0);
          const f32x4 d0 = __builtin_amdgcn_mfma_f32_16x16x32_bf16(k0, pf, z4, 0, 0, 0);
          const float rden = frcp(bfr(bfr(d1[1]) + 1e-6f));
          const f32x2 lo = bfr2(lo2(d0)) * rden, hi = bfr2(hi2(d0)) * rden;
          ov[h2][tg] = (f32x4){lo[0], lo[1], hi[0], hi[1]};
          oe[h2][tg] = bfr(d1[0]) * rden;
        }
      }
#pragma unroll
      for (int tg = 0; tg < 4; ++tg) {
        ow[tg][5 * m + 0] = cvt2(lo2(ov[0][tg]));
        ow[tg][5 * m + 1] = cvt2(hi2(ov[0][tg]));
        ow[tg][5 * m + 2] = cvt2((f32x2){oe[0][tg], ov[1][tg][0]});
        ow[tg][5 * m + 3] = cvt2((f32x2){ov[1][tg][1], ov[1][tg][2]});
        ow[tg][5 * m + 4] = cvt2((f32x2){ov[1][tg][3], oe[1][tg]});
      }
      mma2(m);
      if (m == 3) mma2(4);
      SCHED;
      if (m < 2) STAMPW(5 + m);
    }
  }
  __syncthreads();

  f32x4* red = (f32x4*)smem;
#pragma unroll
  for (int a = 0; a < 4; ++a)
#pragma unroll
    for (int b = 0; b < 5; ++b) red[(wave * 20 + a * 5 + b) * 64 + lane] = acc[a][b];
  __syncthreads();
#pragma unroll
  for (int i = 0; i < 3; ++i) {
    if (i < nb) {
      const int f = wq * 5 + b0 + i;
      f32x4 y1 = red[(0 * 20 + f) * 64 + lane], y2 = red[(4 * 20 + f) * 64 + lane];
#pragma unroll
      for (int sw = 1; sw < 4; ++sw) { y1 += red[(sw * 20 + f) * 64 + lane]; y2 += red[((4 + sw) * 20 + f) * 64 + lane]; }
      const int n = nt * 80 + (b0 + i) * 16 + 4 * g;
      u32x2 w;
      {
        const f32x2 y = bfr2(lo2(y1)), gs = bfr2(y * sigm2(y)), sv = bfr2(lo2(y2));
        w[0] = cvt2(unp2(xw[i][0]) + bfr2(sv * gs));
      }
      {
        const f32x2 y = bfr2(hi2(y1)), gs = bfr2(y * sigm2(y)), sv = bfr2(hi2(y2));
        w[1] = cvt2(unp2(xw[i][1]) + bfr2(sv * gs));
      }
      *(u32x2*)(out + (size_t)tok * D_ + n) = w;
    }
  }
  STAMP(2, 3);
}

extern "C" void kernel_launch(void* const* d_in, const int* in_sizes, int n_in,
                              void* d_out, int out_size, void* d_ws, size_t ws_size,
                              hipStream_t stream) {
  const u16* x = (const u16*)d_in[0];
  const u16* wqkv = (const u16*)d_in[1];
  const u16* wo = (const u16*)d_in[2];
  const u16* wg = (const u16*)d_in[4];
  const u16* wu = (const u16*)d_in[5];
  const u16* wd = (const u16*)d_in[6];
  char* ws = (char*)d_ws;
  u16* out = (u16*)d_out;

  hipFuncSetAttribute((const void*)k1_gemm, hipFuncAttributeMaxDynamicSharedMemorySize, K1_NS * K1_STAGE);
  hipFuncSetAttribute((const void*)k2_gemm, hipFuncAttributeMaxDynamicSharedMemorySize, K2_LDS);

  k0_prep<<<512, 256, 0, stream>>>(x, ws, wqkv, wg, wu);
  k1_gemm<<<256, 512, K1_NS * K1_STAGE, stream>>>(wqkv, wg, wu, wo, wd, ws);
  k2_gemm<<<256, 512, K2_LDS, stream>>>(x, ws, out);
}
```

```cpp
#define K2DD 2
#include <hip/hip_runtime.h>
#include <hip/hip_bf16.h>
#include <stdint.h>

typedef unsigned short u16;
typedef __attribute__((ext_vector_type(8))) short s16x8;
typedef __attribute__((ext_vector_type(4))) short s16x4;
typedef __attribute__((ext_vector_type(4))) float f32x4;
typedef __attribute__((ext_vector_type(16))) float f32x16;
typedef __attribute__((ext_vector_type(2))) unsigned u32x2;
typedef __attribute__((ext_vector_type(4))) unsigned u32x4;
typedef u32x4 u32x4_a8 __attribute__((aligned(8)));
typedef __attribute__((address_space(3))) s16x4* lds_s16x4_ptr;
typedef __attribute__((address_space(1))) unsigned gu32;
typedef __attribute__((address_space(1))) unsigned long long gu64;

#define RLX_AGENT __ATOMIC_RELAXED, __HIP_MEMORY_SCOPE_AGENT

constexpr int S_ = 2048, D_ = 640, F_ = 1728, NQKV_ = 1920;

constexpr size_t OFF_CNT   = 0;
constexpr size_t OFF_XN    = 8192;
constexpr size_t SZ_XN     = (size_t)S_ * D_ * 2;
constexpr size_t OFF_H     = OFF_XN + SZ_XN;
constexpr size_t SZ_H      = (size_t)S_ * F_ * 2;
constexpr size_t OFF_PQ    = OFF_H + SZ_H;
constexpr size_t SZ_PQ     = (size_t)S_ * D_ * 2;
constexpr size_t OFF_WC    = OFF_PQ + SZ_PQ;
constexpr size_t SZ_WC     = (size_t)8 * 54 * 5 * 1024;
constexpr size_t OFF_WO    = OFF_WC + SZ_WC;
constexpr size_t SZ_WO     = (size_t)8 * 4 * 5 * 5 * 1024;
constexpr size_t OFF_KVP   = OFF_WO + SZ_WO;
constexpr size_t SZ_KVP    = (size_t)16 * 32 * 504 * 4;
constexpr size_t OFF_KVF   = OFF_KVP + SZ_KVP;
constexpr size_t SZ_KVF    = 65536;

#define KVAUX 16
constexpr unsigned KV_EMPTY = 0xFFFFFFFFu;
__device__ __forceinline__ float bf2f(u16 v) { return __uint_as_float(((unsigned)v) << 16); }
__device__ __forceinline__ u16 f2bf(float f) { return __builtin_bit_cast(u16, (__bf16)f); }
__device__ __forceinline__ float bfr(float f) { return bf2f(f2bf(f)); }
__device__ __forceinline__ unsigned pack2(float a, float b) { return (unsigned)f2bf(a) | ((unsigned)f2bf(b) << 16); }
__device__ __forceinline__ float phi_f(float t) {
  float p = t > 0.f ? bfr(t + 1.0f) : bfr(__expf(t));
  return bfr(p + 1e-6f);
}
typedef __attribute__((ext_vector_type(2))) float f32x2;
typedef __attribute__((ext_vector_type(2))) __bf16 bf16x2;
__device__ __forceinline__ unsigned cvt2(f32x2 v) { return __builtin_bit_cast(unsigned, __builtin_convertvector(v, bf16x2)); }
__device__ __forceinline__ f32x2 unp2(unsigned u) { return (f32x2){__uint_as_float(u << 16), __uint_as_float(u & 0xffff0000u)}; }
__device__ __forceinline__ f32x2 bfr2(f32x2 v) { return unp2(cvt2(v)); }
__device__ __forceinline__ f32x2 lo2(f32x4 v) { return __builtin_shufflevector(v, v, 0, 1); }
__device__ __forceinline__ f32x2 hi2(f32x4 v) { return __builtin_shufflevector(v, v, 2, 3); }
__device__ __forceinline__ unsigned phi2(f32x2 a) {
  const f32x2 t = bfr2(a), m = t * 1.44269504088896340736f, p = t + 1.0f;
  f32x2 sel;
  sel[0] = __builtin_amdgcn_fmed3f(__builtin_amdgcn_exp2f(m[0]), p[0], 1.0f);
  sel[1] = __builtin_amdgcn_fmed3f(__builtin_amdgcn_exp2f(m[1]), p[1], 1.0f);
  return cvt2(bfr2(sel) + 1e-6f);
}
__device__ __forceinline__ f32x2 sigm2(f32x2 g) {
  const f32x2 m = g * -1.44269504088896340736f;
  f32x2 e; e[0] = __builtin_amdgcn_exp2f(m[0]); e[1] = __builtin_amdgcn_exp2f(m[1]);
  e = e + 1.0f;
  f32x2 r; r[0] = __builtin_amdgcn_rcpf(e[0]); r[1] = __builtin_amdgcn_rcpf(e[1]);
  return r;
}
__device__ __forceinline__ int aswz(int row) { int x = (row >> 2) & 3; return (((x ^ (x >> 1)) & 1) << 1) | (x >> 1); }

#define WAIT_V(n) asm volatile("s_waitcnt vmcnt(" #n ")" ::: "memory")
#define STAMP(kid, idx)
#define STAMPW(idx)
#define WAIT_L0 asm volatile("s_waitcnt lgkmcnt(0)" ::: "memory")
#define BAR __builtin_amdgcn_s_barrier()
#define SCHED __builtin_amdgcn_sched_barrier(0)
__device__ __forceinline__ float frcp(float v) { return __builtin_amdgcn_rcpf(v); }

constexpr int RT_WD = 216 * 80, RT_ITEMS = RT_WD + 80 * 80, RT_R = 3;
__device__ __forceinline__ void retile_all(int t0, const u16* __restrict__ wo, const u16* __restrict__ wdown, char* __restrict__ ws) {
  u32x4 r[RT_R][8];
  char* dst[RT_R];
#pragma unroll
  for (int it = 0; it < RT_R; ++it) {
    int t = t0 + it * 8192; if (t >= RT_ITEMS) t = t0;
    const bool isd = t < RT_WD;
    const int t2 = isd ? t : t - RT_WD;
    const int ko = t2 / 80, noct = t2 - ko * 80;
    const int n0 = noct * 8, nt = n0 / 80, nn = n0 - nt * 80, nf = nn >> 4, c0 = nn & 15, gg = ko & 3, kq = ko >> 2;
    const int wq = kq / 5, ks = kq - wq * 5;
    const u16* src = (isd ? wdown : wo) + n0;
#pragma unroll
    for (int jj = 0; jj < 8; ++jj) {
      const int q = 8 * ks + jj, hh = q / 5, rr = q - hh * 5;
      const int row = isd ? ko * 8 + jj : (wq * 8 + hh) * 20 + (rr < 4 ? 4 * gg + rr : 16 + gg);
      r[it][jj] = *(const u32x4*)(src + (size_t)row * D_);
    }
    dst[it] = ws + (isd ? OFF_WC + (size_t)(nt * 54 + kq) * 5120 : OFF_WO + (size_t)(nt * 20 + kq) * 5120) + (size_t)(nf * 64 + gg * 16 + c0) * 16;
  }
#pragma unroll
  for (int it = 0; it < RT_R; ++it) {
#pragma unroll
    for (int i = 0; i < 4; ++i) {
      u32x4 lo, hi;
#pragma unroll
      for (int q = 0; q < 4; ++q) {
        lo[q] = (r[it][2 * q][i] & 0xffffu) | (r[it][2 * q + 1][i] << 16);
        hi[q] = (r[it][2 * q][i] >> 16) | (r[it][2 * q + 1][i] & 0xffff0000u);
      }
      *(u32x4*)(dst[it] + (2 * i) * 16) = lo;
      *(u32x4*)(dst[it] + (2 * i + 1) * 16) = hi;
    }
  }
}

__global__ __launch_bounds__(256) void k0_prep(const u16* __restrict__ x, char* __restrict__ ws, const u16* __restrict__ wqkv,
                                               const u16* __restrict__ wg, const u16* __restrict__ wu) {
  const int tid = threadIdx.x;
  if (blockIdx.x >= 256) {
    const int t = (blockIdx.x - 256) * 256 + tid;
    unsigned v0, v1;
    const int l1 = t + 65536 < 107520 ? t + 65536 : t;
    const char* p0 = t < 38400 ? (const char*)wqkv + (size_t)t * 64 : (const char*)wg + (size_t)(t - 38400) * 64;
    const char* p1 = l1 < 38400 ? (const char*)wqkv + (size_t)l1 * 64 : (l1 < 72960 ? (const char*)wg + (size_t)(l1 - 38400) * 64 : (const char*)wu + (size_t)(l1 - 72960) * 64);
    asm volatile("global_load_dword %0, %2, off\n\tglobal_load_dword %1, %3, off\n\ts_waitcnt vmcnt(0)" : "=&v"(v0), "=&v"(v1) : "v"(p0), "v"(p1) : "memory");
    return;
  }
  {
    if (blockIdx.x < 16) {
      const int ci = blockIdx.x * 256 + tid, fr = (ci >> 6) & 1, gg = (ci >> 4) & 3, cc = ci & 15;
      const unsigned v = (gg < 3 && (fr == 0 || (cc & 3) < 2)) ? KV_EMPTY : 0u;
      *(u32x4*)(ws + OFF_KVF + (size_t)ci * 16) = (u32x4){v, v, v, v};
    }
    const int row = blockIdx.x * 8 + (tid >> 5);
    const int l32 = tid & 31;
    const u16* xr = x + (size_t)row * D_;
    float v[20];
    float sum = 0.f;
#pragma unroll
    for (int i = 0; i < 5; ++i) {
      u32x2 w = *(const u32x2*)(xr + (l32 + 32 * i) * 4);
      v[4 * i + 0] = __uint_as_float(w[0] << 16);
      v[4 * i + 1] = __uint_as_float(w[0] & 0xffff0000u);
      v[4 * i + 2] = __uint_as_float(w[1] << 16);
      v[4 * i + 3] = __uint_as_float(w[1] & 0xffff0000u);
      sum += v[4 * i] + v[4 * i + 1] + v[4 * i + 2] + v[4 * i + 3];
    }
#pragma unroll
    for (int o = 16; o >= 1; o >>= 1) sum += __shfl_xor(sum, o);
    const float mean = sum * (1.0f / D_);
    float sq = 0.f;
#pragma unroll
    for (int i = 0; i < 20; ++i) { float d = v[i] - mean; sq += d * d; }
#pragma unroll
    for (int o = 16; o >= 1; o >>= 1) sq += __shfl_xor(sq, o);
    const float mb = bfr(mean);
    const float vb = bfr(sq * (1.0f / D_));
    const float sd = bfr(sqrtf(bfr(vb + 1e-5f)));
    const float rsd = 1.0f / sd;
    u16* xn = (u16*)(ws + OFF_XN);
    const int mt = row >> 7, r = row & 127, sw = aswz(r);
#pragma unroll
    for (int i = 0; i < 5; ++i) {
      const int k = (l32 + 32 * i) * 4;
      float o0 = bfr(v[4 * i + 0] - mb) * rsd, o1 = bfr(v[4 * i + 1] - mb) * rsd;
      float o2 = bfr(v[4 * i + 2] - mb) * rsd, o3 = bfr(v[4 * i + 3] - mb) * rsd;
      u32x2 w; w[0] = pack2(o0, o1); w[1] = pack2(o2, o3);
      const int kt = k >> 5, ch = (k >> 3) & 3, hf = (k >> 2) & 1;
      size_t off = ((size_t)(mt * 20 + kt) * 128 + r) * 32 + ((ch ^ sw) << 3) + (hf << 2);
      *(u32x2*)(xn + off) = w;
    }
  }
}

constexpr int K1_STAGE = 32768;
#define K1NS 4
constexpr int K1_NS = K1NS;

template <int TYPE>
__device__ __forceinline__ void k1_body(const u16* __restrict__ wA, const u16* __restrict__ wB, char* __restrict__ ws,
                                        int mt, int j, char* smem) {
  constexpr int NCB = TYPE == 0 ? 6 : 5;
  constexpr int NFR = TYPE == 0 ? 6 : 5;
  constexpr int LDW = TYPE == 0 ? F_ : NQKV_;
  const int tid = threadIdx.x, lane = tid & 63;
  const int wave = __builtin_amdgcn_readfirstlane(tid >> 6);
  const int wm = wave >> 2, wn = (TYPE == 2 && wave >= 4) ? ((wave + 2) & 3) : (wave & 3);
  const int g = lane >> 4, c = lane & 15;

  const char* asrc = ws + OFF_XN + (size_t)mt * 20 * 8192 + wave * 1024 + lane * 16;
  const u16* bsrc[3];
#pragma unroll
  for (int t = 0; t < 3; ++t) {
    int ii = wave + 8 * t; if (ii >= NCB * 4) ii = wave;
    const int cb = ii >> 2, krow = (ii & 3) * 8 + (lane >> 3), u = lane & 7;
    const int sk = ((krow >> 1) & 1) | (((krow >> 3) & 1) << 1);
    const int ch = (u >> 1) ^ sk;
    const int lc = cb * 64 + ch * 16 + (u & 1) * 8;
    const u16* p;
    if (TYPE == 0) p = (lc < 192) ? (wA + j * 192 + lc) : (wB + j * 192 + (lc - 192));
    else if (TYPE == 1) p = wA + j * 320 + lc;
    else p = (lc < 160) ? (wA + 640 + j * 160 + lc) : (wA + 1280 + j * 160 + (lc - 160));
    bsrc[t] = p + (size_t)krow * LDW;
  }
  const bool has3 = (NCB == 6) || (wave < 4);

  auto issue = [&](int kt) {
    char* sb = smem + (kt % K1_NS) * K1_STAGE;
    __builtin_amdgcn_global_load_lds((const unsigned*)(asrc + (size_t)kt * 8192), (unsigned*)(sb + wave * 1024), 16, 0, 0);
    __builtin_amdgcn_global_load_lds((const unsigned*)(bsrc[0] + (size_t)kt * 32 * LDW), (unsigned*)(sb + 8192 + wave * 1024), 16, 0, 0);
    __builtin_amdgcn_global_load_lds((const unsigned*)(bsrc[1] + (size_t)kt * 32 * LDW), (unsigned*)(sb + 8192 + (wave + 8) * 1024), 16, 0, 0);
    if (has3) __builtin_amdgcn_global_load_lds((const unsigned*)(bsrc[2] + (size_t)kt * 32 * LDW), (unsigned*)(sb + 8192 + (wave + 16) * 1024), 16, 0, 0);
  };

  const int xoff = (wm * 64 + c) * 64 + ((g ^ aswz(c)) << 4);
  int woff[NFR];
  {
    const int q = (lane & 15) >> 2, p = lane & 3;
    const int s = (q >> 1) | ((g & 1) << 1);
    const int lo = (8 * g + q) * 128 + (s << 5) + p * 8;
#pragma unroll
    for (int i = 0; i < NFR; ++i) {
      const int nf = TYPE == 0 ? (i < 3 ? 3 * wn + i : 12 + 3 * wn + (i - 3)) : 5 * wn + i;
      woff[i] = 8192 + (nf >> 2) * 4096 + (lo ^ ((nf & 3) << 5));
    }
  }

  f32x4 acc[4][NFR];
#pragma unroll
  for (int a = 0; a < 4; ++a)
#pragma unroll
    for (int b = 0; b < NFR; ++b) acc[a][b] = (f32x4){0.f, 0.f, 0.f, 0.f};

  auto readfr = [&](int kt, s16x8 (&xf)[4], s16x8 (&wf)[NFR]) {
    const char* sb = smem + (kt % K1_NS) * K1_STAGE;
#pragma unroll
    for (int b = 0; b < NFR; ++b) {
      s16x4 lo = __builtin_amdgcn_ds_read_tr16_b64_v4i16((lds_s16x4_ptr)(sb + woff[b]));
      s16x4 hi = __builtin_amdgcn_ds_read_tr16_b64_v4i16((lds_s16x4_ptr)(sb + woff[b] + 512));
      wf[b] = __builtin_shufflevector(lo, hi, 0, 1, 2, 3, 4, 5, 6, 7);
    }
#pragma unroll
    for (int a = 0; a < 4; ++a) xf[a] = *(const s16x8*)(sb + xoff + a * 1024);
  };
  auto mma = [&](const s16x8 (&xf)[4], const s16x8 (&wf)[NFR]) {
#pragma unroll
    for (int a = 0; a < 4; ++a)
#pragma unroll
      for (int b = 0; b < NFR; ++b) {
        if (TYPE == 2) acc[a][b] = __builtin_amdgcn_mfma_f32_16x16x32_bf16(xf[a], wf[b], acc[a][b], 0, 0, 0);
        else           acc[a][b] = __builtin_amdgcn_mfma_f32_16x16x32_bf16(wf[b], xf[a], acc[a][b], 0, 0, 0);
      }
  };
#define WAITN(N) asm volatile("s_waitcnt vmcnt(%0)" :: "n"(N) : "memory")
#define K1_WAIT(n) do { if (has3) WAITN(4 * (n)); else WAITN(3 * (n)); } while (0)

  s16x8 xA[4], wAf[NFR], xB[4], wBf[NFR];
#define SGB(m, n) __builtin_amdgcn_sched_group_barrier(m, n, 0)
  auto pattern_full = [&]() {
    SGB(0x10, 1); SGB(0x8, 1); SGB(0x10, 1); SGB(0x8, 1); SGB(0x10, 1); SGB(0x8, 1); SGB(0x10, 1); SGB(0x8, 1);
#pragma unroll
    for (int i = 0; i < 4 + 2 * NFR; ++i) { SGB(0x100, 1); SGB(0x8, 1); }
    SGB(0x8, 4);
  };
  auto pattern_tail = [&]() {
#pragma unroll
    for (int i = 0; i < 4 + 2 * NFR; ++i) { SGB(0x100, 1); SGB(0x8, 1); }
    SGB(0x8, 8);
  };
  STAMP(1, 0);
#pragma unroll
  for (int i = 0; i < K1_NS; ++i) issue(i);
  K1_WAIT(K1_NS - 1);
  BAR;
  STAMP(1, 1);
  readfr(0, xA, wAf);
  SCHED;
#pragma unroll
  for (int kt = 0; kt < 19; ++kt) {
    if (kt + K1_NS <= 20) K1_WAIT(K1_NS - 2); else K1_WAIT(18 - kt);
    WAIT_L0; BAR; SCHED;
    if (kt + K1_NS < 20) issue(kt + K1_NS);
    if (kt & 1) { readfr(kt + 1, xA, wAf); mma(xB, wBf); }
    else        { readfr(kt + 1, xB, wBf); mma(xA, wAf); }
    if (kt + K1_NS < 20) pattern_full(); else pattern_tail();
    SCHED;
  }
  mma(xB, wBf);

  STAMP(1, 2);
  if (TYPE == 0) {
    char* H = ws + OFF_H + ((size_t)(mt * 8 + wm * 4 + (g & 1)) * 54 + j * 6) * 1024 + ((g >> 1) * 16 + c) * 16;
#pragma unroll
    for (int a = 0; a < 4; a += 2) {
#pragma unroll
      for (int b = 0; b < 3; ++b) {
        u32x2 w[2];
#pragma unroll
        for (int h = 0; h < 2; ++h) {
          w[h][0] = cvt2(bfr2(sigm2(bfr2(lo2(acc[a + h][b])))) * bfr2(lo2(acc[a + h][b + 3])));
          w[h][1] = cvt2(bfr2(sigm2(bfr2(hi2(acc[a + h][b])))) * bfr2(hi2(acc[a + h][b + 3])));
        }
        const auto s0 = __builtin_amdgcn_permlane16_swap(w[0][0], w[1][0], false, false);
        const auto s1 = __builtin_amdgcn_permlane16_swap(w[0][1], w[1][1], false, false);
        const int f0 = wn * 48 + b * 16;
        *(u32x4*)(H + (size_t)a * 54 * 1024 + (f0 >> 5) * 1024 + ((f0 >> 4) & 1) * 512) = (u32x4){s0[0], s1[0], s0[1], s1[1]};
      }
    }
  } else if (TYPE == 1) {
    char* PQ = ws + OFF_PQ + ((size_t)(mt * 8 + wm * 4 + (g & 1)) * 20 + j * 10) * 1024 + ((g >> 1) * 16 + c) * 16;
#pragma unroll
    for (int a = 0; a < 4; a += 2) {
#pragma unroll
      for (int b = 0; b < 5; ++b) {
        u32x2 w[2];
#pragma unroll
        for (int h = 0; h < 2; ++h) { w[h][0] = phi2(lo2(acc[a + h][b])); w[h][1] = phi2(hi2(acc[a + h][b])); }
        const auto s0 = __builtin_amdgcn_permlane16_swap(w[0][0], w[1][0], false, false);
        const auto s1 = __builtin_amdgcn_permlane16_swap(w[0][1], w[1][1], false, false);
        const int f0 = wn * 80 + b * 16;
        *(u32x4*)(PQ + (size_t)a * 20 * 1024 + (f0 >> 5) * 1024 + ((f0 >> 4) & 1) * 512) = (u32x4){s0[0], s1[0], s0[1], s1[1]};
      }
    }
  } else {
    WAIT_L0; BAR;
#pragma unroll
    for (int a = 0; a < 4; ++a)
#pragma unroll
      for (int b = 0; b < 5; ++b) {
        const int col = wn * 80 + b * 16 + c;
        u32x2 w;
        if (wn < 2) { w[0] = phi2(lo2(acc[a][b])); w[1] = phi2(hi2(acc[a][b])); }
        else        { w[0] = cvt2(lo2(acc[a][b])); w[1] = cvt2(hi2(acc[a][b])); }
        *(u32x2*)(smem + col * 272 + (wm * 64 + a * 16 + 4 * g) * 2) = w;
      }
    __syncthreads();
    const int r32 = lane & 31, hh = lane >> 5;
    f32x16 akv, az;
#pragma unroll
    for (int i = 0; i < 16; ++i) { akv[i] = 0.f; az[i] = 0.f; }
    s16x8 ones;
#pragma unroll
    for (int i = 0; i < 8; ++i) ones[i] = (short)0x3F80;
#pragma unroll
    for (int s = 0; s < 8; ++s) {
      s16x8 af = *(const s16x8*)(smem + (20 * wave + r32) * 272 + (16 * s + 8 * hh) * 2);
      s16x8 bfv = *(const s16x8*)(smem + (160 + 20 * wave + r32) * 272 + (16 * s + 8 * hh) * 2);
      akv = __builtin_amdgcn_mfma_f32_32x32x16_bf16(af, bfv, akv, 0, 0, 0);
      az = __builtin_amdgcn_mfma_f32_32x32x16_bf16(af, ones, az, 0, 0, 0);
    }
    const int head = j * 8 + wave;
    float* P = (float*)(ws + OFF_KVP) + (size_t)(mt * 32 + head) * 504;
    if (r32 <= 20) {
#pragma unroll
      for (int q = 0; q < 3; ++q) {
        const int d0 = 8 * q + 4 * hh;
        if (d0 < 20) {
          f32x4 val;
#pragma unroll
          for (int r = 0; r < 4; ++r) val[r] = (r32 == 20) ? az[4 * q + r] : akv[4 * q + r];
          *(f32x4*)(P + r32 * 24 + d0) = val;
        }
      }
    }
  }
}

__global__ __launch_bounds__(512, 2) void k1_gemm(const u16* __restrict__ wqkv, const u16* __restrict__ wg,
                                                   const u16* __restrict__ wu, const u16* __restrict__ wo,
                                                   const u16* __restrict__ wdown, char* __restrict__ ws) {
  extern __shared__ __attribute__((aligned(16))) char smem[];
  const int bid = blockIdx.x;
  if (bid >= 240) {
    retile_all((bid - 240) * 512 + threadIdx.x, wo, wdown, ws);
    return;
  }
  const int p = (bid & 7) * 30 + (bid >> 3);
  const int mh = p / 120, rem = p % 120, nt = rem >> 3, mt = mh * 8 + (rem & 7);
  if (nt < 4) k1_body<2>(wqkv, wqkv, ws, mt, nt, smem);
  else if (nt < 6) k1_body<1>(wqkv, wqkv, ws, mt, nt - 4, smem);
  else k1_body<0>(wg, wu, ws, mt, nt - 6, smem);
  STAMP(1, 3);
}

constexpr int K2_LDS = 163840;
constexpr int K2_D = K2DD;
#define K2DW 3
constexpr int K2_DW = K2DW;

__global__ __launch_bounds__(512, 2) void k2_gemm(const u16* __restrict__ x, const char* __restrict__ ws, u16* __restrict__ out) {
  extern __shared__ __attribute__((aligned(16))) char smem[];
  const int tid = threadIdx.x, lane = tid & 63;
  const int wave = __builtin_amdgcn_readfirstlane(tid >> 6);
  const int wq = wave & 3;
  const int g = lane >> 4, c = lane & 15;
  const int bid = blockIdx.x;
  const int mt = (bid & 7) * 4 + (bid >> 6), nt = (bid >> 3) & 7;

  STAMP(2, 0);
  f32x4 acc[4][5];
#pragma unroll
  for (int a = 0; a < 4; ++a)
#pragma unroll
    for (int b = 0; b < 5; ++b) acc[a][b] = (f32x4){0.f, 0.f, 0.f, 0.f};
  const int b0 = wave < 4 ? 0 : 3, nb = wave < 4 ? 3 : 2;
  const int tok = mt * 64 + wq * 16 + c;
  u32x2 xw[3];
#pragma unroll
  for (int i = 0; i < 3; ++i) xw[i] = *(const u32x2*)(x + (size_t)tok * D_ + nt * 80 + (i < nb ? b0 + i : b0) * 16 + 4 * g);

  if (wave < 4) {
    const char* hsrc = ws + OFF_H + ((size_t)(mt * 4) * 54 + wq) * 1024 + lane * 16;
    const char* wsrc = ws + OFF_WC + ((size_t)nt * 54 + wq) * 5120 + lane * 16;
    s16x8 xr[K2_D][4], wr[K2_D][5];
    auto ld = [&](int slot, int i) {
#pragma unroll
      for (int b = 0; b < 5; ++b) wr[slot][b] = *(const s16x8*)(wsrc + (size_t)(20 * i + b) * 1024);
#pragma unroll
      for (int a = 0; a < 4; ++a) xr[slot][a] = *(const s16x8*)(hsrc + (size_t)(a * 54 + 4 * i) * 1024);
    };
    auto mma1 = [&](int slot) {
#pragma unroll
      for (int a = 0; a < 4; ++a)
#pragma unroll
        for (int b = 0; b < 5; ++b) acc[a][b] = __builtin_amdgcn_mfma_f32_16x16x32_bf16(wr[slot][b], xr[slot][a], acc[a][b], 0, 0, 0);
    };
#pragma unroll
    for (int i = 0; i < K2_D; ++i) ld(i, i);
    SCHED;
#pragma unroll
    for (int i = 0; i < 13; ++i) {
      mma1(i % K2_D);
      if (i + K2_D < 13) ld(i % K2_D, i + K2_D);
      else if (i + K2_D == 13) { if (wave < 2) ld(i % K2_D, 13); }
      SCHED;
    }
    if (wave < 2) mma1(13 % K2_D);
    STAMP(2, 1);
  } else {
    __builtin_amdgcn_s_setprio(3);
    char* reg = smem + wq * 36864;
    if (bid < 32) {
      const int t = tid - 256, u = t >> 2, q = t & 3, uu = u < 63 ? u : 62, e = uu / 3, g3 = uu - e * 3;
      const float* src = (const float*)(ws + OFF_KVP) + ((size_t)(4 * q) * 32 + bid) * 504 + e * 24 + 8 * g3;
      f32x4 lo[4], hi[4];
#pragma unroll
      for (int i = 0; i < 4; ++i) { lo[i] = *(const f32x4*)(src + i * 32 * 504); hi[i] = *(const f32x4*)(src + i * 32 * 504 + (g3 < 2 ? 4 : 0)); }
      f32x4 s0 = (lo[0] + lo[1]) + (lo[2] + lo[3]), s1 = (hi[0] + hi[1]) + (hi[2] + hi[3]);
#pragma unroll
      for (int o = 1; o <= 2; o <<= 1)
#pragma unroll
        for (int r = 0; r < 4; ++r) { s0[r] += __shfl_xor(s0[r], o); s1[r] += __shfl_xor(s1[r], o); }
      u32x4 w;
      w[0] = pack2(s0[0], s0[1]); w[1] = pack2(s0[2], s0[3]);
      w[2] = g3 < 2 ? pack2(s1[0], s1[1]) : 0u; w[3] = g3 < 2 ? pack2(s1[2], s1[3]) : 0u;
#pragma unroll
      for (int r = 0; r < 4; ++r) w[r] = w[r] == KV_EMPTY ? 0x7FC07FC0u : w[r];
      const int slot = e < 16 ? g3 * 16 + e : 64 + g3 * 16 + (e < 20 ? (e - 16) * 4 : 4 * q + 1);
      if ((q == 0 || e == 20) && u < 63)
        asm volatile("global_store_dwordx4 %0, %1, off sc1" :: "v"(ws + OFF_KVF + ((size_t)bid * 128 + slot) * 16), "v"(w) : "memory");
    }
    {
      const char* pqg = ws + OFF_PQ + ((size_t)(mt * 4) * 32 + wq * 8) * 640 + lane * 16;
#pragma unroll
      for (int tg = 0; tg < 4; ++tg)
#pragma unroll
        for (int i = 0; i < 5; ++i)
          __builtin_amdgcn_global_load_lds((const unsigned*)(pqg + tg * 20480 + i * 1024), (unsigned*)(reg + tg * 5120 + i * 1024), 16, 0, 0);
    }
    const char* wosrc = ws + OFF_WO + (size_t)(nt * 4 + wq) * 25600 + lane * 16;
    s16x8 w2[K2_DW][5];
    auto ldW = [&](int s) {
#pragma unroll
      for (int b = 0; b < 5; ++b) w2[s % K2_DW][b] = *(const s16x8*)(wosrc + (s * 5 + b) * 1024);
    };
#pragma unroll
    for (int s = 0; s < K2_DW; ++s) ldW(s);
    SCHED;
    const char* kvl = reg + 20480 + lane * 16;
    {
      const int hp = lane & 3;
      const char* hint = ws + OFF_KVF + ((size_t)(wq * 8 + ((lane >> 2) & 7)) * 128 + (hp == 3 ? 64 : hp * 21)) * 16;
      unsigned hv;
      while (true) {
        asm volatile("global_load_dword %0, %1, off sc1\n\ts_waitcnt vmcnt(0)" : "=v"(hv) : "v"(hint) : "memory");
        if (__builtin_amdgcn_ballot_w64(hv == KV_EMPTY) == 0ull) break;
        __builtin_amdgcn_s_sleep(4);
      }
      const char* kg = ws + OFF_KVF + (size_t)(wq * 8) * 2048 + lane * 16;
      bool bad;
      do {
#pragma unroll
        for (int i = 0; i < 16; ++i) __builtin_amdgcn_global_load_lds((const unsigned*)(kg + i * 1024), (unsigned*)(reg + 20480 + i * 1024), 16, 0, KVAUX);
        asm volatile("s_waitcnt vmcnt(0)" ::: "memory");
        bad = false;
#pragma unroll
        for (int i = 0; i < 16; ++i) {
          const u32x4 v = *(const __attribute__((address_space(3))) u32x4*)(kvl + i * 1024);
          bad = bad || v[0] == KV_EMPTY || v[1] == KV_EMPTY || v[2] == KV_EMPTY || v[3] == KV_EMPTY;
        }
      } while (__builtin_amdgcn_ballot_w64(bad) != 0ull);
    }
    __builtin_amdgcn_s_waitcnt(0x0F70);
    SCHED;
    STAMPW(4);
    const char* pql = reg + (g < 3 ? g : 0) * 256 + c * 16;
    const f32x4 z4 = (f32x4){0.f, 0.f, 0.f, 0.f};
    unsigned ow[4][20];
    auto mma2 = [&](int s) {
#pragma unroll
      for (int a = 0; a < 4; ++a) {
        const u32x4 o4 = (u32x4){ow[a][4 * s], ow[a][4 * s + 1], ow[a][4 * s + 2], ow[a][4 * s + 3]};
        const s16x8 of = __builtin_bit_cast(s16x8, o4);
#pragma unroll
        for (int b = 0; b < 5; ++b) acc[a][b] = __builtin_amdgcn_mfma_f32_16x16x32_bf16(w2[s % K2_DW][b], of, acc[a][b], 0, 0, 0);
      }
      if (s + K2_DW < 5) ldW(s + K2_DW);
    };
#pragma unroll
    for (int m = 0; m < 4; ++m) {
      f32x4 ov[2][4]; float oe[2][4];
#pragma unroll
      for (int h2 = 0; h2 < 2; ++h2) {
        const int hh = 2 * m + h2;
        const s16x8 k0 = *(const s16x8*)(kvl + hh * 2048), k1 = *(const s16x8*)(kvl + hh * 2048 + 1024);
#pragma unroll
        for (int tg = 0; tg < 4; ++tg) {
          u32x4 pq;
          if ((hh & 1) == 0) pq = *(const u32x4*)(pql + tg * 5120 + (5 * hh / 2) * 256);
          else {
            const u32x2 p0 = *(const u32x2*)(pql + tg * 5120 + ((5 * hh - 1) / 2) * 256 + 8), p1 = *(const u32x2*)(pql + tg * 5120 + ((5 * hh - 1) / 2) * 256 + 256);
            pq = (u32x4){p0[0], p0[1], p1[0], p1[1]};
          }
          u32x4 pk4;
          pk4[0] = g < 3 ? pq[0] : 0u; pk4[1] = g < 3 ? pq[1] : 0u;
          pk4[2] = g < 2 ? pq[2] : 0u; pk4[3] = g < 2 ? pq[3] : 0u;
          const s16x8 pf = __builtin_bit_cast(s16x8, pk4);
          const f32x4 d1 = __builtin_amdgcn_mfma_f32_16x16x32_bf16(k1, pf, z4, 0, 0, 0);
          const f32x4 d0 = __builtin_amdgcn_mfma_f32_16x16x32_bf16(k0, pf, z4, 0, 0, 0);
          const float rden = frcp(bfr(bfr(d1[1]) + 1e-6f));
          const f32x2 lo = bfr2(lo2(d0)) * rden, hi = bfr2(hi2(d0)) * rden;
          ov[h2][tg] = (f32x4){lo[0], lo[1], hi[0], hi[1]};
          oe[h2][tg] = bfr(d1[0]) * rden;
        }
      }
#pragma unroll
      for (int tg = 0; tg < 4; ++tg) {
        ow[tg][5 * m + 0] = cvt2(lo2(ov[0][tg]));
        ow[tg][5 * m + 1] = cvt2(hi2(ov[0][tg]));
        ow[tg][5 * m + 2] = cvt2((f32x2){oe[0][tg], ov[1][tg][0]});
        ow[tg][5 * m + 3] = cvt2((f32x2){ov[1][tg][1], ov[1][tg][2]});
        ow[tg][5 * m + 4] = cvt2((f32x2){ov[1][tg][3], oe[1][tg]});
      }
      mma2(m);
      if (m == 3) mma2(4);
      SCHED;
      if (m < 2) STAMPW(5 + m);
    }
  }
  __syncthreads();

  f32x4* red = (f32x4*)smem;
#pragma unroll
  for (int a = 0; a < 4; ++a)
#pragma unroll
    for (int b = 0; b < 5; ++b) red[(wave * 20 + a * 5 + b) * 64 + lane] = acc[a][b];
  __syncthreads();
#pragma unroll
  for (int i = 0; i < 3; ++i) {
    if (i < nb) {
      const int f = wq * 5 + b0 + i;
      f32x4 y1 = red[(0 * 20 + f) * 64 + lane], y2 = red[(4 * 20 + f) * 64 + lane];
#pragma unroll
      for (int sw = 1; sw < 4; ++sw) { y1 += red[(sw * 20 + f) * 64 + lane]; y2 += red[((4 + sw) * 20 + f) * 64 + lane]; }
      const int n = nt * 80 + (b0 + i) * 16 + 4 * g;
      u32x2 w;
      {
        const f32x2 y = bfr2(lo2(y1)), gs = bfr2(y * sigm2(y)), sv = bfr2(lo2(y2));
        w[0] = cvt2(unp2(xw[i][0]) + bfr2(sv * gs));
      }
      {
        const f32x2 y = bfr2(hi2(y1)), gs = bfr2(y * sigm2(y)), sv = bfr2(hi2(y2));
        w[1] = cvt2(unp2(xw[i][1]) + bfr2(sv * gs));
      }
      *(u32x2*)(out + (size_t)tok * D_ + n) = w;
    }
  }
  STAMP(2, 3);
}

extern "C" void kernel_launch(void* const* d_in, const int* in_sizes, int n_in,
                              void* d_out, int out_size, void* d_ws, size_t ws_size,
                              hipStream_t stream) {
  const u16* x = (const u16*)d_in[0];
  const u16* wqkv = (const u16*)d_in[1];
  const u16* wo = (const u16*)d_in[2];
  const u16* wg = (const u16*)d_in[4];
  const u16* wu = (const u16*)d_in[5];
  const u16* wd = (const u16*)d_in[6];
  char* ws = (char*)d_ws;
  u16* out = (u16*)d_out;

  hipFuncSetAttribute((const void*)k1_gemm, hipFuncAttributeMaxDynamicSharedMemorySize, K1_NS * K1_STAGE);
  hipFuncSetAttribute((const void*)k2_gemm, hipFuncAttributeMaxDynamicSharedMemorySize, K2_LDS);

  k0_prep<<<512, 256, 0, stream>>>(x, ws, wqkv, wg, wu);
  k1_gemm<<<256, 512, K1_NS * K1_STAGE, stream>>>(wqkv, wg, wu, wo, wd, ws);
  k2_gemm<<<256, 512, K2_LDS, stream>>>(x, ws, out);
}
```

```cpp
#include <hip/hip_runtime.h>
#include <hip/hip_bf16.h>
#include <stdint.h>

typedef unsigned short u16;
typedef __attribute__((ext_vector_type(8))) short s16x8;
typedef __attribute__((ext_vector_type(4))) short s16x4;
typedef __attribute__((ext_vector_type(4))) float f32x4;
typedef __attribute__((ext_vector_type(16))) float f32x16;
typedef __attribute__((ext_vector_type(2))) unsigned u32x2;
typedef __attribute__((ext_vector_type(4))) unsigned u32x4;
typedef u32x4 u32x4_a8 __attribute__((aligned(8)));
typedef __attribute__((address_space(3))) s16x4* lds_s16x4_ptr;
typedef __attribute__((address_space(1))) unsigned gu32;
typedef __attribute__((address_space(1))) unsigned long long gu64;

#define RLX_AGENT __ATOMIC_RELAXED, __HIP_MEMORY_SCOPE_AGENT

constexpr int S_ = 2048, D_ = 640, F_ = 1728, NQKV_ = 1920;

constexpr size_t OFF_CNT   = 0;
constexpr size_t OFF_XN    = 8192;
constexpr size_t SZ_XN     = (size_t)S_ * D_ * 2;
constexpr size_t OFF_H     = OFF_XN + SZ_XN;
constexpr size_t SZ_H      = (size_t)S_ * F_ * 2;
constexpr size_t OFF_PQ    = OFF_H + SZ_H;
constexpr size_t SZ_PQ     = (size_t)S_ * D_ * 2;
constexpr size_t OFF_WC    = OFF_PQ + SZ_PQ;
constexpr size_t SZ_WC     = (size_t)8 * 54 * 5 * 1024;
constexpr size_t OFF_WO    = OFF_WC + SZ_WC;
constexpr size_t SZ_WO     = (size_t)8 * 4 * 5 * 5 * 1024;
constexpr size_t OFF_KVP   = OFF_WO + SZ_WO;
constexpr size_t SZ_KVP    = (size_t)16 * 32 * 504 * 4;
constexpr size_t OFF_KVF   = OFF_KVP + SZ_KVP;
constexpr size_t SZ_KVF    = 65536;

#define KVAUX 16
constexpr unsigned KV_EMPTY = 0xFFFFFFFFu;
__device__ __forceinline__ float bf2f(u16 v) { return __uint_as_float(((unsigned)v) << 16); }
__device__ __forceinline__ u16 f2bf(float f) { return __builtin_bit_cast(u16, (__bf16)f); }
__device__ __forceinline__ float bfr(float f) { return bf2f(f2bf(f)); }
__device__ __forceinline__ unsigned pack2(float a, float b) { return (unsigned)f2bf(a) | ((unsigned)f2bf(b) << 16); }
__device__ __forceinline__ float phi_f(float t) {
  float p = t > 0.f ? bfr(t + 1.0f) : bfr(__expf(t));
  return bfr(p + 1e-6f);
}
typedef __attribute__((ext_vector_type(2))) float f32x2;
typedef __attribute__((ext_vector_type(2))) __bf16 bf16x2;
__device__ __forceinline__ unsigned cvt2(f32x2 v) { return __builtin_bit_cast(unsigned, __builtin_convertvector(v, bf16x2)); }
__device__ __forceinline__ f32x2 unp2(unsigned u) { return (f32x2){__uint_as_float(u << 16), __uint_as_float(u & 0xffff0000u)}; }
__device__ __forceinline__ f32x2 bfr2(f32x2 v) { return unp2(cvt2(v)); }
__device__ __forceinline__ f32x2 lo2(f32x4 v) { return __builtin_shufflevector(v, v, 0, 1); }
__device__ __forceinline__ f32x2 hi2(f32x4 v) { return __builtin_shufflevector(v, v, 2, 3); }
__device__ __forceinline__ unsigned phi2(f32x2 a) {
  const f32x2 t = bfr2(a), m = t * 1.44269504088896340736f, p = t + 1.0f;
  f32x2 sel;
  sel[0] = __builtin_amdgcn_fmed3f(__builtin_amdgcn_exp2f(m[0]), p[0], 1.0f);
  sel[1] = __builtin_amdgcn_fmed3f(__builtin_amdgcn_exp2f(m[1]), p[1], 1.0f);
  return cvt2(bfr2(sel) + 1e-6f);
}
__device__ __forceinline__ f32x2 sigm2(f32x2 g) {
  const f32x2 m = g * -1.44269504088896340736f;
  f32x2 e; e[0] = __builtin_amdgcn_exp2f(m[0]); e[1] = __builtin_amdgcn_exp2f(m[1]);
  e = e + 1.0f;
  f32x2 r; r[0] = __builtin_amdgcn_rcpf(e[0]); r[1] = __builtin_amdgcn_rcpf(e[1]);
  return r;
}
__device__ __forceinline__ int aswz(int row) { int x = (row >> 2) & 3; return (((x ^ (x >> 1)) & 1) << 1) | (x >> 1); }

#define WAIT_V(n) asm volatile("s_waitcnt vmcnt(" #n ")" ::: "memory")
#define STAMP(kid, idx)
#define STAMPW(idx)
#define WAIT_L0 asm volatile("s_waitcnt lgkmcnt(0)" ::: "memory")
#define BAR __builtin_amdgcn_s_barrier()
#define SCHED __builtin_amdgcn_sched_barrier(0)
__device__ __forceinline__ float frcp(float v) { return __builtin_amdgcn_rcpf(v); }

constexpr int RT_WD = 216 * 80, RT_ITEMS = RT_WD + 80 * 80, RT_R = 3;
__device__ __forceinline__ void retile_all(int t0, const u16* __restrict__ wo, const u16* __restrict__ wdown, char* __restrict__ ws) {
  u32x4 r[RT_R][8];
  char* dst[RT_R];
#pragma unroll
  for (int it = 0; it < RT_R; ++it) {
    int t = t0 + it * 8192; if (t >= RT_ITEMS) t = t0;
    const bool isd = t < RT_WD;
    const int t2 = isd ? t : t - RT_WD;
    const int ko = t2 / 80, noct = t2 - ko * 80;
    const int n0 = noct * 8, nt = n0 / 80, nn = n0 - nt * 80, nf = nn >> 4, c0 = nn & 15, gg = ko & 3, kq = ko >> 2;
    const int wq = kq / 5, ks = kq - wq * 5;
    const u16* src = (isd ? wdown : wo) + n0;
#pragma unroll
    for (int jj = 0; jj < 8; ++jj) {
      const int q = 8 * ks + jj, hh = q / 5, rr = q - hh * 5;
      const int row = isd ? ko * 8 + jj : (wq * 8 + hh) * 20 + (rr < 4 ? 4 * gg + rr : 16 + gg);
      r[it][jj] = *(const u32x4*)(src + (size_t)row * D_);
    }
    dst[it] = ws + (isd ? OFF_WC + (size_t)(nt * 54 + kq) * 5120 : OFF_WO + (size_t)(nt * 20 + kq) * 5120) + (size_t)(nf * 64 + gg * 16 + c0) * 16;
  }
#pragma unroll
  for (int it = 0; it < RT_R; ++it) {
#pragma unroll
    for (int i = 0; i < 4; ++i) {
      u32x4 lo, hi;
#pragma unroll
      for (int q = 0; q < 4; ++q) {
        lo[q] = (r[it][2 * q][i] & 0xffffu) | (r[it][2 * q + 1][i] << 16);
        hi[q] = (r[it][2 * q][i] >> 16) | (r[it][2 * q + 1][i] & 0xffff0000u);
      }
      *(u32x4*)(dst[it] + (2 * i) * 16) = lo;
      *(u32x4*)(dst[it] + (2 * i + 1) * 16) = hi;
    }
  }
}

__global__ __launch_bounds__(256) void k0_prep(const u16* __restrict__ x, char* __restrict__ ws, const u16* __restrict__ wqkv,
                                               const u16* __restrict__ wg, const u16* __restrict__ wu) {
  const int tid = threadIdx.x;
  if (blockIdx.x >= 256) {
    const int t = (blockIdx.x - 256) * 256 + tid;
    unsigned v0, v1;
    const int l1 = t + 65536 < 107520 ? t + 65536 : t;
    const char* p0 = t < 38400 ? (const char*)wqkv + (size_t)t * 64 : (const char*)wg + (size_t)(t - 38400) * 64;
    const char* p1 = l1 < 38400 ? (const char*)wqkv + (size_t)l1 * 64 : (l1 < 72960 ? (const char*)wg + (size_t)(l1 - 38400) * 64 : (const char*)wu + (size_t)(l1 - 72960) * 64);
    asm volatile("global_load_dword %0, %2, off\n\tglobal_load_dword %1, %3, off\n\ts_waitcnt vmcnt(0)" : "=&v"(v0), "=&v"(v1) : "v"(p0), "v"(p1) : "memory");
    return;
  }
  {
    if (blockIdx.x < 16) {
      const int ci = blockIdx.x * 256 + tid, fr = (ci >> 6) & 1, gg = (ci >> 4) & 3, cc = ci & 15;
      const unsigned v = (gg < 3 && (fr == 0 || (cc & 3) < 2)) ? KV_EMPTY : 0u;
      *(u32x4*)(ws + OFF_KVF + (size_t)ci * 16) = (u32x4){v, v, v, v};
    }
    const int row = blockIdx.x * 8 + (tid >> 5);
    const int l32 = tid & 31;
    const u16* xr = x + (size_t)row * D_;
    float v[20];
    float sum = 0.f;
#pragma unroll
    for (int i = 0; i < 5; ++i) {
      u32x2 w = *(const u32x2*)(xr + (l32 + 32 * i) * 4);
      v[4 * i + 0] = __uint_as_float(w[0] << 16);
      v[4 * i + 1] = __uint_as_float(w[0] & 0xffff0000u);
      v[4 * i + 2] = __uint_as_float(w[1] << 16);
      v[4 * i + 3] = __uint_as_float(w[1] & 0xffff0000u);
      sum += v[4 * i] + v[4 * i + 1] + v[4 * i + 2] + v[4 * i + 3];
    }
#pragma unroll
    for (int o = 16; o >= 1; o >>= 1) sum += __shfl_xor(sum, o);
    const float mean = sum * (1.0f / D_);
    float sq = 0.f;
#pragma unroll
    for (int i = 0; i < 20; ++i) { float d = v[i] - mean; sq += d * d; }
#pragma unroll
    for (int o = 16; o >= 1; o >>= 1) sq += __shfl_xor(sq, o);
    const float mb = bfr(mean);
    const float vb = bfr(sq * (1.0f / D_));
    const float sd = bfr(sqrtf(bfr(vb + 1e-5f)));
    const float rsd = 1.0f / sd;
    u16* xn = (u16*)(ws + OFF_XN);
    const int mt = row >> 7, r = row & 127, sw = aswz(r);
#pragma unroll
    for (int i = 0; i < 5; ++i) {
      const int k = (l32 + 32 * i) * 4;
      float o0 = bfr(v[4 * i + 0] - mb) * rsd, o1 = bfr(v[4 * i + 1] - mb) * rsd;
      float o2 = bfr(v[4 * i + 2] - mb) * rsd, o3 = bfr(v[4 * i + 3] - mb) * rsd;
      u32x2 w; w[0] = pack2(o0, o1); w[1] = pack2(o2, o3);
      const int kt = k >> 5, ch = (k >> 3) & 3, hf = (k >> 2) & 1;
      size_t off = ((size_t)(mt * 20 + kt) * 128 + r) * 32 + ((ch ^ sw) << 3) + (hf << 2);
      *(u32x2*)(xn + off) = w;
    }
  }
}

constexpr int K1_STAGE = 32768;
#define K1NS 4
constexpr int K1_NS = K1NS;

template <int TYPE>
__device__ __forceinline__ void k1_body(const u16* __restrict__ wA, const u16* __restrict__ wB, char* __restrict__ ws,
                                        int mt, int j, char* smem) {
  constexpr int NCB = TYPE == 0 ? 6 : 5;
  constexpr int NFR = TYPE == 0 ? 6 : 5;
  constexpr int LDW = TYPE == 0 ? F_ : NQKV_;
  const int tid = threadIdx.x, lane = tid & 63;
  const int wave = __builtin_amdgcn_readfirstlane(tid >> 6);
  const int wm = wave >> 2, wn = (TYPE == 2 && wave >= 4) ? ((wave + 2) & 3) : (wave & 3);
  const int g = lane >> 4, c = lane & 15;

  const char* asrc = ws + OFF_XN + (size_t)mt * 20 * 8192 + wave * 1024 + lane * 16;
  const u16* bsrc[3];
#pragma unroll
  for (int t = 0; t < 3; ++t) {
    int ii = wave + 8 * t; if (ii >= NCB * 4) ii = wave;
    const int cb = ii >> 2, krow = (ii & 3) * 8 + (lane >> 3), u = lane & 7;
    const int sk = ((krow >> 1) & 1) | (((krow >> 3) & 1) << 1);
    const int ch = (u >> 1) ^ sk;
    const int lc = cb * 64 + ch * 16 + (u & 1) * 8;
    const u16* p;
    if (TYPE == 0) p = (lc < 192) ? (wA + j * 192 + lc) : (wB + j * 192 + (lc - 192));
    else if (TYPE == 1) p = wA + j * 320 + lc;
    else p = (lc < 160) ? (wA + 640 + j * 160 + lc) : (wA + 1280 + j * 160 + (lc - 160));
    bsrc[t] = p + (size_t)krow * LDW;
  }
  const bool has3 = (NCB == 6) || (wave < 4);

  auto issue = [&](int kt) {
    char* sb = smem + (kt % K1_NS) * K1_STAGE;
    __builtin_amdgcn_global_load_lds((const unsigned*)(asrc + (size_t)kt * 8192), (unsigned*)(sb + wave * 1024), 16, 0, 0);
    __builtin_amdgcn_global_load_lds((const unsigned*)(bsrc[0] + (size_t)kt * 32 * LDW), (unsigned*)(sb + 8192 + wave * 1024), 16, 0, 0);
    __builtin_amdgcn_global_load_lds((const unsigned*)(bsrc[1] + (size_t)kt * 32 * LDW), (unsigned*)(sb + 8192 + (wave + 8) * 1024), 16, 0, 0);
    if (has3) __builtin_amdgcn_global_load_lds((const unsigned*)(bsrc[2] + (size_t)kt * 32 * LDW), (unsigned*)(sb + 8192 + (wave + 16) * 1024), 16, 0, 0);
  };

  const int xoff = (wm * 64 + c) * 64 + ((g ^ aswz(c)) << 4);
  int woff[NFR];
  {
    const int q = (lane & 15) >> 2, p = lane & 3;
    const int s = (q >> 1) | ((g & 1) << 1);
    const int lo = (8 * g + q) * 128 + (s << 5) + p * 8;
#pragma unroll
    for (int i = 0; i < NFR; ++i) {
      const int nf = TYPE == 0 ? (i < 3 ? 3 * wn + i : 12 + 3 * wn + (i - 3)) : 5 * wn + i;
      woff[i] = 8192 + (nf >> 2) * 4096 + (lo ^ ((nf & 3) << 5));
    }
  }

  f32x4 acc[4][NFR];
#pragma unroll
  for (int a = 0; a < 4; ++a)
#pragma unroll
    for (int b = 0; b < NFR; ++b) acc[a][b] = (f32x4){0.f, 0.f, 0.f, 0.f};

  auto readfr = [&](int kt, s16x8 (&xf)[4], s16x8 (&wf)[NFR]) {
    const char* sb = smem + (kt % K1_NS) * K1_STAGE;
#pragma unroll
    for (int b = 0; b < NFR; ++b) {
      s16x4 lo = __builtin_amdgcn_ds_read_tr16_b64_v4i16((lds_s16x4_ptr)(sb + woff[b]));
      s16x4 hi = __builtin_amdgcn_ds_read_tr16_b64_v4i16((lds_s16x4_ptr)(sb + woff[b] + 512));
      wf[b] = __builtin_shufflevector(lo, hi, 0, 1, 2, 3, 4, 5, 6, 7);
    }
#pragma unroll
    for (int a = 0; a < 4; ++a) xf[a] = *(const s16x8*)(sb + xoff + a * 1024);
  };
  auto mma = [&](const s16x8 (&xf)[4], const s16x8 (&wf)[NFR]) {
#pragma unroll
    for (int a = 0; a < 4; ++a)
#pragma unroll
      for (int b = 0; b < NFR; ++b) {
        if (TYPE == 2) acc[a][b] = __builtin_amdgcn_mfma_f32_16x16x32_bf16(xf[a], wf[b], acc[a][b], 0, 0, 0);
        else           acc[a][b] = __builtin_amdgcn_mfma_f32_16x16x32_bf16(wf[b], xf[a], acc[a][b], 0, 0, 0);
      }
  };
#define WAITN(N) asm volatile("s_waitcnt vmcnt(%0)" :: "n"(N) : "memory")
#define K1_WAIT(n) do { if (has3) WAITN(4 * (n)); else WAITN(3 * (n)); } while (0)

  s16x8 xA[4], wAf[NFR], xB[4], wBf[NFR];
#define SGB(m, n) __builtin_amdgcn_sched_group_barrier(m, n, 0)
  auto pattern_full = [&]() {
    SGB(0x10, 1); SGB(0x8, 1); SGB(0x10, 1); SGB(0x8, 1); SGB(0x10, 1); SGB(0x8, 1); SGB(0x10, 1); SGB(0x8, 1);
#pragma unroll
    for (int i = 0; i < 4 + 2 * NFR; ++i) { SGB(0x100, 1); SGB(0x8, 1); }
    SGB(0x8, 4);
  };
  auto pattern_tail = [&]() {
#pragma unroll
    for (int i = 0; i < 4 + 2 * NFR; ++i) { SGB(0x100, 1); SGB(0x8, 1); }
    SGB(0x8, 8);
  };
  STAMP(1, 0);
#pragma unroll
  for (int i = 0; i < K1_NS; ++i) issue(i);
  K1_WAIT(K1_NS - 1);
  BAR;
  STAMP(1, 1);
  readfr(0, xA, wAf);
  SCHED;
#pragma unroll
  for (int kt = 0; kt < 19; ++kt) {
    if (kt + K1_NS <= 20) K1_WAIT(K1_NS - 2); else K1_WAIT(18 - kt);
    WAIT_L0; BAR; SCHED;
    if (kt + K1_NS < 20) issue(kt + K1_NS);
    if (kt & 1) { readfr(kt + 1, xA, wAf); mma(xB, wBf); }
    else        { readfr(kt + 1, xB, wBf); mma(xA, wAf); }
    if (kt + K1_NS < 20) pattern_full(); else pattern_tail();
    SCHED;
  }
  mma(xB, wBf);

  STAMP(1, 2);
  if (TYPE == 0) {
    char* H = ws + OFF_H + ((size_t)(mt * 8 + wm * 4 + (g & 1)) * 54 + j * 6) * 1024 + ((g >> 1) * 16 + c) * 16;
#pragma unroll
    for (int a = 0; a < 4; a += 2) {
#pragma unroll
      for (int b = 0; b < 3; ++b) {
        u32x2 w[2];
#pragma unroll
        for (int h = 0; h < 2; ++h) {
          w[h][0] = cvt2(bfr2(sigm2(bfr2(lo2(acc[a + h][b])))) * bfr2(lo2(acc[a + h][b + 3])));
          w[h][1] = cvt2(bfr2(sigm2(bfr2(hi2(acc[a + h][b])))) * bfr2(hi2(acc[a + h][b + 3])));
        }
        const auto s0 = __builtin_amdgcn_permlane16_swap(w[0][0], w[1][0], false, false);
        const auto s1 = __builtin_amdgcn_permlane16_swap(w[0][1], w[1][1], false, false);
        const int f0 = wn * 48 + b * 16;
        *(u32x4*)(H + (size_t)a * 54 * 1024 + (f0 >> 5) * 1024 + ((f0 >> 4) & 1) * 512) = (u32x4){s0[0], s1[0], s0[1], s1[1]};
      }
    }
  } else if (TYPE == 1) {
    char* PQ = ws + OFF_PQ + ((size_t)(mt * 8 + wm * 4 + (g & 1)) * 20 + j * 10) * 1024 + ((g >> 1) * 16 + c) * 16;
#pragma unroll
    for (int a = 0; a < 4; a += 2) {
#pragma unroll
      for (int b = 0; b < 5; ++b) {
        u32x2 w[2];
#pragma unroll
        for (int h = 0; h < 2; ++h) { w[h][0] = phi2(lo2(acc[a + h][b])); w[h][1] = phi2(hi2(acc[a + h][b])); }
        const auto s0 = __builtin_amdgcn_permlane16_swap(w[0][0], w[1][0], false, false);
        const auto s1 = __builtin_amdgcn_permlane16_swap(w[0][1], w[1][1], false, false);
        const int f0 = wn * 80 + b * 16;
        *(u32x4*)(PQ + (size_t)a * 20 * 1024 + (f0 >> 5) * 1024 + ((f0 >> 4) & 1) * 512) = (u32x4){s0[0], s1[0], s0[1], s1[1]};
      }
    }
  } else {
    WAIT_L0; BAR;
#pragma unroll
    for (int a = 0; a < 4; ++a)
#pragma unroll
      for (int b = 0; b < 5; ++b) {
        const int col = wn * 80 + b * 16 + c;
        u32x2 w;
        if (wn < 2) { w[0] = phi2(lo2(acc[a][b])); w[1] = phi2(hi2(acc[a][b])); }
        else        { w[0] = cvt2(lo2(acc[a][b])); w[1] = cvt2(hi2(acc[a][b])); }
        *(u32x2*)(smem + col * 272 + (wm * 64 + a * 16 + 4 * g) * 2) = w;
      }
    __syncthreads();
    const int r32 = lane & 31, hh = lane >> 5;
    f32x16 akv, az;
#pragma unroll
    for (int i = 0; i < 16; ++i) { akv[i] = 0.f; az[i] = 0.f; }
    s16x8 ones;
#pragma unroll
    for (int i = 0; i < 8; ++i) ones[i] = (short)0x3F80;
#pragma unroll
    for (int s = 0; s < 8; ++s) {
      s16x8 af = *(const s16x8*)(smem + (20 * wave + r32) * 272 + (16 * s + 8 * hh) * 2);
      s16x8 bfv = *(const s16x8*)(smem + (160 + 20 * wave + r32) * 272 + (16 * s + 8 * hh) * 2);
      akv = __builtin_amdgcn_mfma_f32_32x32x16_bf16(af, bfv, akv, 0, 0, 0);
      az = __builtin_amdgcn_mfma_f32_32x32x16_bf16(af, ones, az, 0, 0, 0);
    }
    const int head = j * 8 + wave;
    float* P = (float*)(ws + OFF_KVP) + (size_t)(mt * 32 + head) * 504;
    if (r32 <= 20) {
#pragma unroll
      for (int q = 0; q < 3; ++q) {
        const int d0 = 8 * q + 4 * hh;
        if (d0 < 20) {
          f32x4 val;
#pragma unroll
          for (int r = 0; r < 4; ++r) val[r] = (r32 == 20) ? az[4 * q + r] : akv[4 * q + r];
          *(f32x4*)(P + r32 * 24 + d0) = val;
        }
      }
    }
  }
}

__global__ __launch_bounds__(512, 2) void k1_gemm(const u16* __restrict__ wqkv, const u16* __restrict__ wg,
                                                   const u16* __restrict__ wu, const u16* __restrict__ wo,
                                                   const u16* __restrict__ wdown, char* __restrict__ ws) {
  extern __shared__ __attribute__((aligned(16))) char smem[];
  const int bid = blockIdx.x;
  if (bid >= 240) {
    retile_all((bid - 240) * 512 + threadIdx.x, wo, wdown, ws);
    return;
  }
  const int p = (bid & 7) * 30 + (bid >> 3);
  const int mh = p / 120, rem = p % 120, nt = rem >> 3, mt = mh * 8 + (rem & 7);
  if (nt < 4) k1_body<2>(wqkv, wqkv, ws, mt, nt, smem);
  else if (nt < 6) k1_body<1>(wqkv, wqkv, ws, mt, nt - 4, smem);
  else k1_body<0>(wg, wu, ws, mt, nt - 6, smem);
  STAMP(1, 3);
}

constexpr int K2_LDS = 163840;
#define K2DD 3
constexpr int K2_D = K2DD;
#define K2DW 3
constexpr int K2_DW = K2DW;

__global__ __launch_bounds__(512, 2) void k2_gemm(const u16* __restrict__ x, const char* __restrict__ ws, u16* __restrict__ out) {
  extern __shared__ __attribute__((aligned(16))) char smem[];
  const int tid = threadIdx.x, lane = tid & 63;
  const int wave = __builtin_amdgcn_readfirstlane(tid >> 6);
  const int wq = wave & 3;
  const int g = lane >> 4, c = lane & 15;
  const int bid = blockIdx.x;
  const int mt = (bid & 7) * 4 + (bid >> 6), nt = (bid >> 3) & 7;

  STAMP(2, 0);
  f32x4 acc[4][5];
#pragma unroll
  for (int a = 0; a < 4; ++a)
#pragma unroll
    for (int b = 0; b < 5; ++b) acc[a][b] = (f32x4){0.f, 0.f, 0.f, 0.f};
  const int b0 = wave < 4 ? 0 : 3, nb = wave < 4 ? 3 : 2;
  const int tok = mt * 64 + wq * 16 + c;

  if (wave < 4) {
    const char* hsrc = ws + OFF_H + ((size_t)(mt * 4) * 54 + wq) * 1024 + lane * 16;
    const char* wsrc = ws + OFF_WC + ((size_t)nt * 54 + wq) * 5120 + lane * 16;
    s16x8 xr[K2_D][4], wr[K2_D][5];
    auto ld = [&](int slot, int i) {
#pragma unroll
      for (int b = 0; b < 5; ++b) wr[slot][b] = *(const s16x8*)(wsrc + (size_t)(20 * i + b) * 1024);
#pragma unroll
      for (int a = 0; a < 4; ++a) xr[slot][a] = *(const s16x8*)(hsrc + (size_t)(a * 54 + 4 * i) * 1024);
    };
    auto mma1 = [&](int slot) {
#pragma unroll
      for (int a = 0; a < 4; ++a)
#pragma unroll
        for (int b = 0; b < 5; ++b) acc[a][b] = __builtin_amdgcn_mfma_f32_16x16x32_bf16(wr[slot][b], xr[slot][a], acc[a][b], 0, 0, 0);
    };
#pragma unroll
    for (int i = 0; i < K2_D; ++i) ld(i, i);
    SCHED;
#pragma unroll
    for (int i = 0; i < 13; ++i) {
      mma1(i % K2_D);
      if (i + K2_D < 13) ld(i % K2_D, i + K2_D);
      else if (i + K2_D == 13) { if (wave < 2) ld(i % K2_D, 13); }
      SCHED;
    }
    if (wave < 2) mma1(13 % K2_D);
    STAMP(2, 1);
  } else {
    __builtin_amdgcn_s_setprio(3);
    char* reg = smem + wq * 36864;
    if (bid < 32) {
      const int t = tid - 256, u = t >> 2, q = t & 3, uu = u < 63 ? u : 62, e = uu / 3, g3 = uu - e * 3;
      const float* src = (const float*)(ws + OFF_KVP) + ((size_t)(4 * q) * 32 + bid) * 504 + e * 24 + 8 * g3;
      f32x4 lo[4], hi[4];
#pragma unroll
      for (int i = 0; i < 4; ++i) { lo[i] = *(const f32x4*)(src + i * 32 * 504); hi[i] = *(const f32x4*)(src + i * 32 * 504 + (g3 < 2 ? 4 : 0)); }
      f32x4 s0 = (lo[0] + lo[1]) + (lo[2] + lo[3]), s1 = (hi[0] + hi[1]) + (hi[2] + hi[3]);
#pragma unroll
      for (int o = 1; o <= 2; o <<= 1)
#pragma unroll
        for (int r = 0; r < 4; ++r) { s0[r] += __shfl_xor(s0[r], o); s1[r] += __shfl_xor(s1[r], o); }
      u32x4 w;
      w[0] = pack2(s0[0], s0[1]); w[1] = pack2(s0[2], s0[3]);
      w[2] = g3 < 2 ? pack2(s1[0], s1[1]) : 0u; w[3] = g3 < 2 ? pack2(s1[2], s1[3]) : 0u;
#pragma unroll
      for (int r = 0; r < 4; ++r) w[r] = w[r] == KV_EMPTY ? 0x7FC07FC0u : w[r];
      const int slot = e < 16 ? g3 * 16 + e : 64 + g3 * 16 + (e < 20 ? (e - 16) * 4 : 4 * q + 1);
      if ((q == 0 || e == 20) && u < 63)
        asm volatile("global_store_dwordx4 %0, %1, off sc1\n\ts_nop 1" :: "v"(ws + OFF_KVF + ((size_t)bid * 128 + slot) * 16), "v"(w) : "memory");
    }
    {
      const char* pqg = ws + OFF_PQ + ((size_t)(mt * 4) * 32 + wq * 8) * 640 + lane * 16;
#pragma unroll
      for (int tg = 0; tg < 4; ++tg)
#pragma unroll
        for (int i = 0; i < 5; ++i)
          __builtin_amdgcn_global_load_lds((const unsigned*)(pqg + tg * 20480 + i * 1024), (unsigned*)(reg + tg * 5120 + i * 1024), 16, 0, 0);
    }
    const char* wosrc = ws + OFF_WO + (size_t)(nt * 4 + wq) * 25600 + lane * 16;
    s16x8 w2[K2_DW][5];
    auto ldW = [&](int s) {
#pragma unroll
      for (int b = 0; b < 5; ++b) w2[s % K2_DW][b] = *(const s16x8*)(wosrc + (s * 5 + b) * 1024);
    };
#pragma unroll
    for (int s = 0; s < K2_DW; ++s) ldW(s);
    SCHED;
    const char* kvl = reg + 20480 + lane * 16;
    const char* kg = ws + OFF_KVF + (size_t)(wq * 8) * 2048;
    const unsigned kvm0 = __builtin_amdgcn_readfirstlane((unsigned)(size_t)(__attribute__((address_space(3))) char*)(reg + 20480));
    auto kvload = [&](int i0_, int i1_) {
#pragma unroll
      for (int i = 0; i < 16; ++i)
        if (i >= i0_ && i < i1_)
          asm volatile("s_mov_b32 m0, %0\n\ts_nop 0\n\tglobal_load_lds_dwordx4 %1, %2 sc1" :: "s"(kvm0 + i * 1024), "v"(lane * 16), "s"(kg + i * 1024) : "memory");
    };
    auto kvwait = [&](int i0_, int i1_) {
      while (true) {
        asm volatile("s_waitcnt vmcnt(0)" ::: "memory");
        bool bad = false;
#pragma unroll
        for (int i = 0; i < 16; ++i)
          if (i >= i0_ && i < i1_) {
            const u32x4 v = *(const __attribute__((address_space(3))) u32x4*)(kvl + i * 1024);
            bad = bad || v[0] == KV_EMPTY || v[1] == KV_EMPTY || v[2] == KV_EMPTY || v[3] == KV_EMPTY;
            if ((i & 3) == 3) __builtin_amdgcn_sched_barrier(0);
          }
        if (__builtin_amdgcn_ballot_w64(bad) == 0ull) break;
        __builtin_amdgcn_s_sleep(2);
        kvload(i0_, i1_);
      }
    };
    kvload(0, 4);
    kvwait(0, 4);
    __builtin_amdgcn_s_waitcnt(0x0F70);
    kvload(4, 16);
    SCHED;
    STAMPW(4);
    const char* pql = reg + (g < 3 ? g : 0) * 256 + c * 16;
    const f32x4 z4 = (f32x4){0.f, 0.f, 0.f, 0.f};
    unsigned ow[4][20];
    auto mma2 = [&](int s) {
#pragma unroll
      for (int a = 0; a < 4; ++a) {
        const u32x4 o4 = (u32x4){ow[a][4 * s], ow[a][4 * s + 1], ow[a][4 * s + 2], ow[a][4 * s + 3]};
        const s16x8 of = __builtin_bit_cast(s16x8, o4);
#pragma unroll
        for (int b = 0; b < 5; ++b) acc[a][b] = __builtin_amdgcn_mfma_f32_16x16x32_bf16(w2[s % K2_DW][b], of, acc[a][b], 0, 0, 0);
      }
      if (s + K2_DW < 5) ldW(s + K2_DW);
    };
#pragma unroll
    for (int m = 0; m < 4; ++m) {
      if (m == 1) kvwait(4, 16);
      f32x4 ov[2][4]; float oe[2][4];
#pragma unroll
      for (int h2 = 0; h2 < 2; ++h2) {
        const int hh = 2 * m + h2;
        const s16x8 k0 = *(const s16x8*)(kvl + hh * 2048), k1 = *(const s16x8*)(kvl + hh * 2048 + 1024);
#pragma unroll
        for (int tg = 0; tg < 4; ++tg) {
          u32x4 pq;
          if ((hh & 1) == 0) pq = *(const u32x4*)(pql + tg * 5120 + (5 * hh / 2) * 256);
          else {
            const u32x2 p0 = *(const u32x2*)(pql + tg * 5120 + ((5 * hh - 1) / 2) * 256 + 8), p1 = *(const u32x2*)(pql + tg * 5120 + ((5 * hh - 1) / 2) * 256 + 256);
            pq = (u32x4){p0[0], p0[1], p1[0], p1[1]};
          }
          u32x4 pk4;
          pk4[0] = g < 3 ? pq[0] : 0u; pk4[1] = g < 3 ? pq[1] : 0u;
          pk4[2] = g < 2 ? pq[2] : 0u; pk4[3] = g < 2 ? pq[3] : 0u;
          const s16x8 pf = __builtin_bit_cast(s16x8, pk4);
          const f32x4 d1 = __builtin_amdgcn_mfma_f32_16x16x32_bf16(k1, pf, z4, 0, 0, 0);
          const f32x4 d0 = __builtin_amdgcn_mfma_f32_16x16x32_bf16(k0, pf, z4, 0, 0, 0);
          const float rden = frcp(bfr(bfr(d1[1]) + 1e-6f));
          const f32x2 lo = bfr2(lo2(d0)) * rden, hi = bfr2(hi2(d0)) * rden;
          ov[h2][tg] = (f32x4){lo[0], lo[1], hi[0], hi[1]};
          oe[h2][tg] = bfr(d1[0]) * rden;
        }
      }
#pragma unroll
      for (int tg = 0; tg < 4; ++tg) {
        ow[tg][5 * m + 0] = cvt2(lo2(ov[0][tg]));
        ow[tg][5 * m + 1] = cvt2(hi2(ov[0][tg]));
        ow[tg][5 * m + 2] = cvt2((f32x2){oe[0][tg], ov[1][tg][0]});
        ow[tg][5 * m + 3] = cvt2((f32x2){ov[1][tg][1], ov[1][tg][2]});
        ow[tg][5 * m + 4] = cvt2((f32x2){ov[1][tg][3], oe[1][tg]});
      }
      mma2(m);
      if (m == 3) mma2(4);
      SCHED;
      if (m < 2) STAMPW(5 + m);
    }
  }
  u32x2 xw[3];
#pragma unroll
  for (int i = 0; i < 3; ++i) xw[i] = *(const u32x2*)(x + (size_t)tok * D_ + nt * 80 + (i < nb ? b0 + i : b0) * 16 + 4 * g);
  __syncthreads();

  f32x4* red = (f32x4*)smem;
#pragma unroll
  for (int a = 0; a < 4; ++a)
#pragma unroll
    for (int b = 0; b < 5; ++b) red[(wave * 20 + a * 5 + b) * 64 + lane] = acc[a][b];
  __syncthreads();
#pragma unroll
  for (int i = 0; i < 3; ++i) {
    if (i < nb) {
      const int f = wq * 5 + b0 + i;
      f32x4 y1 = red[(0 * 20 + f) * 64 + lane], y2 = red[(4 * 20 + f) * 64 + lane];
#pragma unroll
      for (int sw = 1; sw < 4; ++sw) { y1 += red[(sw * 20 + f) * 64 + lane]; y2 += red[((4 + sw) * 20 + f) * 64 + lane]; }
      const int n = nt * 80 + (b0 + i) * 16 + 4 * g;
      u32x2 w;
      {
        const f32x2 y = bfr2(lo2(y1)), gs = bfr2(y * sigm2(y)), sv = bfr2(lo2(y2));
        w[0] = cvt2(unp2(xw[i][0]) + bfr2(sv * gs));
      }
      {
        const f32x2 y = bfr2(hi2(y1)), gs = bfr2(y * sigm2(y)), sv = bfr2(hi2(y2));
        w[1] = cvt2(unp2(xw[i][1]) + bfr2(sv * gs));
      }
      *(u32x2*)(out + (size_t)tok * D_ + n) = w;
    }
  }
  STAMP(2, 3);
}

extern "C" void kernel_launch(void* const* d_in, const int* in_sizes, int n_in,
                              void* d_out, int out_size, void* d_ws, size_t ws_size,
                              hipStream_t stream) {
  const u16* x = (const u16*)d_in[0];
  const u16* wqkv = (const u16*)d_in[1];
  const u16* wo = (const u16*)d_in[2];
  const u16* wg = (const u16*)d_in[4];
  const u16* wu = (const u16*)d_in[5];
  const u16* wd = (const u16*)d_in[6];
  char* ws = (char*)d_ws;
  u16* out = (u16*)d_out;

  hipFuncSetAttribute((const void*)k1_gemm, hipFuncAttributeMaxDynamicSharedMemorySize, K1_NS * K1_STAGE);
  hipFuncSetAttribute((const void*)k2_gemm, hipFuncAttributeMaxDynamicSharedMemorySize, K2_LDS);

  k0_prep<<<512, 256, 0, stream>>>(x, ws, wqkv, wg, wu);
  k1_gemm<<<256, 512, K1_NS * K1_STAGE, stream>>>(wqkv, wg, wu, wo, wd, ws);
  k2_gemm<<<256, 512, K2_LDS, stream>>>(x, ws, out);
}
```

```cpp
#include <hip/hip_runtime.h>
#include <hip/hip_bf16.h>
#include <stdint.h>
#include <type_traits>

typedef unsigned short u16;
typedef __attribute__((ext_vector_type(8))) short s16x8;
typedef __attribute__((ext_vector_type(4))) short s16x4;
typedef __attribute__((ext_vector_type(4))) float f32x4;
typedef __attribute__((ext_vector_type(16))) float f32x16;
typedef __attribute__((ext_vector_type(2))) unsigned u32x2;
typedef __attribute__((ext_vector_type(4))) unsigned u32x4;
typedef u32x4 u32x4_a8 __attribute__((aligned(8)));
typedef __attribute__((address_space(3))) s16x4* lds_s16x4_ptr;
typedef __attribute__((address_space(1))) unsigned gu32;
typedef __attribute__((address_space(1))) unsigned long long gu64;

#define RLX_AGENT __ATOMIC_RELAXED, __HIP_MEMORY_SCOPE_AGENT

constexpr int S_ = 2048, D_ = 640, F_ = 1728, NQKV_ = 1920;

constexpr size_t OFF_CNT   = 0;
constexpr size_t OFF_XN    = 8192;
constexpr size_t SZ_XN     = (size_t)S_ * D_ * 2;
constexpr size_t OFF_H     = OFF_XN + SZ_XN;
constexpr size_t SZ_H      = (size_t)S_ * F_ * 2;
constexpr size_t OFF_PQ    = OFF_H + SZ_H;
constexpr size_t SZ_PQ     = (size_t)S_ * D_ * 2;
constexpr size_t OFF_WC    = OFF_PQ + SZ_PQ;
constexpr size_t SZ_WC     = (size_t)8 * 54 * 5 * 1024;
constexpr size_t OFF_WO    = OFF_WC + SZ_WC;
constexpr size_t SZ_WO     = (size_t)8 * 4 * 5 * 5 * 1024;
constexpr size_t OFF_KVP   = OFF_WO + SZ_WO;
constexpr size_t SZ_KVP    = (size_t)16 * 32 * 504 * 4;
constexpr size_t OFF_KVF   = OFF_KVP + SZ_KVP;
constexpr size_t SZ_KVF    = 65536;

#define KVAUX 16
constexpr unsigned KV_EMPTY = 0xFFFFFFFFu;
__device__ __forceinline__ float bf2f(u16 v) { return __uint_as_float(((unsigned)v) << 16); }
__device__ __forceinline__ u16 f2bf(float f) { return __builtin_bit_cast(u16, (__bf16)f); }
__device__ __forceinline__ float bfr(float f) { return bf2f(f2bf(f)); }
__device__ __forceinline__ unsigned pack2(float a, float b) { return (unsigned)f2bf(a) | ((unsigned)f2bf(b) << 16); }
__device__ __forceinline__ float phi_f(float t) {
  float p = t > 0.f ? bfr(t + 1.0f) : bfr(__expf(t));
  return bfr(p + 1e-6f);
}
typedef __attribute__((ext_vector_type(2))) float f32x2;
typedef __attribute__((ext_vector_type(2))) __bf16 bf16x2;
__device__ __forceinline__ unsigned cvt2(f32x2 v) { return __builtin_bit_cast(unsigned, __builtin_convertvector(v, bf16x2)); }
__device__ __forceinline__ f32x2 unp2(unsigned u) { return (f32x2){__uint_as_float(u << 16), __uint_as_float(u & 0xffff0000u)}; }
__device__ __forceinline__ f32x2 bfr2(f32x2 v) { return unp2(cvt2(v)); }
__device__ __forceinline__ f32x2 lo2(f32x4 v) { return __builtin_shufflevector(v, v, 0, 1); }
__device__ __forceinline__ f32x2 hi2(f32x4 v) { return __builtin_shufflevector(v, v, 2, 3); }
__device__ __forceinline__ unsigned phi2a(f32x2 a, float& mn) {
  const f32x2 t = bfr2(a), m = t * 1.44269504088896340736f, p = t + 1.0f;
  f32x2 sel;
  sel[0] = __builtin_amdgcn_fmed3f(__builtin_amdgcn_exp2f(m[0]), p[0], 1.0f);
  sel[1] = __builtin_amdgcn_fmed3f(__builtin_amdgcn_exp2f(m[1]), p[1], 1.0f);
  mn = __builtin_fminf(__builtin_fminf(mn, sel[0]), sel[1]);
  return cvt2(sel);
}
__device__ __forceinline__ bool phi_need_eps(float mn) { return __builtin_amdgcn_ballot_w64(!(mn >= 0.00048828125f)) != 0ull; }
__device__ __forceinline__ unsigned phi2b(unsigned w) { return cvt2(unp2(w) + 1e-6f); }
__device__ __forceinline__ f32x2 sigm2(f32x2 g) {
  const f32x2 m = g * -1.44269504088896340736f;
  f32x2 e; e[0] = __builtin_amdgcn_exp2f(m[0]); e[1] = __builtin_amdgcn_exp2f(m[1]);
  e = e + 1.0f;
  f32x2 r; r[0] = __builtin_amdgcn_rcpf(e[0]); r[1] = __builtin_amdgcn_rcpf(e[1]);
  return r;
}
__device__ __forceinline__ int aswz(int row) { int x = (row >> 2) & 3; return (((x ^ (x >> 1)) & 1) << 1) | (x >> 1); }

#define WAIT_V(n) asm volatile("s_waitcnt vmcnt(" #n ")" ::: "memory")
#define STAMP(kid, idx)
#define STAMPW(idx)
#define WAIT_L0 asm volatile("s_waitcnt lgkmcnt(0)" ::: "memory")
#define BAR __builtin_amdgcn_s_barrier()
#define SCHED __builtin_amdgcn_sched_barrier(0)
__device__ __forceinline__ float frcp(float v) { return __builtin_amdgcn_rcpf(v); }

constexpr int RT_WD = 216 * 80, RT_ITEMS = RT_WD + 80 * 80, RT_R = 3;
__device__ __forceinline__ void retile_all(int t0, const u16* __restrict__ wo, const u16* __restrict__ wdown, char* __restrict__ ws) {
  u32x4 r[RT_R][8];
  char* dst[RT_R];
#pragma unroll
  for (int it = 0; it < RT_R; ++it) {
    int t = t0 + it * 8192; if (t >= RT_ITEMS) t = t0;
    const bool isd = t < RT_WD;
    const int t2 = isd ? t : t - RT_WD;
    const int ko = t2 / 80, noct = t2 - ko * 80;
    const int n0 = noct * 8, nt = n0 / 80, nn = n0 - nt * 80, nf = nn >> 4, c0 = nn & 15, gg = ko & 3, kq = ko >> 2;
    const int wq = kq / 5, ks = kq - wq * 5;
    const u16* src = (isd ? wdown : wo) + n0;
#pragma unroll
    for (int jj = 0; jj < 8; ++jj) {
      const int q = 8 * ks + jj, hh = q / 5, rr = q - hh * 5;
      const int row = isd ? ko * 8 + jj : (wq * 8 + hh) * 20 + (rr < 4 ? 4 * gg + rr : 16 + gg);
      r[it][jj] = *(const u32x4*)(src + (size_t)row * D_);
    }
    dst[it] = ws + (isd ? OFF_WC + (size_t)(nt * 54 + kq) * 5120 : OFF_WO + (size_t)(nt * 20 + kq) * 5120) + (size_t)(nf * 64 + gg * 16 + c0) * 16;
  }
#pragma unroll
  for (int it = 0; it < RT_R; ++it) {
#pragma unroll
    for (int i = 0; i < 4; ++i) {
      u32x4 lo, hi;
#pragma unroll
      for (int q = 0; q < 4; ++q) {
        lo[q] = (r[it][2 * q][i] & 0xffffu) | (r[it][2 * q + 1][i] << 16);
        hi[q] = (r[it][2 * q][i] >> 16) | (r[it][2 * q + 1][i] & 0xffff0000u);
      }
      *(u32x4*)(dst[it] + (2 * i) * 16) = lo;
      *(u32x4*)(dst[it] + (2 * i + 1) * 16) = hi;
    }
  }
}

__global__ __launch_bounds__(256) void k0_prep(const u16* __restrict__ x, char* __restrict__ ws, const u16* __restrict__ wqkv,
                                               const u16* __restrict__ wg, const u16* __restrict__ wu) {
  const int tid = threadIdx.x;
  if (blockIdx.x >= 256) {
    const int t = (blockIdx.x - 256) * 256 + tid;
    unsigned v0, v1;
    const int l1 = t + 65536 < 107520 ? t + 65536 : t;
    const char* p0 = t < 38400 ? (const char*)wqkv + (size_t)t * 64 : (const char*)wg + (size_t)(t - 38400) * 64;
    const char* p1 = l1 < 38400 ? (const char*)wqkv + (size_t)l1 * 64 : (l1 < 72960 ? (const char*)wg + (size_t)(l1 - 38400) * 64 : (const char*)wu + (size_t)(l1 - 72960) * 64);
    asm volatile("global_load_dword %0, %2, off\n\tglobal_load_dword %1, %3, off\n\ts_waitcnt vmcnt(0)" : "=&v"(v0), "=&v"(v1) : "v"(p0), "v"(p1) : "memory");
    return;
  }
  {
    if (blockIdx.x < 16) {
      const int ci = blockIdx.x * 256 + tid, fr = (ci >> 6) & 1, gg = (ci >> 4) & 3, cc = ci & 15;
      const unsigned v = (gg < 3 && (fr == 0 || (cc & 3) < 2)) ? KV_EMPTY : 0u;
      *(u32x4*)(ws + OFF_KVF + (size_t)ci * 16) = (u32x4){v, v, v, v};
    }
    const int row = blockIdx.x * 8 + (tid >> 5);
    const int l32 = tid & 31;
    const u16* xr = x + (size_t)row * D_;
    float v[20];
    float sum = 0.f;
#pragma unroll
    for (int i = 0; i < 5; ++i) {
      u32x2 w = *(const u32x2*)(xr + (l32 + 32 * i) * 4);
      v[4 * i + 0] = __uint_as_float(w[0] << 16);
      v[4 * i + 1] = __uint_as_float(w[0] & 0xffff0000u);
      v[4 * i + 2] = __uint_as_float(w[1] << 16);
      v[4 * i + 3] = __uint_as_float(w[1] & 0xffff0000u);
      sum += v[4 * i] + v[4 * i + 1] + v[4 * i + 2] + v[4 * i + 3];
    }
#pragma unroll
    for (int o = 16; o >= 1; o >>= 1) sum += __shfl_xor(sum, o);
    const float mean = sum * (1.0f / D_);
    float sq = 0.f;
#pragma unroll
    for (int i = 0; i < 20; ++i) { float d = v[i] - mean; sq += d * d; }
#pragma unroll
    for (int o = 16; o >= 1; o >>= 1) sq += __shfl_xor(sq, o);
    const float mb = bfr(mean);
    const float vb = bfr(sq * (1.0f / D_));
    const float sd = bfr(sqrtf(bfr(vb + 1e-5f)));
    const float rsd = 1.0f / sd;
    u16* xn = (u16*)(ws + OFF_XN);
    const int mt = row >> 7, r = row & 127, sw = aswz(r);
#pragma unroll
    for (int i = 0; i < 5; ++i) {
      const int k = (l32 + 32 * i) * 4;
      float o0 = bfr(v[4 * i + 0] - mb) * rsd, o1 = bfr(v[4 * i + 1] - mb) * rsd;
      float o2 = bfr(v[4 * i + 2] - mb) * rsd, o3 = bfr(v[4 * i + 3] - mb) * rsd;
      u32x2 w; w[0] = pack2(o0, o1); w[1] = pack2(o2, o3);
      const int kt = k >> 5, ch = (k >> 3) & 3, hf = (k >> 2) & 1;
      size_t off = ((size_t)(mt * 20 + kt) * 128 + r) * 32 + ((ch ^ sw) << 3) + (hf << 2);
      *(u32x2*)(xn + off) = w;
    }
  }
  STAMP(0, 3);
}

constexpr int K1_STAGE = 32768;
#define K1NS 4
constexpr int K1_NS = K1NS;

__device__ __forceinline__ int kvnf(int wn, int i) {
  return wn < 2 ? (i < 3 ? 3 * wn + i : 10 + 2 * wn + (i - 3)) : (i < 2 ? 2 + 2 * wn + i : 8 + 3 * wn + (i - 2));
}
template <int TYPE>
__device__ __forceinline__ void k1_body(const u16* __restrict__ wA, const u16* __restrict__ wB, char* __restrict__ ws,
                                        int mt, int j, char* smem) {
  constexpr int NCB = TYPE == 0 ? 6 : 5;
  constexpr int NFR = TYPE == 0 ? 6 : 5;
  constexpr int LDW = TYPE == 0 ? F_ : NQKV_;
  const int tid = threadIdx.x, lane = tid & 63;
  const int wave = __builtin_amdgcn_readfirstlane(tid >> 6);
  const int wm = wave >> 2, wn = (TYPE == 2 && wave >= 4) ? ((wave + 2) & 3) : (wave & 3);
  const int g = lane >> 4, c = lane & 15;

  const char* asrc = ws + OFF_XN + (size_t)mt * 20 * 8192 + wave * 1024 + lane * 16;
  const u16* bsrc[3];
#pragma unroll
  for (int t = 0; t < 3; ++t) {
    int ii = wave + 8 * t; if (ii >= NCB * 4) ii = wave;
    const int cb = ii >> 2, krow = (ii & 3) * 8 + (lane >> 3), u = lane & 7;
    const int sk = ((krow >> 1) & 1) | (((krow >> 3) & 1) << 1);
    const int ch = (u >> 1) ^ sk;
    const int lc = cb * 64 + ch * 16 + (u & 1) * 8;
    const u16* p;
    if (TYPE == 0) p = (lc < 192) ? (wA + j * 192 + lc) : (wB + j * 192 + (lc - 192));
    else if (TYPE == 1) p = wA + j * 320 + lc;
    else p = (lc < 160) ? (wA + 640 + j * 160 + lc) : (wA + 1280 + j * 160 + (lc - 160));
    bsrc[t] = p + (size_t)krow * LDW;
  }
  const bool has3 = (NCB == 6) || (wave < 4);

  auto issue = [&](int kt) {
    char* sb = smem + (kt % K1_NS) * K1_STAGE;
    __builtin_amdgcn_global_load_lds((const unsigned*)(asrc + (size_t)kt * 8192), (unsigned*)(sb + wave * 1024), 16, 0, 0);
    __builtin_amdgcn_global_load_lds((const unsigned*)(bsrc[0] + (size_t)kt * 32 * LDW), (unsigned*)(sb + 8192 + wave * 1024), 16, 0, 0);
    __builtin_amdgcn_global_load_lds((const unsigned*)(bsrc[1] + (size_t)kt * 32 * LDW), (unsigned*)(sb + 8192 + (wave + 8) * 1024), 16, 0, 0);
    if (has3) __builtin_amdgcn_global_load_lds((const unsigned*)(bsrc[2] + (size_t)kt * 32 * LDW), (unsigned*)(sb + 8192 + (wave + 16) * 1024), 16, 0, 0);
  };

  const int xoff = (wm * 64 + c) * 64 + ((g ^ aswz(c)) << 4);
  int woff[NFR];
  {
    const int q = (lane & 15) >> 2, p = lane & 3;
    const int s = (q >> 1) | ((g & 1) << 1);
    const int lo = (8 * g + q) * 128 + (s << 5) + p * 8;
#pragma unroll
    for (int i = 0; i < NFR; ++i) {
      const int nf = TYPE == 0 ? (i < 3 ? 3 * wn + i : 12 + 3 * wn + (i - 3)) : (TYPE == 1 ? 5 * wn + i : kvnf(wn, i));
      woff[i] = 8192 + (nf >> 2) * 4096 + (lo ^ ((nf & 3) << 5));
    }
  }

  f32x4 acc[4][NFR];
#pragma unroll
  for (int a = 0; a < 4; ++a)
#pragma unroll
    for (int b = 0; b < NFR; ++b) acc[a][b] = (f32x4){0.f, 0.f, 0.f, 0.f};

  auto readfr = [&](int kt, s16x8 (&xf)[4], s16x8 (&wf)[NFR]) {
    const char* sb = smem + (kt % K1_NS) * K1_STAGE;
#pragma unroll
    for (int b = 0; b < NFR; ++b) {
      s16x4 lo = __builtin_amdgcn_ds_read_tr16_b64_v4i16((lds_s16x4_ptr)(sb + woff[b]));
      s16x4 hi = __builtin_amdgcn_ds_read_tr16_b64_v4i16((lds_s16x4_ptr)(sb + woff[b] + 512));
      wf[b] = __builtin_shufflevector(lo, hi, 0, 1, 2, 3, 4, 5, 6, 7);
    }
#pragma unroll
    for (int a = 0; a < 4; ++a) xf[a] = *(const s16x8*)(sb + xoff + a * 1024);
  };
  auto mma = [&](const s16x8 (&xf)[4], const s16x8 (&wf)[NFR]) {
#pragma unroll
    for (int a = 0; a < 4; ++a)
#pragma unroll
      for (int b = 0; b < NFR; ++b) {
        if (TYPE == 2) acc[a][b] = __builtin_amdgcn_mfma_f32_16x16x32_bf16(xf[a], wf[b], acc[a][b], 0, 0, 0);
        else           acc[a][b] = __builtin_amdgcn_mfma_f32_16x16x32_bf16(wf[b], xf[a], acc[a][b], 0, 0, 0);
      }
  };
#define WAITN(N) asm volatile("s_waitcnt vmcnt(%0)" :: "n"(N) : "memory")
#define K1_WAIT(n) do { if (has3) WAITN(4 * (n)); else WAITN(3 * (n)); } while (0)

  s16x8 xA[4], wAf[NFR], xB[4], wBf[NFR];
#define SGB(m, n) __builtin_amdgcn_sched_group_barrier(m, n, 0)
  auto pattern_full = [&]() {
    SGB(0x10, 1); SGB(0x8, 1); SGB(0x10, 1); SGB(0x8, 1); SGB(0x10, 1); SGB(0x8, 1); SGB(0x10, 1); SGB(0x8, 1);
#pragma unroll
    for (int i = 0; i < 4 + 2 * NFR; ++i) { SGB(0x100, 1); SGB(0x8, 1); }
    SGB(0x8, 4);
  };
  auto pattern_tail = [&]() {
#pragma unroll
    for (int i = 0; i < 4 + 2 * NFR; ++i) { SGB(0x100, 1); SGB(0x8, 1); }
    SGB(0x8, 8);
  };
  STAMP(1, 0);
#pragma unroll
  for (int i = 0; i < K1_NS; ++i) issue(i);
  K1_WAIT(K1_NS - 1);
  BAR;
  STAMP(1, 1);
  readfr(0, xA, wAf);
  SCHED;
#pragma unroll
  for (int kt = 0; kt < 19; ++kt) {
    if (kt + K1_NS <= 20) K1_WAIT(K1_NS - 2); else K1_WAIT(18 - kt);
    WAIT_L0; BAR; SCHED;
    if (kt + K1_NS < 20) issue(kt + K1_NS);
    if (kt & 1) { readfr(kt + 1, xA, wAf); mma(xB, wBf); }
    else        { readfr(kt + 1, xB, wBf); mma(xA, wAf); }
    if (kt + K1_NS < 20) pattern_full(); else pattern_tail();
    SCHED;
  }
  mma(xB, wBf);

  STAMP(1, 2);
  if (TYPE == 0) {
    char* H = ws + OFF_H + ((size_t)(mt * 8 + wm * 4 + (g & 1)) * 54 + j * 6) * 1024 + ((g >> 1) * 16 + c) * 16;
#pragma unroll
    for (int a = 0; a < 4; a += 2) {
#pragma unroll
      for (int b = 0; b < 3; ++b) {
        u32x2 w[2];
#pragma unroll
        for (int h = 0; h < 2; ++h) {
          w[h][0] = cvt2(bfr2(sigm2(bfr2(lo2(acc[a + h][b])))) * bfr2(lo2(acc[a + h][b + 3])));
          w[h][1] = cvt2(bfr2(sigm2(bfr2(hi2(acc[a + h][b])))) * bfr2(hi2(acc[a + h][b + 3])));
        }
        const auto s0 = __builtin_amdgcn_permlane16_swap(w[0][0], w[1][0], false, false);
        const auto s1 = __builtin_amdgcn_permlane16_swap(w[0][1], w[1][1], false, false);
        const int f0 = wn * 48 + b * 16;
        *(u32x4*)(H + (size_t)a * 54 * 1024 + (f0 >> 5) * 1024 + ((f0 >> 4) & 1) * 512) = (u32x4){s0[0], s1[0], s0[1], s1[1]};
      }
    }
  } else if (TYPE == 1) {
    char* PQ = ws + OFF_PQ + ((size_t)(mt * 8 + wm * 4 + (g & 1)) * 20 + j * 10) * 1024 + ((g >> 1) * 16 + c) * 16;
#pragma unroll
    for (int a = 0; a < 4; a += 2) {
      u32x2 w[5][2];
      float mn = 1.0f;
#pragma unroll
      for (int b = 0; b < 5; ++b)
#pragma unroll
        for (int h = 0; h < 2; ++h) { w[b][h][0] = phi2a(lo2(acc[a + h][b]), mn); w[b][h][1] = phi2a(hi2(acc[a + h][b]), mn); }
      if (phi_need_eps(mn)) {
#pragma unroll
        for (int b = 0; b < 5; ++b)
#pragma unroll
          for (int h = 0; h < 2; ++h) { w[b][h][0] = phi2b(w[b][h][0]); w[b][h][1] = phi2b(w[b][h][1]); }
      }
#pragma unroll
      for (int b = 0; b < 5; ++b) {
        const auto s0 = __builtin_amdgcn_permlane16_swap(w[b][0][0], w[b][1][0], false, false);
        const auto s1 = __builtin_amdgcn_permlane16_swap(w[b][0][1], w[b][1][1], false, false);
        const int f0 = wn * 80 + b * 16;
        *(u32x4*)(PQ + (size_t)a * 20 * 1024 + (f0 >> 5) * 1024 + ((f0 >> 4) & 1) * 512) = (u32x4){s0[0], s1[0], s0[1], s1[1]};
      }
    }
  } else {
    static_assert(K1_NS == 4 && 332 * 272 <= 3 * K1_STAGE, "T must not overlap the slot of the last stage");
    char* tw = smem + c * 272 + (wm * 64 + 4 * g) * 2;
    if (tid < 16) *(u32x4*)(smem + 320 * 272 + tid * 16) = (u32x4){0x3F803F80u, 0x3F803F80u, 0x3F803F80u, 0x3F803F80u};
    auto kvepi = [&](auto NK_) {
      constexpr int NK = decltype(NK_)::value;
      u32x2 w[4][NK];
      float mn = 1.0f;
#pragma unroll
      for (int a = 0; a < 4; ++a)
#pragma unroll
        for (int b = 0; b < NK; ++b) w[a][b] = (u32x2){phi2a(lo2(acc[a][b]), mn), phi2a(hi2(acc[a][b]), mn)};
#pragma unroll
      for (int a = 0; a < 4; ++a)
#pragma unroll
        for (int b = NK; b < 5; ++b)
          *(u32x2*)(tw + kvnf(wn, b) * 16 * 272 + a * 32) = (u32x2){cvt2(lo2(acc[a][b])), cvt2(hi2(acc[a][b]))};
      if (phi_need_eps(mn)) {
#pragma unroll
        for (int a = 0; a < 4; ++a)
#pragma unroll
          for (int b = 0; b < NK; ++b) w[a][b] = (u32x2){phi2b(w[a][b][0]), phi2b(w[a][b][1])};
      }
#pragma unroll
      for (int a = 0; a < 4; ++a)
#pragma unroll
        for (int b = 0; b < NK; ++b) *(u32x2*)(tw + kvnf(wn, b) * 16 * 272 + a * 32) = w[a][b];
    };
    if (wn < 2) kvepi(std::integral_constant<int, 3>{}); else kvepi(std::integral_constant<int, 2>{});
    STAMP(1, 4);
    __syncthreads();
    STAMP(1, 5);
    const int r32 = lane & 31, hh = lane >> 5;
    f32x16 akv;
#pragma unroll
    for (int i = 0; i < 16; ++i) akv[i] = 0.f;
    const char* ap = smem + (20 * wave + r32) * 272 + 16 * hh;
    const char* bp = smem + (r32 == 20 ? 320 : 160 + 20 * wave + r32) * 272 + 16 * hh;
#pragma unroll
    for (int s = 0; s < 8; ++s)
      akv = __builtin_amdgcn_mfma_f32_32x32x16_bf16(*(const s16x8*)(ap + 32 * s), *(const s16x8*)(bp + 32 * s), akv, 0, 0, 0);
    STAMP(1, 6);
    const int head = j * 8 + wave;
    float* P = (float*)(ws + OFF_KVP) + (size_t)(mt * 32 + head) * 504;
    if (r32 <= 20) {
#pragma unroll
      for (int q = 0; q < 3; ++q) {
        const int d0 = 8 * q + 4 * hh;
        if (d0 < 20) {
          *(f32x4*)(P + r32 * 24 + d0) = (f32x4){akv[4 * q], akv[4 * q + 1], akv[4 * q + 2], akv[4 * q + 3]};
        }
      }
    }
  }
}

__global__ __launch_bounds__(512, 2) void k1_gemm(const u16* __restrict__ wqkv, const u16* __restrict__ wg,
                                                   const u16* __restrict__ wu, const u16* __restrict__ wo,
                                                   const u16* __restrict__ wdown, char* __restrict__ ws) {
  extern __shared__ __attribute__((aligned(16))) char smem[];
  const int bid = blockIdx.x;
  if (bid >= 240) {
    retile_all((bid - 240) * 512 + threadIdx.x, wo, wdown, ws);
    return;
  }
  const int p = (bid & 7) * 30 + (bid >> 3);
  const int mh = p / 120, rem = p % 120, nt = rem >> 3, mt = mh * 8 + (rem & 7);
  if (nt < 4) k1_body<2>(wqkv, wqkv, ws, mt, nt, smem);
  else if (nt < 6) k1_body<1>(wqkv, wqkv, ws, mt, nt - 4, smem);
  else k1_body<0>(wg, wu, ws, mt, nt - 6, smem);
  STAMP(1, 3);
}

constexpr int K2_LDS = 163840;
#define K2DD 3
constexpr int K2_D = K2DD;
#define K2DW 3
constexpr int K2_DW = K2DW;

__global__ __launch_bounds__(512, 2) void k2_gemm(const u16* __restrict__ x, const char* __restrict__ ws, u16* __restrict__ out) {
  extern __shared__ __attribute__((aligned(16))) char smem[];
  const int tid = threadIdx.x, lane = tid & 63;
  const int wave = __builtin_amdgcn_readfirstlane(tid >> 6);
  const int wq = wave & 3;
  const int g = lane >> 4, c = lane & 15;
  const int bid = blockIdx.x;
  const int mt = (bid & 7) * 4 + (bid >> 6), nt = (bid >> 3) & 7;

  STAMP(2, 0);
  f32x4 acc[4][5];
#pragma unroll
  for (int a = 0; a < 4; ++a)
#pragma unroll
    for (int b = 0; b < 5; ++b) acc[a][b] = (f32x4){0.f, 0.f, 0.f, 0.f};
  const int b0 = wave < 4 ? 0 : 3, nb = wave < 4 ? 3 : 2;
  const int tok = mt * 64 + wq * 16 + c;

  if (wave < 4) {
    const char* hsrc = ws + OFF_H + ((size_t)(mt * 4) * 54 + wq) * 1024 + lane * 16;
    const char* wsrc = ws + OFF_WC + ((size_t)nt * 54 + wq) * 5120 + lane * 16;
    s16x8 xr[K2_D][4], wr[K2_D][5];
    auto ld = [&](int slot, int i) {
#pragma unroll
      for (int b = 0; b < 5; ++b) wr[slot][b] = *(const s16x8*)(wsrc + (size_t)(20 * i + b) * 1024);
#pragma unroll
      for (int a = 0; a < 4; ++a) xr[slot][a] = *(const s16x8*)(hsrc + (size_t)(a * 54 + 4 * i) * 1024);
    };
    auto mma1 = [&](int slot) {
#pragma unroll
      for (int a = 0; a < 4; ++a)
#pragma unroll
        for (int b = 0; b < 5; ++b) acc[a][b] = __builtin_amdgcn_mfma_f32_16x16x32_bf16(wr[slot][b], xr[slot][a], acc[a][b], 0, 0, 0);
    };
#pragma unroll
    for (int i = 0; i < K2_D; ++i) ld(i, i);
    SCHED;
#pragma unroll
    for (int i = 0; i < 13; ++i) {
      mma1(i % K2_D);
      if (i + K2_D < 13) ld(i % K2_D, i + K2_D);
      else if (i + K2_D == 13) { if (wave < 2) ld(i % K2_D, 13); }
      SCHED;
    }
    if (wave < 2) mma1(13 % K2_D);
    STAMP(2, 1);
  } else {
    __builtin_amdgcn_s_setprio(3);
    char* reg = smem + wq * 36864;
    if (bid < 32) {
      const int t = tid - 256, u = t >> 2, q = t & 3, uu = u < 63 ? u : 62, e = uu / 3, g3 = uu - e * 3;
      const float* src = (const float*)(ws + OFF_KVP) + ((size_t)(4 * q) * 32 + bid) * 504 + e * 24 + 8 * g3;
      f32x4 lo[4], hi[4];
#pragma unroll
      for (int i = 0; i < 4; ++i) { lo[i] = *(const f32x4*)(src + i * 32 * 504); hi[i] = *(const f32x4*)(src + i * 32 * 504 + (g3 < 2 ? 4 : 0)); }
      f32x4 s0 = (lo[0] + lo[1]) + (lo[2] + lo[3]), s1 = (hi[0] + hi[1]) + (hi[2] + hi[3]);
#pragma unroll
      for (int o = 1; o <= 2; o <<= 1)
#pragma unroll
        for (int r = 0; r < 4; ++r) { s0[r] += __shfl_xor(s0[r], o); s1[r] += __shfl_xor(s1[r], o); }
      u32x4 w;
      w[0] = pack2(s0[0], s0[1]); w[1] = pack2(s0[2], s0[3]);
      w[2] = g3 < 2 ? pack2(s1[0], s1[1]) : 0u; w[3] = g3 < 2 ? pack2(s1[2], s1[3]) : 0u;
#pragma unroll
      for (int r = 0; r < 4; ++r) w[r] = w[r] == KV_EMPTY ? 0x7FC07FC0u : w[r];
      const int slot = e < 16 ? g3 * 16 + e : 64 + g3 * 16 + (e < 20 ? (e - 16) * 4 : 4 * q + 1);
      if ((q == 0 || e == 20) && u < 63)
        asm volatile("global_store_dwordx4 %0, %1, off sc1\n\ts_nop 1" :: "v"(ws + OFF_KVF + ((size_t)bid * 128 + slot) * 16), "v"(w) : "memory");
    }
    {
      const char* pqg = ws + OFF_PQ + ((size_t)(mt * 4) * 32 + wq * 8) * 640 + lane * 16;
#pragma unroll
      for (int tg = 0; tg < 4; ++tg)
#pragma unroll
        for (int i = 0; i < 5; ++i)
          __builtin_amdgcn_global_load_lds((const unsigned*)(pqg + tg * 20480 + i * 1024), (unsigned*)(reg + tg * 5120 + i * 1024), 16, 0, 0);
    }
    const char* wosrc = ws + OFF_WO + (size_t)(nt * 4 + wq) * 25600 + lane * 16;
    s16x8 w2[K2_DW][5];
    auto ldW = [&](int s) {
#pragma unroll
      for (int b = 0; b < 5; ++b) w2[s % K2_DW][b] = *(const s16x8*)(wosrc + (s * 5 + b) * 1024);
    };
#pragma unroll
    for (int s = 0; s < K2_DW; ++s) ldW(s);
    SCHED;
    const char* kvl = reg + 20480 + lane * 16;
    const char* kg = ws + OFF_KVF + (size_t)(wq * 8) * 2048;
    const unsigned kvm0 = __builtin_amdgcn_readfirstlane((unsigned)(size_t)(__attribute__((address_space(3))) char*)(reg + 20480));
    auto kvload = [&](int i0_, int i1_) {
#pragma unroll
      for (int i = 0; i < 16; ++i)
        if (i >= i0_ && i < i1_)
          asm volatile("s_mov_b32 m0, %0\n\ts_nop 0\n\tglobal_load_lds_dwordx4 %1, %2 sc1" :: "s"(kvm0 + i * 1024), "v"(lane * 16), "s"(kg + i * 1024) : "memory");
    };
    auto kvload_l2 = [&](int i0_, int i1_) {
#pragma unroll
      for (int i = 0; i < 16; ++i)
        if (i >= i0_ && i < i1_)
          asm volatile("s_mov_b32 m0, %0\n\ts_nop 0\n\tglobal_load_lds_dwordx4 %1, %2" :: "s"(kvm0 + i * 1024), "v"(lane * 16), "s"(kg + i * 1024) : "memory");
    };
    auto kvwait = [&](int i0_, int i1_) {
      while (true) {
        asm volatile("s_waitcnt vmcnt(0)" ::: "memory");
        bool bad = false;
#pragma unroll
        for (int i = 0; i < 16; ++i)
          if (i >= i0_ && i < i1_) {
            const u32x4 v = *(const __attribute__((address_space(3))) u32x4*)(kvl + i * 1024);
            bad = bad || v[0] == KV_EMPTY || v[1] == KV_EMPTY || v[2] == KV_EMPTY || v[3] == KV_EMPTY;
            if ((i & 3) == 3) __builtin_amdgcn_sched_barrier(0);
          }
        if (__builtin_amdgcn_ballot_w64(bad) == 0ull) break;
        __builtin_amdgcn_s_sleep(2);
        kvload(i0_, i1_);
      }
    };
    kvload(0, 4);
    kvwait(0, 4);
    __builtin_amdgcn_s_waitcnt(0x0F70);
    kvload_l2(4, 16);
    SCHED;
    STAMPW(4);
    const char* pql = reg + (g < 3 ? g : 0) * 256 + c * 16;
    const f32x4 z4 = (f32x4){0.f, 0.f, 0.f, 0.f};
    unsigned ow[4][20];
    auto mma2 = [&](int s) {
#pragma unroll
      for (int a = 0; a < 4; ++a) {
        const u32x4 o4 = (u32x4){ow[a][4 * s], ow[a][4 * s + 1], ow[a][4 * s + 2], ow[a][4 * s + 3]};
        const s16x8 of = __builtin_bit_cast(s16x8, o4);
#pragma unroll
        for (int b = 0; b < 5; ++b) acc[a][b] = __builtin_amdgcn_mfma_f32_16x16x32_bf16(w2[s % K2_DW][b], of, acc[a][b], 0, 0, 0);
      }
      if (s + K2_DW < 5) ldW(s + K2_DW);
    };
#pragma unroll
    for (int m = 0; m < 4; ++m) {
      if (m == 1) kvwait(4, 16);
      f32x4 ov[2][4]; float oe[2][4];
#pragma unroll
      for (int h2 = 0; h2 < 2; ++h2) {
        const int hh = 2 * m + h2;
        const s16x8 k0 = *(const s16x8*)(kvl + hh * 2048), k1 = *(const s16x8*)(kvl + hh * 2048 + 1024);
#pragma unroll
        for (int tg = 0; tg < 4; ++tg) {
          u32x4 pq;
          if ((hh & 1) == 0) pq = *(const u32x4*)(pql + tg * 5120 + (5 * hh / 2) * 256);
          else {
            const u32x2 p0 = *(const u32x2*)(pql + tg * 5120 + ((5 * hh - 1) / 2) * 256 + 8), p1 = *(const u32x2*)(pql + tg * 5120 + ((5 * hh - 1) / 2) * 256 + 256);
            pq = (u32x4){p0[0], p0[1], p1[0], p1[1]};
          }
          u32x4 pk4;
          pk4[0] = g < 3 ? pq[0] : 0u; pk4[1] = g < 3 ? pq[1] : 0u;
          pk4[2] = g < 2 ? pq[2] : 0u; pk4[3] = g < 2 ? pq[3] : 0u;
          const s16x8 pf = __builtin_bit_cast(s16x8, pk4);
          const f32x4 d1 = __builtin_amdgcn_mfma_f32_16x16x32_bf16(k1, pf, z4, 0, 0, 0);
          const f32x4 d0 = __builtin_amdgcn_mfma_f32_16x16x32_bf16(k0, pf, z4, 0, 0, 0);
          const float rden = frcp(bfr(bfr(d1[1]) + 1e-6f));
          const f32x2 lo = bfr2(lo2(d0)) * rden, hi = bfr2(hi2(d0)) * rden;
          ov[h2][tg] = (f32x4){lo[0], lo[1], hi[0], hi[1]};
          oe[h2][tg] = bfr(d1[0]) * rden;
        }
      }
#pragma unroll
      for (int tg = 0; tg < 4; ++tg) {
        ow[tg][5 * m + 0] = cvt2(lo2(ov[0][tg]));
        ow[tg][5 * m + 1] = cvt2(hi2(ov[0][tg]));
        ow[tg][5 * m + 2] = cvt2((f32x2){oe[0][tg], ov[1][tg][0]});
        ow[tg][5 * m + 3] = cvt2((f32x2){ov[1][tg][1], ov[1][tg][2]});
        ow[tg][5 * m + 4] = cvt2((f32x2){ov[1][tg][3], oe[1][tg]});
      }
      mma2(m);
      if (m == 3) mma2(4);
      SCHED;
      if (m < 2) STAMPW(5 + m);
    }
  }
  u32x2 xw[3];
#pragma unroll
  for (int i = 0; i < 3; ++i) xw[i] = *(const u32x2*)(x + (size_t)tok * D_ + nt * 80 + (i < nb ? b0 + i : b0) * 16 + 4 * g);
  __syncthreads();

  f32x4* red = (f32x4*)smem;
#pragma unroll
  for (int a = 0; a < 4; ++a)
#pragma unroll
    for (int b = 0; b < 5; ++b) red[(wave * 20 + a * 5 + b) * 64 + lane] = acc[a][b];
  __syncthreads();
#pragma unroll
  for (int i = 0; i < 3; ++i) {
    if (i < nb) {
      const int f = wq * 5 + b0 + i;
      f32x4 y1 = red[(0 * 20 + f) * 64 + lane], y2 = red[(4 * 20 + f) * 64 + lane];
#pragma unroll
      for (int sw = 1; sw < 4; ++sw) { y1 += red[(sw * 20 + f) * 64 + lane]; y2 += red[((4 + sw) * 20 + f) * 64 + lane]; }
      const int n = nt * 80 + (b0 + i) * 16 + 4 * g;
      u32x2 w;
      {
        const f32x2 y = bfr2(lo2(y1)), gs = bfr2(y * sigm2(y)), sv = bfr2(lo2(y2));
        w[0] = cvt2(unp2(xw[i][0]) + bfr2(sv * gs));
      }
      {
        const f32x2 y = bfr2(hi2(y1)), gs = bfr2(y * sigm2(y)), sv = bfr2(hi2(y2));
        w[1] = cvt2(unp2(xw[i][1]) + bfr2(sv * gs));
      }
      *(u32x2*)(out + (size_t)tok * D_ + n) = w;
    }
  }
  STAMP(2, 3);
}

extern "C" void kernel_launch(void* const* d_in, const int* in_sizes, int n_in,
                              void* d_out, int out_size, void* d_ws, size_t ws_size,
                              hipStream_t stream) {
  const u16* x = (const u16*)d_in[0];
  const u16* wqkv = (const u16*)d_in[1];
  const u16* wo = (const u16*)d_in[2];
  const u16* wg = (const u16*)d_in[4];
  const u16* wu = (const u16*)d_in[5];
  const u16* wd = (const u16*)d_in[6];
  char* ws = (char*)d_ws;
  u16* out = (u16*)d_out;

  hipFuncSetAttribute((const void*)k1_gemm, hipFuncAttributeMaxDynamicSharedMemorySize, K1_NS * K1_STAGE);
  hipFuncSetAttribute((const void*)k2_gemm, hipFuncAttributeMaxDynamicSharedMemorySize, K2_LDS);

  k0_prep<<<512, 256, 0, stream>>>(x, ws, wqkv, wg, wu);
  k1_gemm<<<256, 512, K1_NS * K1_STAGE, stream>>>(wqkv, wg, wu, wo, wd, ws);
  k2_gemm<<<256, 512, K2_LDS, stream>>>(x, ws, out);
}
```

```cpp
#include <hip/hip_runtime.h>
#include <hip/hip_bf16.h>
#include <stdint.h>
#include <type_traits>

typedef unsigned short u16;
typedef __attribute__((ext_vector_type(8))) short s16x8;
typedef __attribute__((ext_vector_type(4))) short s16x4;
typedef __attribute__((ext_vector_type(4))) float f32x4;
typedef __attribute__((ext_vector_type(16))) float f32x16;
typedef __attribute__((ext_vector_type(2))) unsigned u32x2;
typedef __attribute__((ext_vector_type(4))) unsigned u32x4;
typedef u32x4 u32x4_a8 __attribute__((aligned(8)));
typedef __attribute__((address_space(3))) s16x4* lds_s16x4_ptr;
typedef __attribute__((address_space(1))) unsigned gu32;
typedef __attribute__((address_space(1))) unsigned long long gu64;

#define RLX_AGENT __ATOMIC_RELAXED, __HIP_MEMORY_SCOPE_AGENT

constexpr int S_ = 2048, D_ = 640, F_ = 1728, NQKV_ = 1920;

constexpr size_t OFF_CNT   = 0;
constexpr size_t OFF_XN    = 8192;
constexpr size_t SZ_XN     = (size_t)S_ * D_ * 2;
constexpr size_t OFF_H     = OFF_XN + SZ_XN;
constexpr size_t SZ_H      = (size_t)S_ * F_ * 2;
constexpr size_t OFF_PQ    = OFF_H + SZ_H;
constexpr size_t SZ_PQ     = (size_t)S_ * D_ * 2;
constexpr size_t OFF_WC    = OFF_PQ + SZ_PQ;
constexpr size_t SZ_WC     = (size_t)8 * 54 * 5 * 1024;
constexpr size_t OFF_WO    = OFF_WC + SZ_WC;
constexpr size_t SZ_WO     = (size_t)8 * 4 * 5 * 5 * 1024;
constexpr size_t OFF_KVP   = OFF_WO + SZ_WO;
constexpr size_t SZ_KVP    = (size_t)16 * 32 * 504 * 4;
constexpr size_t OFF_KVF   = OFF_KVP + SZ_KVP;
constexpr size_t SZ_KVF    = 32768;

#define KVAUX 16
constexpr unsigned KV_EMPTY = 0xFFFFFFFFu;
__device__ __forceinline__ float bf2f(u16 v) { return __uint_as_float(((unsigned)v) << 16); }
__device__ __forceinline__ u16 f2bf(float f) { return __builtin_bit_cast(u16, (__bf16)f); }
__device__ __forceinline__ float bfr(float f) { return bf2f(f2bf(f)); }
__device__ __forceinline__ unsigned pack2(float a, float b) { return (unsigned)f2bf(a) | ((unsigned)f2bf(b) << 16); }
__device__ __forceinline__ float phi_f(float t) {
  float p = t > 0.f ? bfr(t + 1.0f) : bfr(__expf(t));
  return bfr(p + 1e-6f);
}
typedef __attribute__((ext_vector_type(2))) float f32x2;
typedef __attribute__((ext_vector_type(2))) __bf16 bf16x2;
__device__ __forceinline__ unsigned cvt2(f32x2 v) { return __builtin_bit_cast(unsigned, __builtin_convertvector(v, bf16x2)); }
__device__ __forceinline__ f32x2 unp2(unsigned u) { return (f32x2){__uint_as_float(u << 16), __uint_as_float(u & 0xffff0000u)}; }
__device__ __forceinline__ f32x2 bfr2(f32x2 v) { return unp2(cvt2(v)); }
__device__ __forceinline__ f32x2 lo2(f32x4 v) { return __builtin_shufflevector(v, v, 0, 1); }
__device__ __forceinline__ f32x2 hi2(f32x4 v) { return __builtin_shufflevector(v, v, 2, 3); }
__device__ __forceinline__ unsigned phi2a(f32x2 a, float& mn) {
  const f32x2 t = bfr2(a), m = t * 1.44269504088896340736f, p = t + 1.0f;
  f32x2 sel;
  sel[0] = __builtin_amdgcn_fmed3f(__builtin_amdgcn_exp2f(m[0]), p[0], 1.0f);
  sel[1] = __builtin_amdgcn_fmed3f(__builtin_amdgcn_exp2f(m[1]), p[1], 1.0f);
  mn = __builtin_fminf(__builtin_fminf(mn, sel[0]), sel[1]);
  return cvt2(sel);
}
__device__ __forceinline__ bool phi_need_eps(float mn) { return __builtin_amdgcn_ballot_w64(!(mn >= 0.00048828125f)) != 0ull; }
__device__ __forceinline__ unsigned phi2b(unsigned w) { return cvt2(unp2(w) + 1e-6f); }
__device__ __forceinline__ f32x2 sigm2(f32x2 g) {
  const f32x2 m = g * -1.44269504088896340736f;
  f32x2 e; e[0] = __builtin_amdgcn_exp2f(m[0]); e[1] = __builtin_amdgcn_exp2f(m[1]);
  e = e + 1.0f;
  f32x2 r; r[0] = __builtin_amdgcn_rcpf(e[0]); r[1] = __builtin_amdgcn_rcpf(e[1]);
  return r;
}
__device__ __forceinline__ int aswz(int row) { int x = (row >> 2) & 3; return (((x ^ (x >> 1)) & 1) << 1) | (x >> 1); }

#define WAIT_V(n) asm volatile("s_waitcnt vmcnt(" #n ")" ::: "memory")
#define STAMP(kid, idx)
#define STAMPW(idx)
#define WAIT_L0 asm volatile("s_waitcnt lgkmcnt(0)" ::: "memory")
#define BAR __builtin_amdgcn_s_barrier()
#define SCHED __builtin_amdgcn_sched_barrier(0)
__device__ __forceinline__ float frcp(float v) { return __builtin_amdgcn_rcpf(v); }

constexpr int RT_WD = 216 * 80, RT_ITEMS = RT_WD + 80 * 80, RT_R = 3;
__device__ __forceinline__ void retile_all(int t0, const u16* __restrict__ wo, const u16* __restrict__ wdown, char* __restrict__ ws) {
  u32x4 r[RT_R][8];
  char* dst[RT_R];
#pragma unroll
  for (int it = 0; it < RT_R; ++it) {
    int t = t0 + it * 8192; if (t >= RT_ITEMS) t = t0;
    const bool isd = t < RT_WD;
    const int t2 = isd ? t : t - RT_WD;
    const int ko = t2 / 80, noct = t2 - ko * 80;
    const int n0 = noct * 8, nt = n0 / 80, nn = n0 - nt * 80, nf = nn >> 4, c0 = nn & 15, gg = ko & 3, kq = ko >> 2;
    const int wq = kq / 5, ks = kq - wq * 5;
    const u16* src = (isd ? wdown : wo) + n0;
#pragma unroll
    for (int jj = 0; jj < 8; ++jj) {
      const int q = 8 * ks + jj, hh = q / 5, rr = q - hh * 5;
      const int row = isd ? ko * 8 + jj : (wq * 8 + hh) * 20 + (rr < 4 ? 4 * gg + rr : 16 + gg);
      r[it][jj] = *(const u32x4*)(src + (size_t)row * D_);
    }
    dst[it] = ws + (isd ? OFF_WC + (size_t)(nt * 54 + kq) * 5120 : OFF_WO + (size_t)(nt * 20 + kq) * 5120) + (size_t)(nf * 64 + gg * 16 + c0) * 16;
  }
#pragma unroll
  for (int it = 0; it < RT_R; ++it) {
#pragma unroll
    for (int i = 0; i < 4; ++i) {
      u32x4 lo, hi;
#pragma unroll
      for (int q = 0; q < 4; ++q) {
        lo[q] = (r[it][2 * q][i] & 0xffffu) | (r[it][2 * q + 1][i] << 16);
        hi[q] = (r[it][2 * q][i] >> 16) | (r[it][2 * q + 1][i] & 0xffff0000u);
      }
      *(u32x4*)(dst[it] + (2 * i) * 16) = lo;
      *(u32x4*)(dst[it] + (2 * i + 1) * 16) = hi;
    }
  }
}

__global__ __launch_bounds__(256) void k0_prep(const u16* __restrict__ x, char* __restrict__ ws, const u16* __restrict__ wqkv,
                                               const u16* __restrict__ wg, const u16* __restrict__ wu) {
  const int tid = threadIdx.x;
  if (blockIdx.x >= 256) {
    const int t = (blockIdx.x - 256) * 256 + tid;
    unsigned v0, v1;
    const int l1 = t + 65536 < 107520 ? t + 65536 : t;
    const char* p0 = t < 38400 ? (const char*)wqkv + (size_t)t * 64 : (const char*)wg + (size_t)(t - 38400) * 64;
    const char* p1 = l1 < 38400 ? (const char*)wqkv + (size_t)l1 * 64 : (l1 < 72960 ? (const char*)wg + (size_t)(l1 - 38400) * 64 : (const char*)wu + (size_t)(l1 - 72960) * 64);
    asm volatile("global_load_dword %0, %2, off\n\tglobal_load_dword %1, %3, off\n\ts_waitcnt vmcnt(0)" : "=&v"(v0), "=&v"(v1) : "v"(p0), "v"(p1) : "memory");
    return;
  }
  {
    if (blockIdx.x < 8) {
      const int ci = blockIdx.x * 256 + tid;
      const unsigned v = (ci & 63) != 63 ? KV_EMPTY : 0u;
      *(u32x4*)(ws + OFF_KVF + (size_t)ci * 16) = (u32x4){v, v, v, v};
    }
    const int row = blockIdx.x * 8 + (tid >> 5);
    const int l32 = tid & 31;
    const u16* xr = x + (size_t)row * D_;
    float v[20];
    float sum = 0.f;
#pragma unroll
    for (int i = 0; i < 5; ++i) {
      u32x2 w = *(const u32x2*)(xr + (l32 + 32 * i) * 4);
      v[4 * i + 0] = __uint_as_float(w[0] << 16);
      v[4 * i + 1] = __uint_as_float(w[0] & 0xffff0000u);
      v[4 * i + 2] = __uint_as_float(w[1] << 16);
      v[4 * i + 3] = __uint_as_float(w[1] & 0xffff0000u);
      sum += v[4 * i] + v[4 * i + 1] + v[4 * i + 2] + v[4 * i + 3];
    }
#pragma unroll
    for (int o = 16; o >= 1; o >>= 1) sum += __shfl_xor(sum, o);
    const float mean = sum * (1.0f / D_);
    float sq = 0.f;
#pragma unroll
    for (int i = 0; i < 20; ++i) { float d = v[i] - mean; sq += d * d; }
#pragma unroll
    for (int o = 16; o >= 1; o >>= 1) sq += __shfl_xor(sq, o);
    const float mb = bfr(mean);
    const float vb = bfr(sq * (1.0f / D_));
    const float sd = bfr(sqrtf(bfr(vb + 1e-5f)));
    const float rsd = 1.0f / sd;
    u16* xn = (u16*)(ws + OFF_XN);
    const int mt = row >> 7, r = row & 127, sw = aswz(r);
#pragma unroll
    for (int i = 0; i < 5; ++i) {
      const int k = (l32 + 32 * i) * 4;
      float o0 = bfr(v[4 * i + 0] - mb) * rsd, o1 = bfr(v[4 * i + 1] - mb) * rsd;
      float o2 = bfr(v[4 * i + 2] - mb) * rsd, o3 = bfr(v[4 * i + 3] - mb) * rsd;
      u32x2 w; w[0] = pack2(o0, o1); w[1] = pack2(o2, o3);
      const int kt = k >> 5, ch = (k >> 3) & 3, hf = (k >> 2) & 1;
      size_t off = ((size_t)(mt * 20 + kt) * 128 + r) * 32 + ((ch ^ sw) << 3) + (hf << 2);
      *(u32x2*)(xn + off) = w;
    }
  }
  STAMP(0, 3);
}

constexpr int K1_STAGE = 32768;
#define K1NS 4
constexpr int K1_NS = K1NS;

__device__ __forceinline__ int kvnf(int wn, int i) {
  return wn < 2 ? (i < 3 ? 3 * wn + i : 10 + 2 * wn + (i - 3)) : (i < 2 ? 2 + 2 * wn + i : 8 + 3 * wn + (i - 2));
}
template <int TYPE>
__device__ __forceinline__ void k1_body(const u16* __restrict__ wA, const u16* __restrict__ wB, char* __restrict__ ws,
                                        int mt, int j, char* smem) {
  constexpr int NCB = TYPE == 0 ? 6 : 5;
  constexpr int NFR = TYPE == 0 ? 6 : 5;
  constexpr int LDW = TYPE == 0 ? F_ : NQKV_;
  const int tid = threadIdx.x, lane = tid & 63;
  const int wave = __builtin_amdgcn_readfirstlane(tid >> 6);
  const int wm = wave >> 2, wn = (TYPE == 2 && wave >= 4) ? ((wave + 2) & 3) : (wave & 3);
  const int g = lane >> 4, c = lane & 15;

  const char* asrc = ws + OFF_XN + (size_t)mt * 20 * 8192 + wave * 1024 + lane * 16;
  const u16* bsrc[3];
#pragma unroll
  for (int t = 0; t < 3; ++t) {
    int ii = wave + 8 * t; if (ii >= NCB * 4) ii = wave;
    const int cb = ii >> 2, krow = (ii & 3) * 8 + (lane >> 3), u = lane & 7;
    const int sk = ((krow >> 1) & 1) | (((krow >> 3) & 1) << 1);
    const int ch = (u >> 1) ^ sk;
    const int lc = cb * 64 + ch * 16 + (u & 1) * 8;
    const u16* p;
    if (TYPE == 0) p = (lc < 192) ? (wA + j * 192 + lc) : (wB + j * 192 + (lc - 192));
    else if (TYPE == 1) p = wA + j * 320 + lc;
    else p = (lc < 160) ? (wA + 640 + j * 160 + lc) : (wA + 1280 + j * 160 + (lc - 160));
    bsrc[t] = p + (size_t)krow * LDW;
  }
  const bool has3 = (NCB == 6) || (wave < 4);

  auto issue = [&](int kt) {
    char* sb = smem + (kt % K1_NS) * K1_STAGE;
    __builtin_amdgcn_global_load_lds((const unsigned*)(asrc + (size_t)kt * 8192), (unsigned*)(sb + wave * 1024), 16, 0, 0);
    __builtin_amdgcn_global_load_lds((const unsigned*)(bsrc[0] + (size_t)kt * 32 * LDW), (unsigned*)(sb + 8192 + wave * 1024), 16, 0, 0);
    __builtin_amdgcn_global_load_lds((const unsigned*)(bsrc[1] + (size_t)kt * 32 * LDW), (unsigned*)(sb + 8192 + (wave + 8) * 1024), 16, 0, 0);
    if (has3) __builtin_amdgcn_global_load_lds((const unsigned*)(bsrc[2] + (size_t)kt * 32 * LDW), (unsigned*)(sb + 8192 + (wave + 16) * 1024), 16, 0, 0);
  };

  const int xoff = (wm * 64 + c) * 64 + ((g ^ aswz(c)) << 4);
  int woff[NFR];
  {
    const int q = (lane & 15) >> 2, p = lane & 3;
    const int s = (q >> 1) | ((g & 1) << 1);
    const int lo = (8 * g + q) * 128 + (s << 5) + p * 8;
#pragma unroll
    for (int i = 0; i < NFR; ++i) {
      const int nf = TYPE == 0 ? (i < 3 ? 3 * wn + i : 12 + 3 * wn + (i - 3)) : (TYPE == 1 ? 5 * wn + i : kvnf(wn, i));
      woff[i] = 8192 + (nf >> 2) * 4096 + (lo ^ ((nf & 3) << 5));
    }
  }

  f32x4 acc[4][NFR];
#pragma unroll
  for (int a = 0; a < 4; ++a)
#pragma unroll
    for (int b = 0; b < NFR; ++b) acc[a][b] = (f32x4){0.f, 0.f, 0.f, 0.f};

  auto readfr = [&](int kt, s16x8 (&xf)[4], s16x8 (&wf)[NFR]) {
    const char* sb = smem + (kt % K1_NS) * K1_STAGE;
#pragma unroll
    for (int b = 0; b < NFR; ++b) {
      s16x4 lo = __builtin_amdgcn_ds_read_tr16_b64_v4i16((lds_s16x4_ptr)(sb + woff[b]));
      s16x4 hi = __builtin_amdgcn_ds_read_tr16_b64_v4i16((lds_s16x4_ptr)(sb + woff[b] + 512));
      wf[b] = __builtin_shufflevector(lo, hi, 0, 1, 2, 3, 4, 5, 6, 7);
    }
#pragma unroll
    for (int a = 0; a < 4; ++a) xf[a] = *(const s16x8*)(sb + xoff + a * 1024);
  };
  auto mma = [&](const s16x8 (&xf)[4], const s16x8 (&wf)[NFR]) {
#pragma unroll
    for (int a = 0; a < 4; ++a)
#pragma unroll
      for (int b = 0; b < NFR; ++b) {
        if (TYPE == 2) acc[a][b] = __builtin_amdgcn_mfma_f32_16x16x32_bf16(xf[a], wf[b], acc[a][b], 0, 0, 0);
        else           acc[a][b] = __builtin_amdgcn_mfma_f32_16x16x32_bf16(wf[b], xf[a], acc[a][b], 0, 0, 0);
      }
  };
#define WAITN(N) asm volatile("s_waitcnt vmcnt(%0)" :: "n"(N) : "memory")
#define K1_WAIT(n) do { if (has3) WAITN(4 * (n)); else WAITN(3 * (n)); } while (0)

  s16x8 xA[4], wAf[NFR], xB[4], wBf[NFR];
#define SGB(m, n) __builtin_amdgcn_sched_group_barrier(m, n, 0)
  auto pattern_full = [&]() {
    SGB(0x10, 1); SGB(0x8, 1); SGB(0x10, 1); SGB(0x8, 1); SGB(0x10, 1); SGB(0x8, 1); SGB(0x10, 1); SGB(0x8, 1);
#pragma unroll
    for (int i = 0; i < 4 + 2 * NFR; ++i) { SGB(0x100, 1); SGB(0x8, 1); }
    SGB(0x8, 4);
  };
  auto pattern_tail = [&]() {
#pragma unroll
    for (int i = 0; i < 4 + 2 * NFR; ++i) { SGB(0x100, 1); SGB(0x8, 1); }
    SGB(0x8, 8);
  };
  STAMP(1, 0);
#pragma unroll
  for (int i = 0; i < K1_NS; ++i) issue(i);
  K1_WAIT(K1_NS - 1);
  BAR;
  STAMP(1, 1);
  readfr(0, xA, wAf);
  SCHED;
#pragma unroll
  for (int kt = 0; kt < 19; ++kt) {
    if (kt + K1_NS <= 20) K1_WAIT(K1_NS - 2); else K1_WAIT(18 - kt);
    WAIT_L0; BAR; SCHED;
    if (kt + K1_NS < 20) issue(kt + K1_NS);
    if (kt & 1) { readfr(kt + 1, xA, wAf); mma(xB, wBf); }
    else        { readfr(kt + 1, xB, wBf); mma(xA, wAf); }
    if (kt + K1_NS < 20) pattern_full(); else pattern_tail();
    SCHED;
  }
  mma(xB, wBf);

  STAMP(1, 2);
  if (TYPE == 0) {
    char* H = ws + OFF_H + ((size_t)(mt * 8 + wm * 4 + (g & 1)) * 54 + j * 6) * 1024 + ((g >> 1) * 16 + c) * 16;
#pragma unroll
    for (int a = 0; a < 4; a += 2) {
#pragma unroll
      for (int b = 0; b < 3; ++b) {
        u32x2 w[2];
#pragma unroll
        for (int h = 0; h < 2; ++h) {
          w[h][0] = cvt2(bfr2(sigm2(bfr2(lo2(acc[a + h][b])))) * bfr2(lo2(acc[a + h][b + 3])));
          w[h][1] = cvt2(bfr2(sigm2(bfr2(hi2(acc[a + h][b])))) * bfr2(hi2(acc[a + h][b + 3])));
        }
        const auto s0 = __builtin_amdgcn_permlane16_swap(w[0][0], w[1][0], false, false);
        const auto s1 = __builtin_amdgcn_permlane16_swap(w[0][1], w[1][1], false, false);
        const int f0 = wn * 48 + b * 16;
        *(u32x4*)(H + (size_t)a * 54 * 1024 + (f0 >> 5) * 1024 + ((f0 >> 4) & 1) * 512) = (u32x4){s0[0], s1[0], s0[1], s1[1]};
      }
    }
  } else if (TYPE == 1) {
    char* PQ = ws + OFF_PQ + ((size_t)(mt * 8 + wm * 4 + (g & 1)) * 20 + j * 10) * 1024 + ((g >> 1) * 16 + c) * 16;
#pragma unroll
    for (int a = 0; a < 4; a += 2) {
      u32x2 w[5][2];
      float mn = 1.0f;
#pragma unroll
      for (int b = 0; b < 5; ++b)
#pragma unroll
        for (int h = 0; h < 2; ++h) { w[b][h][0] = phi2a(lo2(acc[a + h][b]), mn); w[b][h][1] = phi2a(hi2(acc[a + h][b]), mn); }
      if (phi_need_eps(mn)) {
#pragma unroll
        for (int b = 0; b < 5; ++b)
#pragma unroll
          for (int h = 0; h < 2; ++h) { w[b][h][0] = phi2b(w[b][h][0]); w[b][h][1] = phi2b(w[b][h][1]); }
      }
#pragma unroll
      for (int b = 0; b < 5; ++b) {
        const auto s0 = __builtin_amdgcn_permlane16_swap(w[b][0][0], w[b][1][0], false, false);
        const auto s1 = __builtin_amdgcn_permlane16_swap(w[b][0][1], w[b][1][1], false, false);
        const int f0 = wn * 80 + b * 16;
        *(u32x4*)(PQ + (size_t)a * 20 * 1024 + (f0 >> 5) * 1024 + ((f0 >> 4) & 1) * 512) = (u32x4){s0[0], s1[0], s0[1], s1[1]};
      }
    }
  } else {
    static_assert(K1_NS == 4 && 332 * 272 <= 3 * K1_STAGE, "T must not overlap the slot of the last stage");
    char* tw = smem + c * 272 + (wm * 64 + 4 * g) * 2;
    if (tid < 16) *(u32x4*)(smem + 320 * 272 + tid * 16) = (u32x4){0x3F803F80u, 0x3F803F80u, 0x3F803F80u, 0x3F803F80u};
    auto kvepi = [&](auto NK_) {
      constexpr int NK = decltype(NK_)::value;
      u32x2 w[4][NK];
      float mn = 1.0f;
#pragma unroll
      for (int a = 0; a < 4; ++a)
#pragma unroll
        for (int b = 0; b < NK; ++b) w[a][b] = (u32x2){phi2a(lo2(acc[a][b]), mn), phi2a(hi2(acc[a][b]), mn)};
#pragma unroll
      for (int a = 0; a < 4; ++a)
#pragma unroll
        for (int b = NK; b < 5; ++b)
          *(u32x2*)(tw + kvnf(wn, b) * 16 * 272 + a * 32) = (u32x2){cvt2(lo2(acc[a][b])), cvt2(hi2(acc[a][b]))};
      if (phi_need_eps(mn)) {
#pragma unroll
        for (int a = 0; a < 4; ++a)
#pragma unroll
          for (int b = 0; b < NK; ++b) w[a][b] = (u32x2){phi2b(w[a][b][0]), phi2b(w[a][b][1])};
      }
#pragma unroll
      for (int a = 0; a < 4; ++a)
#pragma unroll
        for (int b = 0; b < NK; ++b) *(u32x2*)(tw + kvnf(wn, b) * 16 * 272 + a * 32) = w[a][b];
    };
    if (wn < 2) kvepi(std::integral_constant<int, 3>{}); else kvepi(std::integral_constant<int, 2>{});
    STAMP(1, 4);
    __syncthreads();
    STAMP(1, 5);
    const int r32 = lane & 31, hh = lane >> 5;
    f32x16 akv;
#pragma unroll
    for (int i = 0; i < 16; ++i) akv[i] = 0.f;
    const char* ap = smem + (20 * wave + r32) * 272 + 16 * hh;
    const char* bp = smem + (r32 == 20 ? 320 : 160 + 20 * wave + r32) * 272 + 16 * hh;
#pragma unroll
    for (int s = 0; s < 8; ++s)
      akv = __builtin_amdgcn_mfma_f32_32x32x16_bf16(*(const s16x8*)(ap + 32 * s), *(const s16x8*)(bp + 32 * s), akv, 0, 0, 0);
    STAMP(1, 6);
    const int head = j * 8 + wave;
    float* P = (float*)(ws + OFF_KVP) + (size_t)(mt * 32 + head) * 504;
    if (r32 <= 20) {
#pragma unroll
      for (int q = 0; q < 3; ++q) {
        const int d0 = 8 * q + 4 * hh;
        if (d0 < 20) {
          *(f32x4*)(P + r32 * 24 + d0) = (f32x4){akv[4 * q], akv[4 * q + 1], akv[4 * q + 2], akv[4 * q + 3]};
        }
      }
    }
  }
}

__global__ __launch_bounds__(512, 2) void k1_gemm(const u16* __restrict__ wqkv, const u16* __restrict__ wg,
                                                   const u16* __restrict__ wu, const u16* __restrict__ wo,
                                                   const u16* __restrict__ wdown, char* __restrict__ ws) {
  extern __shared__ __attribute__((aligned(16))) char smem[];
  const int bid = blockIdx.x;
  if (bid >= 240) {
    retile_all((bid - 240) * 512 + threadIdx.x, wo, wdown, ws);
    return;
  }
  const int p = (bid & 7) * 30 + (bid >> 3);
  const int mh = p / 120, rem = p % 120, nt = rem >> 3, mt = mh * 8 + (rem & 7);
  if (nt < 4) k1_body<2>(wqkv, wqkv, ws, mt, nt, smem);
  else if (nt < 6) k1_body<1>(wqkv, wqkv, ws, mt, nt - 4, smem);
  else k1_body<0>(wg, wu, ws, mt, nt - 6, smem);
  STAMP(1, 3);
}

constexpr int K2_LDS = 163840;
#define K2DD 3
constexpr int K2_D = K2DD;
#define K2DW 3
constexpr int K2_DW = K2DW;

__global__ __launch_bounds__(512, 2) void k2_gemm(const u16* __restrict__ x, const char* __restrict__ ws, u16* __restrict__ out) {
  extern __shared__ __attribute__((aligned(16))) char smem[];
  const int tid = threadIdx.x, lane = tid & 63;
  const int wave = __builtin_amdgcn_readfirstlane(tid >> 6);
  const int wq = wave & 3;
  const int g = lane >> 4, c = lane & 15;
  const int bid = blockIdx.x;
  const int mt = (bid & 7) * 4 + (bid >> 6), nt = (bid >> 3) & 7;

  STAMP(2, 0);
  f32x4 acc[4][5];
#pragma unroll
  for (int a = 0; a < 4; ++a)
#pragma unroll
    for (int b = 0; b < 5; ++b) acc[a][b] = (f32x4){0.f, 0.f, 0.f, 0.f};
  const int b0 = wave < 4 ? 0 : 3, nb = wave < 4 ? 3 : 2;
  const int tok = mt * 64 + wq * 16 + c;

  if (wave < 4) {
    const char* hsrc = ws + OFF_H + ((size_t)(mt * 4) * 54 + wq) * 1024 + lane * 16;
    const char* wsrc = ws + OFF_WC + ((size_t)nt * 54 + wq) * 5120 + lane * 16;
    s16x8 xr[K2_D][4], wr[K2_D][5];
    auto ld = [&](int slot, int i) {
#pragma unroll
      for (int b = 0; b < 5; ++b) wr[slot][b] = *(const s16x8*)(wsrc + (size_t)(20 * i + b) * 1024);
#pragma unroll
      for (int a = 0; a < 4; ++a) xr[slot][a] = *(const s16x8*)(hsrc + (size_t)(a * 54 + 4 * i) * 1024);
    };
    auto mma1 = [&](int slot) {
#pragma unroll
      for (int a = 0; a < 4; ++a)
#pragma unroll
        for (int b = 0; b < 5; ++b) acc[a][b] = __builtin_amdgcn_mfma_f32_16x16x32_bf16(wr[slot][b], xr[slot][a], acc[a][b], 0, 0, 0);
    };
#pragma unroll
    for (int i = 0; i < K2_D; ++i) ld(i, i);
    SCHED;
#pragma unroll
    for (int i = 0; i < 13; ++i) {
      mma1(i % K2_D);
      if (i + K2_D < 13) ld(i % K2_D, i + K2_D);
      else if (i + K2_D == 13) { if (wave < 2) ld(i % K2_D, 13); }
      SCHED;
    }
    if (wave < 2) mma1(13 % K2_D);
    STAMP(2, 1);
  } else {
    __builtin_amdgcn_s_setprio(3);
    char* reg = smem + wq * 36864;
    if (bid < 32) {
      const int t = tid - 256, u = t >> 2, q = t & 3, uu = u < 63 ? u : 62, e = uu / 3, g3 = uu - e * 3;
      const float* src = (const float*)(ws + OFF_KVP) + ((size_t)(4 * q) * 32 + bid) * 504 + e * 24 + 8 * g3;
      f32x4 lo[4], hi[4];
#pragma unroll
      for (int i = 0; i < 4; ++i) { lo[i] = *(const f32x4*)(src + i * 32 * 504); hi[i] = *(const f32x4*)(src + i * 32 * 504 + (g3 < 2 ? 4 : 0)); }
      f32x4 s0 = (lo[0] + lo[1]) + (lo[2] + lo[3]), s1 = (hi[0] + hi[1]) + (hi[2] + hi[3]);
#pragma unroll
      for (int o = 1; o <= 2; o <<= 1)
#pragma unroll
        for (int r = 0; r < 4; ++r) { s0[r] += __shfl_xor(s0[r], o); s1[r] += __shfl_xor(s1[r], o); }
      u32x4 w;
      w[0] = pack2(s0[0], s0[1]); w[1] = pack2(s0[2], s0[3]);
      w[2] = g3 < 2 ? pack2(s1[0], s1[1]) : 0u; w[3] = g3 < 2 ? pack2(s1[2], s1[3]) : 0u;
#pragma unroll
      for (int r = 0; r < 4; ++r) w[r] = w[r] == KV_EMPTY ? 0x7FC07FC0u : w[r];
      const int slot = e < 16 ? g3 * 16 + e : 48 + (e - 16) * 3 + g3;
      if (q == 0 && u < 63)
        asm volatile("global_store_dwordx4 %0, %1, off sc1\n\ts_nop 1" :: "v"(ws + OFF_KVF + ((size_t)bid * 64 + slot) * 16), "v"(w) : "memory");
    }
    {
      const char* pqg = ws + OFF_PQ + ((size_t)(mt * 4) * 32 + wq * 8) * 640 + lane * 16;
#pragma unroll
      for (int tg = 0; tg < 4; ++tg)
#pragma unroll
        for (int i = 0; i < 5; ++i)
          __builtin_amdgcn_global_load_lds((const unsigned*)(pqg + tg * 20480 + i * 1024), (unsigned*)(reg + tg * 5120 + i * 1024), 16, 0, 0);
    }
    const char* wosrc = ws + OFF_WO + (size_t)(nt * 4 + wq) * 25600 + lane * 16;
    s16x8 w2[K2_DW][5];
    auto ldW = [&](int s) {
#pragma unroll
      for (int b = 0; b < 5; ++b) w2[s % K2_DW][b] = *(const s16x8*)(wosrc + (s * 5 + b) * 1024);
    };
#pragma unroll
    for (int s = 0; s < K2_DW; ++s) ldW(s);
    SCHED;
    const char* kvl = reg + 20480 + lane * 16;
    const char* kg = ws + OFF_KVF + (size_t)(wq * 8) * 1024;
    const unsigned kvm0 = __builtin_amdgcn_readfirstlane((unsigned)(size_t)(__attribute__((address_space(3))) char*)(reg + 20480));
    auto kvload = [&](int i0_, int i1_) {
#pragma unroll
      for (int i = 0; i < 8; ++i)
        if (i >= i0_ && i < i1_)
          asm volatile("s_mov_b32 m0, %0\n\ts_nop 0\n\tglobal_load_lds_dwordx4 %1, %2 sc1" :: "s"(kvm0 + i * 1024), "v"(lane * 16), "s"(kg + i * 1024) : "memory");
    };
    auto kvload_l2 = [&](int i0_, int i1_) {
#pragma unroll
      for (int i = 0; i < 8; ++i)
        if (i >= i0_ && i < i1_)
          asm volatile("s_mov_b32 m0, %0\n\ts_nop 0\n\tglobal_load_lds_dwordx4 %1, %2" :: "s"(kvm0 + i * 1024), "v"(lane * 16), "s"(kg + i * 1024) : "memory");
    };
    auto kvwait = [&](int i0_, int i1_) {
      while (true) {
        asm volatile("s_waitcnt vmcnt(0)" ::: "memory");
        bool bad = false;
#pragma unroll
        for (int i = 0; i < 8; ++i)
          if (i >= i0_ && i < i1_) {
            const u32x4 v = *(const __attribute__((address_space(3))) u32x4*)(kvl + i * 1024);
            bad = bad || v[0] == KV_EMPTY || v[1] == KV_EMPTY || v[2] == KV_EMPTY || v[3] == KV_EMPTY;
            if ((i & 3) == 3) __builtin_amdgcn_sched_barrier(0);
          }
        if (__builtin_amdgcn_ballot_w64(bad) == 0ull) break;
        __builtin_amdgcn_s_sleep(2);
        kvload(i0_, i1_);
      }
    };
    kvload(0, 2);
    kvwait(0, 2);
    __builtin_amdgcn_s_waitcnt(0x0F70);
    kvload_l2(2, 8);
    SCHED;
    STAMPW(4);
    const char* pql = reg + (g < 3 ? g : 0) * 256 + c * 16;
    const char* kv0 = reg + 20480 + (g < 3 ? lane : 63) * 16;
    const char* kv1 = reg + 20480 + ((g < 3 && (c & 3) < 2) ? ((c & 3) == 0 ? 48 + (c >> 2) * 3 + g : 60 + g) : 63) * 16;
    const f32x4 z4 = (f32x4){0.f, 0.f, 0.f, 0.f};
    unsigned ow[4][20];
    auto mma2 = [&](int s) {
#pragma unroll
      for (int a = 0; a < 4; ++a) {
        const u32x4 o4 = (u32x4){ow[a][4 * s], ow[a][4 * s + 1], ow[a][4 * s + 2], ow[a][4 * s + 3]};
        const s16x8 of = __builtin_bit_cast(s16x8, o4);
#pragma unroll
        for (int b = 0; b < 5; ++b) acc[a][b] = __builtin_amdgcn_mfma_f32_16x16x32_bf16(w2[s % K2_DW][b], of, acc[a][b], 0, 0, 0);
      }
      if (s + K2_DW < 5) ldW(s + K2_DW);
    };
#pragma unroll
    for (int m = 0; m < 4; ++m) {
      if (m == 1) kvwait(2, 8);
      f32x4 ov[2][4]; float oe[2][4];
#pragma unroll
      for (int h2 = 0; h2 < 2; ++h2) {
        const int hh = 2 * m + h2;
        const s16x8 k0 = *(const s16x8*)(kv0 + hh * 1024), k1 = *(const s16x8*)(kv1 + hh * 1024);
#pragma unroll
        for (int tg = 0; tg < 4; ++tg) {
          u32x4 pq;
          if ((hh & 1) == 0) pq = *(const u32x4*)(pql + tg * 5120 + (5 * hh / 2) * 256);
          else {
            const u32x2 p0 = *(const u32x2*)(pql + tg * 5120 + ((5 * hh - 1) / 2) * 256 + 8), p1 = *(const u32x2*)(pql + tg * 5120 + ((5 * hh - 1) / 2) * 256 + 256);
            pq = (u32x4){p0[0], p0[1], p1[0], p1[1]};
          }
          u32x4 pk4;
          pk4[0] = g < 3 ? pq[0] : 0u; pk4[1] = g < 3 ? pq[1] : 0u;
          pk4[2] = g < 2 ? pq[2] : 0u; pk4[3] = g < 2 ? pq[3] : 0u;
          const s16x8 pf = __builtin_bit_cast(s16x8, pk4);
          const f32x4 d1 = __builtin_amdgcn_mfma_f32_16x16x32_bf16(k1, pf, z4, 0, 0, 0);
          const f32x4 d0 = __builtin_amdgcn_mfma_f32_16x16x32_bf16(k0, pf, z4, 0, 0, 0);
          const float rden = frcp(bfr(bfr(d1[1]) + 1e-6f));
          const f32x2 lo = bfr2(lo2(d0)) * rden, hi = bfr2(hi2(d0)) * rden;
          ov[h2][tg] = (f32x4){lo[0], lo[1], hi[0], hi[1]};
          oe[h2][tg] = bfr(d1[0]) * rden;
        }
      }
#pragma unroll
      for (int tg = 0; tg < 4; ++tg) {
        ow[tg][5 * m + 0] = cvt2(lo2(ov[0][tg]));
        ow[tg][5 * m + 1] = cvt2(hi2(ov[0][tg]));
        ow[tg][5 * m + 2] = cvt2((f32x2){oe[0][tg], ov[1][tg][0]});
        ow[tg][5 * m + 3] = cvt2((f32x2){ov[1][tg][1], ov[1][tg][2]});
        ow[tg][5 * m + 4] = cvt2((f32x2){ov[1][tg][3], oe[1][tg]});
      }
      mma2(m);
      if (m == 3) mma2(4);
      SCHED;
      if (m < 2) STAMPW(5 + m);
    }
  }
  u32x2 xw[3];
#pragma unroll
  for (int i = 0; i < 3; ++i) xw[i] = *(const u32x2*)(x + (size_t)tok * D_ + nt * 80 + (i < nb ? b0 + i : b0) * 16 + 4 * g);
  __syncthreads();

  f32x4* red = (f32x4*)smem;
#pragma unroll
  for (int a = 0; a < 4; ++a)
#pragma unroll
    for (int b = 0; b < 5; ++b) red[(wave * 20 + a * 5 + b) * 64 + lane] = acc[a][b];
  __syncthreads();
#pragma unroll
  for (int i = 0; i < 3; ++i) {
    if (i < nb) {
      const int f = wq * 5 + b0 + i;
      f32x4 y1 = red[(0 * 20 + f) * 64 + lane], y2 = red[(4 * 20 + f) * 64 + lane];
#pragma unroll
      for (int sw = 1; sw < 4; ++sw) { y1 += red[(sw * 20 + f) * 64 + lane]; y2 += red[((4 + sw) * 20 + f) * 64 + lane]; }
      const int n = nt * 80 + (b0 + i) * 16 + 4 * g;
      u32x2 w;
      {
        const f32x2 y = bfr2(lo2(y1)), gs = bfr2(y * sigm2(y)), sv = bfr2(lo2(y2));
        w[0] = cvt2(unp2(xw[i][0]) + bfr2(sv * gs));
      }
      {
        const f32x2 y = bfr2(hi2(y1)), gs = bfr2(y * sigm2(y)), sv = bfr2(hi2(y2));
        w[1] = cvt2(unp2(xw[i][1]) + bfr2(sv * gs));
      }
      *(u32x2*)(out + (size_t)tok * D_ + n) = w;
    }
  }
  STAMP(2, 3);
}

extern "C" void kernel_launch(void* const* d_in, const int* in_sizes, int n_in,
                              void* d_out, int out_size, void* d_ws, size_t ws_size,
                              hipStream_t stream) {
  const u16* x = (const u16*)d_in[0];
  const u16* wqkv = (const u16*)d_in[1];
  const u16* wo = (const u16*)d_in[2];
  const u16* wg = (const u16*)d_in[4];
  const u16* wu = (const u16*)d_in[5];
  const u16* wd = (const u16*)d_in[6];
  char* ws = (char*)d_ws;
  u16* out = (u16*)d_out;

  hipFuncSetAttribute((const void*)k1_gemm, hipFuncAttributeMaxDynamicSharedMemorySize, K1_NS * K1_STAGE);
  hipFuncSetAttribute((const void*)k2_gemm, hipFuncAttributeMaxDynamicSharedMemorySize, K2_LDS);

  k0_prep<<<512, 256, 0, stream>>>(x, ws, wqkv, wg, wu);
  k1_gemm<<<256, 512, K1_NS * K1_STAGE, stream>>>(wqkv, wg, wu, wo, wd, ws);
  k2_gemm<<<256, 512, K2_LDS, stream>>>(x, ws, out);
}
```

```cpp
#include <hip/hip_runtime.h>
#include <hip/hip_bf16.h>
#include <stdint.h>
#include <type_traits>

typedef unsigned short u16;
typedef __attribute__((ext_vector_type(8))) short s16x8;
typedef __attribute__((ext_vector_type(4))) short s16x4;
typedef __attribute__((ext_vector_type(4))) float f32x4;
typedef __attribute__((ext_vector_type(16))) float f32x16;
typedef __attribute__((ext_vector_type(2))) unsigned u32x2;
typedef __attribute__((ext_vector_type(4))) unsigned u32x4;
typedef u32x4 u32x4_a8 __attribute__((aligned(8)));
typedef __attribute__((address_space(3))) s16x4* lds_s16x4_ptr;
typedef __attribute__((address_space(1))) unsigned gu32;
typedef __attribute__((address_space(1))) unsigned long long gu64;

#define RLX_AGENT __ATOMIC_RELAXED, __HIP_MEMORY_SCOPE_AGENT

constexpr int S_ = 2048, D_ = 640, F_ = 1728, NQKV_ = 1920;

constexpr size_t OFF_XN    = 8192;
constexpr size_t SZ_XN     = (size_t)S_ * D_ * 2;
constexpr size_t OFF_H     = OFF_XN + SZ_XN;
constexpr size_t SZ_H      = (size_t)S_ * F_ * 2;
constexpr size_t OFF_PQ    = OFF_H + SZ_H;
constexpr size_t SZ_PQ     = (size_t)S_ * D_ * 2;
constexpr size_t OFF_WC    = OFF_PQ + SZ_PQ;
constexpr size_t SZ_WC     = (size_t)8 * 54 * 5 * 1024;
constexpr size_t OFF_WO    = OFF_WC + SZ_WC;
constexpr size_t SZ_WO     = (size_t)8 * 4 * 5 * 5 * 1024;
constexpr size_t OFF_KVP   = OFF_WO + SZ_WO;
constexpr size_t SZ_KVP    = (size_t)16 * 32 * 504 * 4;
constexpr size_t OFF_KVF   = OFF_KVP + SZ_KVP;
constexpr size_t SZ_KVF    = 32768;

#define KVAUX 16
constexpr unsigned KV_EMPTY = 0xFFFFFFFFu;
__device__ __forceinline__ float bf2f(u16 v) { return __uint_as_float(((unsigned)v) << 16); }
__device__ __forceinline__ u16 f2bf(float f) { return __builtin_bit_cast(u16, (__bf16)f); }
__device__ __forceinline__ float bfr(float f) { return bf2f(f2bf(f)); }
__device__ __forceinline__ unsigned pack2(float a, float b) { return (unsigned)f2bf(a) | ((unsigned)f2bf(b) << 16); }
typedef __attribute__((ext_vector_type(2))) float f32x2;
typedef __attribute__((ext_vector_type(2))) __bf16 bf16x2;
__device__ __forceinline__ unsigned cvt2(f32x2 v) { return __builtin_bit_cast(unsigned, __builtin_convertvector(v, bf16x2)); }
__device__ __forceinline__ f32x2 unp2(unsigned u) { return (f32x2){__uint_as_float(u << 16), __uint_as_float(u & 0xffff0000u)}; }
__device__ __forceinline__ f32x2 bfr2(f32x2 v) { return unp2(cvt2(v)); }
__device__ __forceinline__ f32x2 lo2(f32x4 v) { return __builtin_shufflevector(v, v, 0, 1); }
__device__ __forceinline__ f32x2 hi2(f32x4 v) { return __builtin_shufflevector(v, v, 2, 3); }
__device__ __forceinline__ unsigned phi2a(f32x2 a, float& mn) {
  const f32x2 t = bfr2(a), m = t * 1.44269504088896340736f, p = t + 1.0f;
  f32x2 sel;
  sel[0] = __builtin_amdgcn_fmed3f(__builtin_amdgcn_exp2f(m[0]), p[0], 1.0f);
  sel[1] = __builtin_amdgcn_fmed3f(__builtin_amdgcn_exp2f(m[1]), p[1], 1.0f);
  mn = __builtin_fminf(__builtin_fminf(mn, sel[0]), sel[1]);
  return cvt2(sel);
}
__device__ __forceinline__ bool phi_need_eps(float mn) { return __builtin_amdgcn_ballot_w64(!(mn >= 0.00048828125f)) != 0ull; }
__device__ __forceinline__ unsigned phi2b(unsigned w) { return cvt2(unp2(w) + 1e-6f); }
__device__ __forceinline__ f32x2 sigm2(f32x2 g) {
  const f32x2 m = g * -1.44269504088896340736f;
  f32x2 e; e[0] = __builtin_amdgcn_exp2f(m[0]); e[1] = __builtin_amdgcn_exp2f(m[1]);
  e = e + 1.0f;
  f32x2 r; r[0] = __builtin_amdgcn_rcpf(e[0]); r[1] = __builtin_amdgcn_rcpf(e[1]);
  return r;
}
__device__ __forceinline__ int aswz(int row) { int x = (row >> 2) & 3; return (((x ^ (x >> 1)) & 1) << 1) | (x >> 1); }

#define WAIT_V(n) asm volatile("s_waitcnt vmcnt(" #n ")" ::: "memory")
#define STAMP(kid, idx)
#define STAMPW(idx)
#define WAIT_L0 asm volatile("s_waitcnt lgkmcnt(0)" ::: "memory")
#define BAR __builtin_amdgcn_s_barrier()
#define SCHED __builtin_amdgcn_sched_barrier(0)
__device__ __forceinline__ float frcp(float v) { return __builtin_amdgcn_rcpf(v); }

constexpr int RT_WD = 216 * 80, RT_ITEMS = RT_WD + 80 * 80, RT_R = 3;
__device__ __forceinline__ void retile_all(int t0, const u16* __restrict__ wo, const u16* __restrict__ wdown, char* __restrict__ ws) {
  u32x4 r[RT_R][8];
  char* dst[RT_R];
#pragma unroll
  for (int it = 0; it < RT_R; ++it) {
    int t = t0 + it * 8192; if (t >= RT_ITEMS) t = t0;
    const bool isd = t < RT_WD;
    const int t2 = isd ? t : t - RT_WD;
    const int ko = t2 / 80, noct = t2 - ko * 80;
    const int n0 = noct * 8, nt = n0 / 80, nn = n0 - nt * 80, nf = nn >> 4, c0 = nn & 15, gg = ko & 3, kq = ko >> 2;
    const int wq = kq / 5, ks = kq - wq * 5;
    const u16* src = (isd ? wdown : wo) + n0;
#pragma unroll
    for (int jj = 0; jj < 8; ++jj) {
      const int q = 8 * ks + jj, hh = q / 5, rr = q - hh * 5;
      const int row = isd ? ko * 8 + jj : (wq * 8 + hh) * 20 + (rr < 4 ? 4 * gg + rr : 16 + gg);
      r[it][jj] = *(const u32x4*)(src + (size_t)row * D_);
    }
    dst[it] = ws + (isd ? OFF_WC + (size_t)(nt * 54 + kq) * 5120 : OFF_WO + (size_t)(nt * 20 + kq) * 5120) + (size_t)(nf * 64 + gg * 16 + c0) * 16;
  }
#pragma unroll
  for (int it = 0; it < RT_R; ++it) {
#pragma unroll
    for (int i = 0; i < 4; ++i) {
      u32x4 lo, hi;
#pragma unroll
      for (int q = 0; q < 4; ++q) {
        lo[q] = (r[it][2 * q][i] & 0xffffu) | (r[it][2 * q + 1][i] << 16);
        hi[q] = (r[it][2 * q][i] >> 16) | (r[it][2 * q + 1][i] & 0xffff0000u);
      }
      *(u32x4*)(dst[it] + (2 * i) * 16) = lo;
      *(u32x4*)(dst[it] + (2 * i + 1) * 16) = hi;
    }
  }
}

__global__ __launch_bounds__(256) void k0_prep(const u16* __restrict__ x, char* __restrict__ ws, const u16* __restrict__ wqkv,
                                               const u16* __restrict__ wg, const u16* __restrict__ wu, const u16* __restrict__ outp) {
  const int tid = threadIdx.x;
  if (blockIdx.x >= 256) {
    const int t = (blockIdx.x - 256) * 256 + tid;
    unsigned v0, v1;
    const int l1 = t + 65536 < 107520 ? t + 65536 : t;
    const char* p0 = t < 38400 ? (const char*)wqkv + (size_t)t * 64 : (const char*)wg + (size_t)(t - 38400) * 64;
    const char* p1 = l1 < 38400 ? (const char*)wqkv + (size_t)l1 * 64 : (l1 < 72960 ? (const char*)wg + (size_t)(l1 - 38400) * 64 : (const char*)wu + (size_t)(l1 - 72960) * 64);
    constexpr int NWS = (int)((OFF_KVF + SZ_KVF + 65535) / 65536), NOUT = S_ * D_ * 2 / 65536;
    const int ti = ((blockIdx.x - 256) >> 3) * 256 + tid;
    const char* p2 = ti < NWS ? ws + (size_t)ti * 65536 : (ti < NWS + NOUT ? (const char*)outp + (size_t)(ti - NWS) * 65536 : p0);
    unsigned v2;
    asm volatile("global_load_dword %0, %3, off\n\tglobal_load_dword %1, %4, off\n\tglobal_load_dword %2, %5, off\n\ts_waitcnt vmcnt(0)" : "=&v"(v0), "=&v"(v1), "=&v"(v2) : "v"(p0), "v"(p1), "v"(p2) : "memory");
    return;
  }
  {
    if (blockIdx.x < 8) {
      const int ci = blockIdx.x * 256 + tid;
      const unsigned v = (ci & 63) != 63 ? KV_EMPTY : 0u;
      *(u32x4*)(ws + OFF_KVF + (size_t)ci * 16) = (u32x4){v, v, v, v};
    }
    const int row = blockIdx.x * 8 + (tid >> 5);
    const int l32 = tid & 31;
    const u16* xr = x + (size_t)row * D_;
    float v[20];
    float sum = 0.f;
#pragma unroll
    for (int i = 0; i < 5; ++i) {
      u32x2 w = *(const u32x2*)(xr + (l32 + 32 * i) * 4);
      v[4 * i + 0] = __uint_as_float(w[0] << 16);
      v[4 * i + 1] = __uint_as_float(w[0] & 0xffff0000u);
      v[4 * i + 2] = __uint_as_float(w[1] << 16);
      v[4 * i + 3] = __uint_as_float(w[1] & 0xffff0000u);
      sum += v[4 * i] + v[4 * i + 1] + v[4 * i + 2] + v[4 * i + 3];
    }
#pragma unroll
    for (int o = 16; o >= 1; o >>= 1) sum += __shfl_xor(sum, o);
    const float mean = sum * (1.0f / D_);
    float sq = 0.f;
#pragma unroll
    for (int i = 0; i < 20; ++i) { float d = v[i] - mean; sq += d * d; }
#pragma unroll
    for (int o = 16; o >= 1; o >>= 1) sq += __shfl_xor(sq, o);
    const float mb = bfr(mean);
    const float vb = bfr(sq * (1.0f / D_));
    const float sd = bfr(sqrtf(bfr(vb + 1e-5f)));
    const float rsd = 1.0f / sd;
    u16* xn = (u16*)(ws + OFF_XN);
    const int mt = row >> 7, r = row & 127, sw = aswz(r);
#pragma unroll
    for (int i = 0; i < 5; ++i) {
      const int k = (l32 + 32 * i) * 4;
      float o0 = bfr(v[4 * i + 0] - mb) * rsd, o1 = bfr(v[4 * i + 1] - mb) * rsd;
      float o2 = bfr(v[4 * i + 2] - mb) * rsd, o3 = bfr(v[4 * i + 3] - mb) * rsd;
      u32x2 w; w[0] = pack2(o0, o1); w[1] = pack2(o2, o3);
      const int kt = k >> 5, ch = (k >> 3) & 3, hf = (k >> 2) & 1;
      size_t off = ((size_t)(mt * 20 + kt) * 128 + r) * 32 + ((ch ^ sw) << 3) + (hf << 2);
      *(u32x2*)(xn + off) = w;
    }
  }
  STAMP(0, 3);
}

constexpr int K1_STAGE = 32768;
#define K1NS 4
constexpr int K1_NS = K1NS;

__device__ __forceinline__ int kvnf(int wn, int i) {
  return wn < 2 ? (i < 3 ? 3 * wn + i : 10 + 2 * wn + (i - 3)) : (i < 2 ? 2 + 2 * wn + i : 8 + 3 * wn + (i - 2));
}
template <int TYPE>
__device__ __forceinline__ void k1_body(const u16* __restrict__ wA, const u16* __restrict__ wB, char* __restrict__ ws,
                                        int mt, int j, char* smem) {
  constexpr int NCB = TYPE == 0 ? 6 : 5;
  constexpr int NFR = TYPE == 0 ? 6 : 5;
  constexpr int LDW = TYPE == 0 ? F_ : NQKV_;
  const int tid = threadIdx.x, lane = tid & 63;
  const int wave = __builtin_amdgcn_readfirstlane(tid >> 6);
  const int wm = wave >> 2, wn = (TYPE == 2 && wave >= 4) ? ((wave + 2) & 3) : (wave & 3);
  const int g = lane >> 4, c = lane & 15;

  const char* asrc = ws + OFF_XN + (size_t)mt * 20 * 8192 + wave * 1024 + lane * 16;
  const u16* bsrc[3];
#pragma unroll
  for (int t = 0; t < 3; ++t) {
    int ii = wave + 8 * t; if (ii >= NCB * 4) ii = wave;
    const int cb = ii >> 2, krow = (ii & 3) * 8 + (lane >> 3), u = lane & 7;
    const int sk = ((krow >> 1) & 1) | (((krow >> 3) & 1) << 1);
    const int ch = (u >> 1) ^ sk;
    const int lc = cb * 64 + ch * 16 + (u & 1) * 8;
    const u16* p;
    if (TYPE == 0) p = (lc < 192) ? (wA + j * 192 + lc) : (wB + j * 192 + (lc - 192));
    else if (TYPE == 1) p = wA + j * 320 + lc;
    else p = (lc < 160) ? (wA + 640 + j * 160 + lc) : (wA + 1280 + j * 160 + (lc - 160));
    bsrc[t] = p + (size_t)krow * LDW;
  }
  const bool has3 = (NCB == 6) || (wave < 4);

  auto issue = [&](int kt) {
    char* sb = smem + (kt % K1_NS) * K1_STAGE;
    __builtin_amdgcn_global_load_lds((const unsigned*)(asrc + (size_t)kt * 8192), (unsigned*)(sb + wave * 1024), 16, 0, 0);
    __builtin_amdgcn_global_load_lds((const unsigned*)(bsrc[0] + (size_t)kt * 32 * LDW), (unsigned*)(sb + 8192 + wave * 1024), 16, 0, 0);
    __builtin_amdgcn_global_load_lds((const unsigned*)(bsrc[1] + (size_t)kt * 32 * LDW), (unsigned*)(sb + 8192 + (wave + 8) * 1024), 16, 0, 0);
    if (has3) __builtin_amdgcn_global_load_lds((const unsigned*)(bsrc[2] + (size_t)kt * 32 * LDW), (unsigned*)(sb + 8192 + (wave + 16) * 1024), 16, 0, 0);
  };

  const int xoff = (wm * 64 + c) * 64 + ((g ^ aswz(c)) << 4);
  int woff[NFR];
  {
    const int q = (lane & 15) >> 2, p = lane & 3;
    const int s = (q >> 1) | ((g & 1) << 1);
    const int lo = (8 * g + q) * 128 + (s << 5) + p * 8;
#pragma unroll
    for (int i = 0; i < NFR; ++i) {
      const int nf = TYPE == 0 ? (i < 3 ? 3 * wn + i : 12 + 3 * wn + (i - 3)) : (TYPE == 1 ? 5 * wn + i : kvnf(wn, i));
      woff[i] = 8192 + (nf >> 2) * 4096 + (lo ^ ((nf & 3) << 5));
    }
  }

  f32x4 acc[4][NFR];
#pragma unroll
  for (int a = 0; a < 4; ++a)
#pragma unroll
    for (int b = 0; b < NFR; ++b) acc[a][b] = (f32x4){0.f, 0.f, 0.f, 0.f};

  auto readfr = [&](int kt, s16x8 (&xf)[4], s16x8 (&wf)[NFR]) {
    const char* sb = smem + (kt % K1_NS) * K1_STAGE;
#pragma unroll
    for (int b = 0; b < NFR; ++b) {
      s16x4 lo = __builtin_amdgcn_ds_read_tr16_b64_v4i16((lds_s16x4_ptr)(sb + woff[b]));
      s16x4 hi = __builtin_amdgcn_ds_read_tr16_b64_v4i16((lds_s16x4_ptr)(sb + woff[b] + 512));
      wf[b] = __builtin_shufflevector(lo, hi, 0, 1, 2, 3, 4, 5, 6, 7);
    }
#pragma unroll
    for (int a = 0; a < 4; ++a) xf[a] = *(const s16x8*)(sb + xoff + a * 1024);
  };
  auto mma = [&](const s16x8 (&xf)[4], const s16x8 (&wf)[NFR]) {
#pragma unroll
    for (int a = 0; a < 4; ++a)
#pragma unroll
      for (int b = 0; b < NFR; ++b) {
        if (TYPE == 2) acc[a][b] = __builtin_amdgcn_mfma_f32_16x16x32_bf16(xf[a], wf[b], acc[a][b], 0, 0, 0);
        else           acc[a][b] = __builtin_amdgcn_mfma_f32_16x16x32_bf16(wf[b], xf[a], acc[a][b], 0, 0, 0);
      }
  };
#define WAITN(N) asm volatile("s_waitcnt vmcnt(%0)" :: "n"(N) : "memory")
#define K1_WAIT(n) do { if (has3) WAITN(4 * (n)); else WAITN(3 * (n)); } while (0)

  s16x8 xA[4], wAf[NFR], xB[4], wBf[NFR];
#define SGB(m, n) __builtin_amdgcn_sched_group_barrier(m, n, 0)
  auto pattern_full = [&]() {
    SGB(0x10, 1); SGB(0x8, 1); SGB(0x10, 1); SGB(0x8, 1); SGB(0x10, 1); SGB(0x8, 1); SGB(0x10, 1); SGB(0x8, 1);
#pragma unroll
    for (int i = 0; i < 4 + 2 * NFR; ++i) { SGB(0x100, 1); SGB(0x8, 1); }
    SGB(0x8, 4);
  };
  auto pattern_tail = [&]() {
#pragma unroll
    for (int i = 0; i < 4 + 2 * NFR; ++i) { SGB(0x100, 1); SGB(0x8, 1); }
    SGB(0x8, 8);
  };
  STAMP(1, 0);
#pragma unroll
  for (int i = 0; i < K1_NS; ++i) issue(i);
  K1_WAIT(K1_NS - 1);
  BAR;
  STAMP(1, 1);
  readfr(0, xA, wAf);
  SCHED;
#pragma unroll
  for (int kt = 0; kt < 19; ++kt) {
    if (kt + K1_NS <= 20) K1_WAIT(K1_NS - 2); else K1_WAIT(18 - kt);
    WAIT_L0; BAR; SCHED;
    if (kt + K1_NS < 20) issue(kt + K1_NS);
    if (kt & 1) { readfr(kt + 1, xA, wAf); mma(xB, wBf); }
    else        { readfr(kt + 1, xB, wBf); mma(xA, wAf); }
    if (kt + K1_NS < 20) pattern_full(); else pattern_tail();
    SCHED;
  }
  mma(xB, wBf);

  STAMP(1, 2);
  if (TYPE == 0) {
    char* H = ws + OFF_H + ((size_t)(mt * 8 + wm * 4 + (g & 1)) * 54 + j * 6) * 1024 + ((g >> 1) * 16 + c) * 16;
#pragma unroll
    for (int a = 0; a < 4; a += 2) {
#pragma unroll
      for (int b = 0; b < 3; ++b) {
        u32x2 w[2];
#pragma unroll
        for (int h = 0; h < 2; ++h) {
          w[h][0] = cvt2(bfr2(sigm2(bfr2(lo2(acc[a + h][b])))) * bfr2(lo2(acc[a + h][b + 3])));
          w[h][1] = cvt2(bfr2(sigm2(bfr2(hi2(acc[a + h][b])))) * bfr2(hi2(acc[a + h][b + 3])));
        }
        const auto s0 = __builtin_amdgcn_permlane16_swap(w[0][0], w[1][0], false, false);
        const auto s1 = __builtin_amdgcn_permlane16_swap(w[0][1], w[1][1], false, false);
        const int f0 = wn * 48 + b * 16;
        *(u32x4*)(H + (size_t)a * 54 * 1024 + (f0 >> 5) * 1024 + ((f0 >> 4) & 1) * 512) = (u32x4){s0[0], s1[0], s0[1], s1[1]};
      }
    }
  } else if (TYPE == 1) {
    char* PQ = ws + OFF_PQ + ((size_t)(mt * 8 + wm * 4 + (g & 1)) * 20 + j * 10) * 1024 + ((g >> 1) * 16 + c) * 16;
#pragma unroll
    for (int a = 0; a < 4; a += 2) {
      u32x2 w[5][2];
      float mn = 1.0f;
#pragma unroll
      for (int b = 0; b < 5; ++b)
#pragma unroll
        for (int h = 0; h < 2; ++h) { w[b][h][0] = phi2a(lo2(acc[a + h][b]), mn); w[b][h][1] = phi2a(hi2(acc[a + h][b]), mn); }
      if (phi_need_eps(mn)) {
#pragma unroll
        for (int b = 0; b < 5; ++b)
#pragma unroll
          for (int h = 0; h < 2; ++h) { w[b][h][0] = phi2b(w[b][h][0]); w[b][h][1] = phi2b(w[b][h][1]); }
      }
#pragma unroll
      for (int b = 0; b < 5; ++b) {
        const auto s0 = __builtin_amdgcn_permlane16_swap(w[b][0][0], w[b][1][0], false, false);
        const auto s1 = __builtin_amdgcn_permlane16_swap(w[b][0][1], w[b][1][1], false, false);
        const int f0 = wn * 80 + b * 16;
        *(u32x4*)(PQ + (size_t)a * 20 * 1024 + (f0 >> 5) * 1024 + ((f0 >> 4) & 1) * 512) = (u32x4){s0[0], s1[0], s0[1], s1[1]};
      }
    }
  } else {
    static_assert(K1_NS == 4 && 332 * 272 <= 3 * K1_STAGE, "T must not overlap the slot of the last stage");
    char* tw = smem + c * 272 + (wm * 64 + 4 * g) * 2;
    if (tid < 16) *(u32x4*)(smem + 320 * 272 + tid * 16) = (u32x4){0x3F803F80u, 0x3F803F80u, 0x3F803F80u, 0x3F803F80u};
    auto kvepi = [&](auto NK_) {
      constexpr int NK = decltype(NK_)::value;
      u32x2 w[4][NK];
      float mn = 1.0f;
#pragma unroll
      for (int a = 0; a < 4; ++a)
#pragma unroll
        for (int b = 0; b < NK; ++b) w[a][b] = (u32x2){phi2a(lo2(acc[a][b]), mn), phi2a(hi2(acc[a][b]), mn)};
#pragma unroll
      for (int a = 0; a < 4; ++a)
#pragma unroll
        for (int b = NK; b < 5; ++b)
          *(u32x2*)(tw + kvnf(wn, b) * 16 * 272 + a * 32) = (u32x2){cvt2(lo2(acc[a][b])), cvt2(hi2(acc[a][b]))};
      if (phi_need_eps(mn)) {
#pragma unroll
        for (int a = 0; a < 4; ++a)
#pragma unroll
          for (int b = 0; b < NK; ++b) w[a][b] = (u32x2){phi2b(w[a][b][0]), phi2b(w[a][b][1])};
      }
#pragma unroll
      for (int a = 0; a < 4; ++a)
#pragma unroll
        for (int b = 0; b < NK; ++b) *(u32x2*)(tw + kvnf(wn, b) * 16 * 272 + a * 32) = w[a][b];
    };
    if (wn < 2) kvepi(std::integral_constant<int, 3>{}); else kvepi(std::integral_constant<int, 2>{});
    STAMP(1, 4);
    __syncthreads();
    STAMP(1, 5);
    const int r32 = lane & 31, hh = lane >> 5;
    f32x16 akv;
#pragma unroll
    for (int i = 0; i < 16; ++i) akv[i] = 0.f;
    const char* ap = smem + (20 * wave + r32) * 272 + 16 * hh;
    const char* bp = smem + (r32 == 20 ? 320 : 160 + 20 * wave + r32) * 272 + 16 * hh;
#pragma unroll
    for (int s = 0; s < 8; ++s)
      akv = __builtin_amdgcn_mfma_f32_32x32x16_bf16(*(const s16x8*)(ap + 32 * s), *(const s16x8*)(bp + 32 * s), akv, 0, 0, 0);
    STAMP(1, 6);
    const int head = j * 8 + wave;
    float* P = (float*)(ws + OFF_KVP) + (size_t)(mt * 32 + head) * 504;
    if (r32 <= 20) {
#pragma unroll
      for (int q = 0; q < 3; ++q) {
        const int d0 = 8 * q + 4 * hh;
        if (d0 < 20) {
          *(f32x4*)(P + r32 * 24 + d0) = (f32x4){akv[4 * q], akv[4 * q + 1], akv[4 * q + 2], akv[4 * q + 3]};
        }
      }
    }
  }
}

__global__ __launch_bounds__(512, 2) void k1_gemm(const u16* __restrict__ wqkv, const u16* __restrict__ wg,
                                                   const u16* __restrict__ wu, const u16* __restrict__ wo,
                                                   const u16* __restrict__ wdown, char* __restrict__ ws) {
  extern __shared__ __attribute__((aligned(16))) char smem[];
  const int bid = blockIdx.x;
  if (bid >= 240) {
    retile_all((bid - 240) * 512 + threadIdx.x, wo, wdown, ws);
    return;
  }
  const int p = (bid & 7) * 30 + (bid >> 3);
  const int mh = p / 120, rem = p % 120, nt = rem >> 3, mt = mh * 8 + (rem & 7);
  if (nt < 4) k1_body<2>(wqkv, wqkv, ws, mt, nt, smem);
  else if (nt < 6) k1_body<1>(wqkv, wqkv, ws, mt, nt - 4, smem);
  else k1_body<0>(wg, wu, ws, mt, nt - 6, smem);
  STAMP(1, 3);
}

constexpr int K2_LDS = 163840;
#define K2DD 3
constexpr int K2_D = K2DD;
#define K2DW 3
constexpr int K2_DW = K2DW;

__global__ __launch_bounds__(512, 2) void k2_gemm(const u16* __restrict__ x, const char* __restrict__ ws, u16* __restrict__ out) {
  extern __shared__ __attribute__((aligned(16))) char smem[];
  const int tid = threadIdx.x, lane = tid & 63;
  const int wave = __builtin_amdgcn_readfirstlane(tid >> 6);
  const int wq = wave & 3;
  const int g = lane >> 4, c = lane & 15;
  const int bid = blockIdx.x;
  const int mt = (bid & 7) * 4 + (bid >> 6), nt = (bid >> 3) & 7;

  STAMP(2, 0);
  f32x4 acc[4][5];
#pragma unroll
  for (int a = 0; a < 4; ++a)
#pragma unroll
    for (int b = 0; b < 5; ++b) acc[a][b] = (f32x4){0.f, 0.f, 0.f, 0.f};
  const int b0 = wave < 4 ? 0 : 3, nb = wave < 4 ? 3 : 2;
  const int tok = mt * 64 + wq * 16 + c;

  if (wave < 4) {
    const char* hsrc = ws + OFF_H + ((size_t)(mt * 4) * 54 + wq) * 1024 + lane * 16;
    const char* wsrc = ws + OFF_WC + ((size_t)nt * 54 + wq) * 5120 + lane * 16;
    s16x8 xr[K2_D][4], wr[K2_D][5];
    auto ld = [&](int slot, int i) {
#pragma unroll
      for (int b = 0; b < 5; ++b) wr[slot][b] = *(const s16x8*)(wsrc + (size_t)(20 * i + b) * 1024);
#pragma unroll
      for (int a = 0; a < 4; ++a) xr[slot][a] = *(const s16x8*)(hsrc + (size_t)(a * 54 + 4 * i) * 1024);
    };
    auto mma1 = [&](int slot) {
#pragma unroll
      for (int a = 0; a < 4; ++a)
#pragma unroll
        for (int b = 0; b < 5; ++b) acc[a][b] = __builtin_amdgcn_mfma_f32_16x16x32_bf16(wr[slot][b], xr[slot][a], acc[a][b], 0, 0, 0);
    };
#pragma unroll
    for (int i = 0; i < K2_D; ++i) ld(i, i);
    SCHED;
#pragma unroll
    for (int i = 0; i < 13; ++i) {
      mma1(i % K2_D);
      if (i + K2_D < 13) ld(i % K2_D, i + K2_D);
      else if (i + K2_D == 13) { if (wave < 2) ld(i % K2_D, 13); }
      SCHED;
    }
    if (wave < 2) mma1(13 % K2_D);
    STAMP(2, 1);
  } else {
    __builtin_amdgcn_s_setprio(3);
    char* reg = smem + wq * 36864;
    if (bid < 32) {
      const int t = tid - 256, u = t >> 2, q = t & 3, uu = u < 63 ? u : 62, e = uu / 3, g3 = uu - e * 3;
      const float* src = (const float*)(ws + OFF_KVP) + ((size_t)(4 * q) * 32 + bid) * 504 + e * 24 + 8 * g3;
      f32x4 lo[4], hi[4];
#pragma unroll
      for (int i = 0; i < 4; ++i) { lo[i] = *(const f32x4*)(src + i * 32 * 504); hi[i] = *(const f32x4*)(src + i * 32 * 504 + (g3 < 2 ? 4 : 0)); }
      f32x4 s0 = (lo[0] + lo[1]) + (lo[2] + lo[3]), s1 = (hi[0] + hi[1]) + (hi[2] + hi[3]);
#pragma unroll
      for (int o = 1; o <= 2; o <<= 1)
#pragma unroll
        for (int r = 0; r < 4; ++r) { s0[r] += __shfl_xor(s0[r], o); s1[r] += __shfl_xor(s1[r], o); }
      u32x4 w;
      w[0] = pack2(s0[0], s0[1]); w[1] = pack2(s0[2], s0[3]);
      w[2] = g3 < 2 ? pack2(s1[0], s1[1]) : 0u; w[3] = g3 < 2 ? pack2(s1[2], s1[3]) : 0u;
#pragma unroll
      for (int r = 0; r < 4; ++r) w[r] = w[r] == KV_EMPTY ? 0x7FC07FC0u : w[r];
      const int slot = e < 16 ? g3 * 16 + e : 48 + (e - 16) * 3 + g3;
      if (q == 0 && u < 63)
        asm volatile("global_store_dwordx4 %0, %1, off sc1\n\ts_nop 1" :: "v"(ws + OFF_KVF + ((size_t)bid * 64 + slot) * 16), "v"(w) : "memory");
    }
    {
      const char* pqg = ws + OFF_PQ + ((size_t)(mt * 4) * 32 + wq * 8) * 640 + lane * 16;
#pragma unroll
      for (int tg = 0; tg < 4; ++tg)
#pragma unroll
        for (int i = 0; i < 5; ++i)
          __builtin_amdgcn_global_load_lds((const unsigned*)(pqg + tg * 20480 + i * 1024), (unsigned*)(reg + tg * 5120 + i * 1024), 16, 0, 0);
    }
    const char* wosrc = ws + OFF_WO + (size_t)(nt * 4 + wq) * 25600 + lane * 16;
    s16x8 w2[K2_DW][5];
    auto ldW = [&](int s) {
#pragma unroll
      for (int b = 0; b < 5; ++b) w2[s % K2_DW][b] = *(const s16x8*)(wosrc + (s * 5 + b) * 1024);
    };
#pragma unroll
    for (int s = 0; s < K2_DW; ++s) ldW(s);
    SCHED;
    const char* kvl = reg + 20480 + lane * 16;
    const char* kg = ws + OFF_KVF + (size_t)(wq * 8) * 1024;
    const unsigned kvm0 = __builtin_amdgcn_readfirstlane((unsigned)(size_t)(__attribute__((address_space(3))) char*)(reg + 20480));
    auto kvload = [&](int i0_, int i1_) {
#pragma unroll
      for (int i = 0; i < 8; ++i)
        if (i >= i0_ && i < i1_)
          asm volatile("s_mov_b32 m0, %0\n\ts_nop 0\n\tglobal_load_lds_dwordx4 %1, %2 sc1" :: "s"(kvm0 + i * 1024), "v"(lane * 16), "s"(kg + i * 1024) : "memory");
    };
    auto kvload_l2 = [&](int i0_, int i1_) {
#pragma unroll
      for (int i = 0; i < 8; ++i)
        if (i >= i0_ && i < i1_)
          asm volatile("s_mov_b32 m0, %0\n\ts_nop 0\n\tglobal_load_lds_dwordx4 %1, %2" :: "s"(kvm0 + i * 1024), "v"(lane * 16), "s"(kg + i * 1024) : "memory");
    };
    auto kvwait = [&](int i0_, int i1_) {
      while (true) {
        asm volatile("s_waitcnt vmcnt(0)" ::: "memory");
        bool bad = false;
#pragma unroll
        for (int i = 0; i < 8; ++i)
          if (i >= i0_ && i < i1_) {
            const u32x4 v = *(const __attribute__((address_space(3))) u32x4*)(kvl + i * 1024);
            bad = bad || v[0] == KV_EMPTY || v[1] == KV_EMPTY || v[2] == KV_EMPTY || v[3] == KV_EMPTY;
            if ((i & 3) == 3) __builtin_amdgcn_sched_barrier(0);
          }
        if (__builtin_amdgcn_ballot_w64(bad) == 0ull) break;
        __builtin_amdgcn_s_sleep(2);
        kvload(i0_, i1_);
      }
    };
    kvload(0, 2);
    kvwait(0, 2);
    __builtin_amdgcn_s_waitcnt(0x0F70);
    kvload_l2(2, 8);
    SCHED;
    STAMPW(4);
    const char* pql = reg + (g < 3 ? g : 0) * 256 + c * 16;
    const char* kv0 = reg + 20480 + (g < 3 ? lane : 63) * 16;
    const char* kv1 = reg + 20480 + ((g < 3 && (c & 3) < 2) ? ((c & 3) == 0 ? 48 + (c >> 2) * 3 + g : 60 + g) : 63) * 16;
    const f32x4 z4 = (f32x4){0.f, 0.f, 0.f, 0.f};
    unsigned ow[4][20];
    auto mma2 = [&](int s) {
#pragma unroll
      for (int a = 0; a < 4; ++a) {
        const u32x4 o4 = (u32x4){ow[a][4 * s], ow[a][4 * s + 1], ow[a][4 * s + 2], ow[a][4 * s + 3]};
        const s16x8 of = __builtin_bit_cast(s16x8, o4);
#pragma unroll
        for (int b = 0; b < 5; ++b) acc[a][b] = __builtin_amdgcn_mfma_f32_16x16x32_bf16(w2[s % K2_DW][b], of, acc[a][b], 0, 0, 0);
      }
      if (s + K2_DW < 5) ldW(s + K2_DW);
    };
#pragma unroll
    for (int m = 0; m < 4; ++m) {
      f32x4 ov[2][4]; float oe[2][4];
#pragma unroll
      for (int h2 = 0; h2 < 2; ++h2) {
        const int hh = 2 * m + h2;
        const s16x8 k0 = *(const s16x8*)(kv0 + hh * 1024), k1 = *(const s16x8*)(kv1 + hh * 1024);
#pragma unroll
        for (int tg = 0; tg < 4; ++tg) {
          u32x4 pq;
          if ((hh & 1) == 0) pq = *(const u32x4*)(pql + tg * 5120 + (5 * hh / 2) * 256);
          else {
            const u32x2 p0 = *(const u32x2*)(pql + tg * 5120 + ((5 * hh - 1) / 2) * 256 + 8), p1 = *(const u32x2*)(pql + tg * 5120 + ((5 * hh - 1) / 2) * 256 + 256);
            pq = (u32x4){p0[0], p0[1], p1[0], p1[1]};
          }
          u32x4 pk4;
          pk4[0] = g < 3 ? pq[0] : 0u; pk4[1] = g < 3 ? pq[1] : 0u;
          pk4[2] = g < 2 ? pq[2] : 0u; pk4[3] = g < 2 ? pq[3] : 0u;
          const s16x8 pf = __builtin_bit_cast(s16x8, pk4);
          const f32x4 d1 = __builtin_amdgcn_mfma_f32_16x16x32_bf16(k1, pf, z4, 0, 0, 0);
          const f32x4 d0 = __builtin_amdgcn_mfma_f32_16x16x32_bf16(k0, pf, z4, 0, 0, 0);
          const float rden = frcp(bfr(bfr(d1[1]) + 1e-6f));
          const f32x2 lo = bfr2(lo2(d0)) * rden, hi = bfr2(hi2(d0)) * rden;
          ov[h2][tg] = (f32x4){lo[0], lo[1], hi[0], hi[1]};
          oe[h2][tg] = bfr(d1[0]) * rden;
        }
      }
#pragma unroll
      for (int tg = 0; tg < 4; ++tg) {
        ow[tg][5 * m + 0] = cvt2(lo2(ov[0][tg]));
        ow[tg][5 * m + 1] = cvt2(hi2(ov[0][tg]));
        ow[tg][5 * m + 2] = cvt2((f32x2){oe[0][tg], ov[1][tg][0]});
        ow[tg][5 * m + 3] = cvt2((f32x2){ov[1][tg][1], ov[1][tg][2]});
        ow[tg][5 * m + 4] = cvt2((f32x2){ov[1][tg][3], oe[1][tg]});
      }
      if (m == 0) kvwait(2, 8);
      mma2(m);
      if (m == 3) mma2(4);
      SCHED;
      if (m < 2) STAMPW(5 + m);
    }
  }
  u32x2 xw[3];
#pragma unroll
  for (int i = 0; i < 3; ++i) xw[i] = *(const u32x2*)(x + (size_t)tok * D_ + nt * 80 + (i < nb ? b0 + i : b0) * 16 + 4 * g);
  __syncthreads();

  f32x4* red = (f32x4*)smem;
#pragma unroll
  for (int a = 0; a < 4; ++a)
#pragma unroll
    for (int b = 0; b < 5; ++b) red[(wave * 20 + a * 5 + b) * 64 + lane] = acc[a][b];
  __syncthreads();
#pragma unroll
  for (int i = 0; i < 3; ++i) {
    if (i < nb) {
      const int f = wq * 5 + b0 + i;
      f32x4 y1 = red[(0 * 20 + f) * 64 + lane], y2 = red[(4 * 20 + f) * 64 + lane];
#pragma unroll
      for (int sw = 1; sw < 4; ++sw) { y1 += red[(sw * 20 + f) * 64 + lane]; y2 += red[((4 + sw) * 20 + f) * 64 + lane]; }
      const int n = nt * 80 + (b0 + i) * 16 + 4 * g;
      u32x2 w;
      {
        const f32x2 y = bfr2(lo2(y1)), gs = bfr2(y * sigm2(y)), sv = bfr2(lo2(y2));
        w[0] = cvt2(unp2(xw[i][0]) + bfr2(sv * gs));
      }
      {
        const f32x2 y = bfr2(hi2(y1)), gs = bfr2(y * sigm2(y)), sv = bfr2(hi2(y2));
        w[1] = cvt2(unp2(xw[i][1]) + bfr2(sv * gs));
      }
      *(u32x2*)(out + (size_t)tok * D_ + n) = w;
    }
  }
  STAMP(2, 3);
}

extern "C" void kernel_launch(void* const* d_in, const int* in_sizes, int n_in,
                              void* d_out, int out_size, void* d_ws, size_t ws_size,
                              hipStream_t stream) {
  const u16* x = (const u16*)d_in[0];
  const u16* wqkv = (const u16*)d_in[1];
  const u16* wo = (const u16*)d_in[2];
  const u16* wg = (const u16*)d_in[4];
  const u16* wu = (const u16*)d_in[5];
  const u16* wd = (const u16*)d_in[6];
  char* ws = (char*)d_ws;
  u16* out = (u16*)d_out;

  hipFuncSetAttribute((const void*)k1_gemm, hipFuncAttributeMaxDynamicSharedMemorySize, K1_NS * K1_STAGE);
  hipFuncSetAttribute((const void*)k2_gemm, hipFuncAttributeMaxDynamicSharedMemorySize, K2_LDS);

  k0_prep<<<512, 256, 0, stream>>>(x, ws, wqkv, wg, wu, out);
  k1_gemm<<<256, 512, K1_NS * K1_STAGE, stream>>>(wqkv, wg, wu, wo, wd, ws);
  k2_gemm<<<256, 512, K2_LDS, stream>>>(x, ws, out);
}
```

```cpp
#include <hip/hip_runtime.h>
#include <hip/hip_bf16.h>
#include <stdint.h>
#include <type_traits>

typedef unsigned short u16;
typedef __attribute__((ext_vector_type(8))) short s16x8;
typedef __attribute__((ext_vector_type(4))) short s16x4;
typedef __attribute__((ext_vector_type(4))) float f32x4;
typedef __attribute__((ext_vector_type(16))) float f32x16;
typedef __attribute__((ext_vector_type(2))) unsigned u32x2;
typedef __attribute__((ext_vector_type(4))) unsigned u32x4;
typedef u32x4 u32x4_a8 __attribute__((aligned(8)));
typedef __attribute__((address_space(3))) s16x4* lds_s16x4_ptr;
typedef __attribute__((address_space(1))) unsigned gu32;
typedef __attribute__((address_space(1))) unsigned long long gu64;

#define RLX_AGENT __ATOMIC_RELAXED, __HIP_MEMORY_SCOPE_AGENT

constexpr int S_ = 2048, D_ = 640, F_ = 1728, NQKV_ = 1920;

constexpr size_t OFF_XN    = 8192;
constexpr size_t SZ_XN     = (size_t)S_ * D_ * 2;
constexpr size_t OFF_H     = OFF_XN + SZ_XN;
constexpr size_t SZ_H      = (size_t)S_ * F_ * 2;
constexpr size_t OFF_PQ    = OFF_H + SZ_H;
constexpr size_t SZ_PQ     = (size_t)S_ * D_ * 2;
constexpr size_t OFF_WC    = OFF_PQ + SZ_PQ;
constexpr size_t SZ_WC     = (size_t)8 * 54 * 5 * 1024;
constexpr size_t OFF_WO    = OFF_WC + SZ_WC;
constexpr size_t SZ_WO     = (size_t)8 * 4 * 5 * 5 * 1024;
constexpr size_t OFF_KVP   = OFF_WO + SZ_WO;
constexpr size_t SZ_KVP    = (size_t)16 * 32 * 504 * 4;
constexpr size_t OFF_KVF   = OFF_KVP + SZ_KVP;
constexpr size_t SZ_KVF    = 32768;

#define KVAUX 16
constexpr unsigned KV_EMPTY = 0xFFFFFFFFu;
__device__ __forceinline__ float bf2f(u16 v) { return __uint_as_float(((unsigned)v) << 16); }
__device__ __forceinline__ u16 f2bf(float f) { return __builtin_bit_cast(u16, (__bf16)f); }
__device__ __forceinline__ float bfr(float f) { return bf2f(f2bf(f)); }
__device__ __forceinline__ unsigned pack2(float a, float b) { return (unsigned)f2bf(a) | ((unsigned)f2bf(b) << 16); }
typedef __attribute__((ext_vector_type(2))) float f32x2;
typedef __attribute__((ext_vector_type(2))) __bf16 bf16x2;
__device__ __forceinline__ unsigned cvt2(f32x2 v) { return __builtin_bit_cast(unsigned, __builtin_convertvector(v, bf16x2)); }
__device__ __forceinline__ f32x2 unp2(unsigned u) { return (f32x2){__uint_as_float(u << 16), __uint_as_float(u & 0xffff0000u)}; }
__device__ __forceinline__ f32x2 bfr2(f32x2 v) { return unp2(cvt2(v)); }
__device__ __forceinline__ f32x2 lo2(f32x4 v) { return __builtin_shufflevector(v, v, 0, 1); }
__device__ __forceinline__ f32x2 hi2(f32x4 v) { return __builtin_shufflevector(v, v, 2, 3); }
__device__ __forceinline__ unsigned phi2a(f32x2 a, float& mn) {
  const f32x2 t = bfr2(a), m = t * 1.44269504088896340736f, p = t + 1.0f;
  f32x2 sel;
  sel[0] = __builtin_amdgcn_fmed3f(__builtin_amdgcn_exp2f(m[0]), p[0], 1.0f);
  sel[1] = __builtin_amdgcn_fmed3f(__builtin_amdgcn_exp2f(m[1]), p[1], 1.0f);
  mn = __builtin_fminf(__builtin_fminf(mn, sel[0]), sel[1]);
  return cvt2(sel);
}
__device__ __forceinline__ bool phi_need_eps(float mn) { return __builtin_amdgcn_ballot_w64(!(mn >= 0.00048828125f)) != 0ull; }
__device__ __forceinline__ unsigned phi2b(unsigned w) { return cvt2(unp2(w) + 1e-6f); }
__device__ __forceinline__ f32x2 sigm2(f32x2 g) {
  const f32x2 m = g * -1.44269504088896340736f;
  f32x2 e; e[0] = __builtin_amdgcn_exp2f(m[0]); e[1] = __builtin_amdgcn_exp2f(m[1]);
  e = e + 1.0f;
  f32x2 r; r[0] = __builtin_amdgcn_rcpf(e[0]); r[1] = __builtin_amdgcn_rcpf(e[1]);
  return r;
}
__device__ __forceinline__ int aswz(int row) { int x = (row >> 2) & 3; return (((x ^ (x >> 1)) & 1) << 1) | (x >> 1); }

#define WAIT_V(n) asm volatile("s_waitcnt vmcnt(" #n ")" ::: "memory")
#define STAMP(kid, idx)
#define STAMPW(idx)
#define WAIT_L0 asm volatile("s_waitcnt lgkmcnt(0)" ::: "memory")
#define BAR __builtin_amdgcn_s_barrier()
#define SCHED __builtin_amdgcn_sched_barrier(0)
__device__ __forceinline__ float frcp(float v) { return __builtin_amdgcn_rcpf(v); }

constexpr int RT_WD = 216 * 80, RT_ITEMS = RT_WD + 80 * 80, RT_R = 3;
__device__ __forceinline__ void retile_all(int t0, const u16* __restrict__ wo, const u16* __restrict__ wdown, char* __restrict__ ws) {
  u32x4 r[RT_R][8];
  char* dst[RT_R];
#pragma unroll
  for (int it = 0; it < RT_R; ++it) {
    int t = t0 + it * 8192; if (t >= RT_ITEMS) t = t0;
    const bool isd = t < RT_WD;
    const int t2 = isd ? t : t - RT_WD;
    const int ko = t2 / 80, noct = t2 - ko * 80;
    const int n0 = noct * 8, nt = n0 / 80, nn = n0 - nt * 80, nf = nn >> 4, c0 = nn & 15, gg = ko & 3, kq = ko >> 2;
    const int wq = kq / 5, ks = kq - wq * 5;
    const u16* src = (isd ? wdown : wo) + n0;
#pragma unroll
    for (int jj = 0; jj < 8; ++jj) {
      const int q = 8 * ks + jj, hh = q / 5, rr = q - hh * 5;
      const int row = isd ? ko * 8 + jj : (wq * 8 + hh) * 20 + (rr < 4 ? 4 * gg + rr : 16 + gg);
      r[it][jj] = *(const u32x4*)(src + (size_t)row * D_);
    }
    dst[it] = ws + (isd ? OFF_WC + (size_t)(nt * 54 + kq) * 5120 : OFF_WO + (size_t)(nt * 20 + kq) * 5120) + (size_t)(nf * 64 + gg * 16 + c0) * 16;
  }
#pragma unroll
  for (int it = 0; it < RT_R; ++it) {
#pragma unroll
    for (int i = 0; i < 4; ++i) {
      u32x4 lo, hi;
#pragma unroll
      for (int q = 0; q < 4; ++q) {
        lo[q] = (r[it][2 * q][i] & 0xffffu) | (r[it][2 * q + 1][i] << 16);
        hi[q] = (r[it][2 * q][i] >> 16) | (r[it][2 * q + 1][i] & 0xffff0000u);
      }
      *(u32x4*)(dst[it] + (2 * i) * 16) = lo;
      *(u32x4*)(dst[it] + (2 * i + 1) * 16) = hi;
    }
  }
}

__global__ __launch_bounds__(256) void k0_prep(const u16* __restrict__ x, char* __restrict__ ws, const u16* __restrict__ wqkv,
                                               const u16* __restrict__ wg, const u16* __restrict__ wu) {
  const int tid = threadIdx.x;
  if (blockIdx.x >= 256) {
    const int t = (blockIdx.x - 256) * 256 + tid;
    unsigned v0, v1;
    const int l1 = t + 65536 < 107520 ? t + 65536 : t;
    const char* p0 = t < 38400 ? (const char*)wqkv + (size_t)t * 64 : (const char*)wg + (size_t)(t - 38400) * 64;
    const char* p1 = l1 < 38400 ? (const char*)wqkv + (size_t)l1 * 64 : (l1 < 72960 ? (const char*)wg + (size_t)(l1 - 38400) * 64 : (const char*)wu + (size_t)(l1 - 72960) * 64);
    asm volatile("global_load_dword %0, %2, off\n\tglobal_load_dword %1, %3, off\n\ts_waitcnt vmcnt(0)" : "=&v"(v0), "=&v"(v1) : "v"(p0), "v"(p1) : "memory");
    return;
  }
  {
    if (blockIdx.x < 8) {
      const int ci = blockIdx.x * 256 + tid;
      const unsigned v = (ci & 63) != 63 ? KV_EMPTY : 0u;
      *(u32x4*)(ws + OFF_KVF + (size_t)ci * 16) = (u32x4){v, v, v, v};
    }
    const int row = blockIdx.x * 8 + (tid >> 5);
    const int l32 = tid & 31;
    const u16* xr = x + (size_t)row * D_;
    float v[20];
    float sum = 0.f;
#pragma unroll
    for (int i = 0; i < 5; ++i) {
      u32x2 w = *(const u32x2*)(xr + (l32 + 32 * i) * 4);
      v[4 * i + 0] = __uint_as_float(w[0] << 16);
      v[4 * i + 1] = __uint_as_float(w[0] & 0xffff0000u);
      v[4 * i + 2] = __uint_as_float(w[1] << 16);
      v[4 * i + 3] = __uint_as_float(w[1] & 0xffff0000u);
      sum += v[4 * i] + v[4 * i + 1] + v[4 * i + 2] + v[4 * i + 3];
    }
#pragma unroll
    for (int o = 16; o >= 1; o >>= 1) sum += __shfl_xor(sum, o);
    const float mean = sum * (1.0f / D_);
    float sq = 0.f;
#pragma unroll
    for (int i = 0; i < 20; ++i) { float d = v[i] - mean; sq += d * d; }
#pragma unroll
    for (int o = 16; o >= 1; o >>= 1) sq += __shfl_xor(sq, o);
    const float mb = bfr(mean);
    const float vb = bfr(sq * (1.0f / D_));
    const float sd = bfr(sqrtf(bfr(vb + 1e-5f)));
    const float rsd = 1.0f / sd;
    u16* xn = (u16*)(ws + OFF_XN);
    const int mt = row >> 7, r = row & 127, sw = aswz(r);
#pragma unroll
    for (int i = 0; i < 5; ++i) {
      const int k = (l32 + 32 * i) * 4;
      float o0 = bfr(v[4 * i + 0] - mb) * rsd, o1 = bfr(v[4 * i + 1] - mb) * rsd;
      float o2 = bfr(v[4 * i + 2] - mb) * rsd, o3 = bfr(v[4 * i + 3] - mb) * rsd;
      u32x2 w; w[0] = pack2(o0, o1); w[1] = pack2(o2, o3);
      const int kt = k >> 5, ch = (k >> 3) & 3, hf = (k >> 2) & 1;
      size_t off = ((size_t)(mt * 20 + kt) * 128 + r) * 32 + ((ch ^ sw) << 3) + (hf << 2);
      *(u32x2*)(xn + off) = w;
    }
  }
  STAMP(0, 3);
}

constexpr int K1_STAGE = 32768;
#define K1NS 4
constexpr int K1_NS = K1NS;

__device__ __forceinline__ int kvnf(int wn, int i) {
  return wn < 2 ? (i < 3 ? 3 * wn + i : 10 + 2 * wn + (i - 3)) : (i < 2 ? 2 + 2 * wn + i : 8 + 3 * wn + (i - 2));
}
template <int TYPE>
__device__ __forceinline__ void k1_body(const u16* __restrict__ wA, const u16* __restrict__ wB, char* __restrict__ ws,
                                        int mt, int j, char* smem) {
  constexpr int NCB = TYPE == 0 ? 6 : 5;
  constexpr int NFR = TYPE == 0 ? 6 : 5;
  constexpr int LDW = TYPE == 0 ? F_ : NQKV_;
  const int tid = threadIdx.x, lane = tid & 63;
  const int wave = __builtin_amdgcn_readfirstlane(tid >> 6);
  const int wm = wave >> 2, wn = (TYPE == 2 && wave >= 4) ? ((wave + 2) & 3) : (wave & 3);
  const int g = lane >> 4, c = lane & 15;

  const char* asrc = ws + OFF_XN + (size_t)mt * 20 * 8192 + wave * 1024 + lane * 16;
  const u16* bsrc[3];
#pragma unroll
  for (int t = 0; t < 3; ++t) {
    int ii = wave + 8 * t; if (ii >= NCB * 4) ii = wave;
    const int cb = ii >> 2, krow = (ii & 3) * 8 + (lane >> 3), u = lane & 7;
    const int sk = ((krow >> 1) & 1) | (((krow >> 3) & 1) << 1);
    const int ch = (u >> 1) ^ sk;
    const int lc = cb * 64 + ch * 16 + (u & 1) * 8;
    const u16* p;
    if (TYPE == 0) p = (lc < 192) ? (wA + j * 192 + lc) : (wB + j * 192 + (lc - 192));
    else if (TYPE == 1) p = wA + j * 320 + lc;
    else p = (lc < 160) ? (wA + 640 + j * 160 + lc) : (wA + 1280 + j * 160 + (lc - 160));
    bsrc[t] = p + (size_t)krow * LDW;
  }
  const bool has3 = (NCB == 6) || (wave < 4);

  auto issue = [&](int kt) {
    char* sb = smem + (kt % K1_NS) * K1_STAGE;
    __builtin_amdgcn_global_load_lds((const unsigned*)(asrc + (size_t)kt * 8192), (unsigned*)(sb + wave * 1024), 16, 0, 0);
    __builtin_amdgcn_global_load_lds((const unsigned*)(bsrc[0] + (size_t)kt * 32 * LDW), (unsigned*)(sb + 8192 + wave * 1024), 16, 0, 0);
    __builtin_amdgcn_global_load_lds((const unsigned*)(bsrc[1] + (size_t)kt * 32 * LDW), (unsigned*)(sb + 8192 + (wave + 8) * 1024), 16, 0, 0);
    if (has3) __builtin_amdgcn_global_load_lds((const unsigned*)(bsrc[2] + (size_t)kt * 32 * LDW), (unsigned*)(sb + 8192 + (wave + 16) * 1024), 16, 0, 0);
  };

  const int xoff = (wm * 64 + c) * 64 + ((g ^ aswz(c)) << 4);
  int woff[NFR];
  {
    const int q = (lane & 15) >> 2, p = lane & 3;
    const int s = (q >> 1) | ((g & 1) << 1);
    const int lo = (8 * g + q) * 128 + (s << 5) + p * 8;
#pragma unroll
    for (int i = 0; i < NFR; ++i) {
      const int nf = TYPE == 0 ? (i < 3 ? 3 * wn + i : 12 + 3 * wn + (i - 3)) : (TYPE == 1 ? 5 * wn + i : kvnf(wn, i));
      woff[i] = 8192 + (nf >> 2) * 4096 + (lo ^ ((nf & 3) << 5));
    }
  }

  f32x4 acc[4][NFR];
#pragma unroll
  for (int a = 0; a < 4; ++a)
#pragma unroll
    for (int b = 0; b < NFR; ++b) acc[a][b] = (f32x4){0.f, 0.f, 0.f, 0.f};

  auto readfr = [&](int kt, s16x8 (&xf)[4], s16x8 (&wf)[NFR]) {
    const char* sb = smem + (kt % K1_NS) * K1_STAGE;
#pragma unroll
    for (int b = 0; b < NFR; ++b) {
      s16x4 lo = __builtin_amdgcn_ds_read_tr16_b64_v4i16((lds_s16x4_ptr)(sb + woff[b]));
      s16x4 hi = __builtin_amdgcn_ds_read_tr16_b64_v4i16((lds_s16x4_ptr)(sb + woff[b] + 512));
      wf[b] = __builtin_shufflevector(lo, hi, 0, 1, 2, 3, 4, 5, 6, 7);
    }
#pragma unroll
    for (int a = 0; a < 4; ++a) xf[a] = *(const s16x8*)(sb + xoff + a * 1024);
  };
  auto mma = [&](const s16x8 (&xf)[4], const s16x8 (&wf)[NFR]) {
#pragma unroll
    for (int a = 0; a < 4; ++a)
#pragma unroll
      for (int b = 0; b < NFR; ++b) {
        if (TYPE == 2) acc[a][b] = __builtin_amdgcn_mfma_f32_16x16x32_bf16(xf[a], wf[b], acc[a][b], 0, 0, 0);
        else           acc[a][b] = __builtin_amdgcn_mfma_f32_16x16x32_bf16(wf[b], xf[a], acc[a][b], 0, 0, 0);
      }
  };
#define WAITN(N) asm volatile("s_waitcnt vmcnt(%0)" :: "n"(N) : "memory")
#define K1_WAIT(n) do { if (has3) WAITN(4 * (n)); else WAITN(3 * (n)); } while (0)

  s16x8 xA[4], wAf[NFR], xB[4], wBf[NFR];
#define SGB(m, n) __builtin_amdgcn_sched_group_barrier(m, n, 0)
  auto pattern_full = [&]() {
    SGB(0x10, 1); SGB(0x8, 1); SGB(0x10, 1); SGB(0x8, 1); SGB(0x10, 1); SGB(0x8, 1); SGB(0x10, 1); SGB(0x8, 1);
#pragma unroll
    for (int i = 0; i < 4 + 2 * NFR; ++i) { SGB(0x100, 1); SGB(0x8, 1); }
    SGB(0x8, 4);
  };
  auto pattern_tail = [&]() {
#pragma unroll
    for (int i = 0; i < 4 + 2 * NFR; ++i) { SGB(0x100, 1); SGB(0x8, 1); }
    SGB(0x8, 8);
  };
  STAMP(1, 0);
#pragma unroll
  for (int i = 0; i < K1_NS; ++i) issue(i);
  K1_WAIT(K1_NS - 1);
  BAR;
  STAMP(1, 1);
  readfr(0, xA, wAf);
  SCHED;
#pragma unroll
  for (int kt = 0; kt < 19; ++kt) {
    if (kt + K1_NS <= 20) K1_WAIT(K1_NS - 2); else K1_WAIT(18 - kt);
    WAIT_L0; BAR; SCHED;
    if (kt + K1_NS < 20) issue(kt + K1_NS);
    if (kt & 1) { readfr(kt + 1, xA, wAf); mma(xB, wBf); }
    else        { readfr(kt + 1, xB, wBf); mma(xA, wAf); }
    if (kt + K1_NS < 20) pattern_full(); else pattern_tail();
    SCHED;
  }
  mma(xB, wBf);

  STAMP(1, 2);
  if (TYPE == 0) {
    char* H = ws + OFF_H + ((size_t)(mt * 8 + wm * 4 + (g & 1)) * 54 + j * 6) * 1024 + ((g >> 1) * 16 + c) * 16;
#pragma unroll
    for (int a = 0; a < 4; a += 2) {
#pragma unroll
      for (int b = 0; b < 3; ++b) {
        u32x2 w[2];
#pragma unroll
        for (int h = 0; h < 2; ++h) {
          w[h][0] = cvt2(bfr2(sigm2(bfr2(lo2(acc[a + h][b])))) * bfr2(lo2(acc[a + h][b + 3])));
          w[h][1] = cvt2(bfr2(sigm2(bfr2(hi2(acc[a + h][b])))) * bfr2(hi2(acc[a + h][b + 3])));
        }
        const auto s0 = __builtin_amdgcn_permlane16_swap(w[0][0], w[1][0], false, false);
        const auto s1 = __builtin_amdgcn_permlane16_swap(w[0][1], w[1][1], false, false);
        const int f0 = wn * 48 + b * 16;
        *(u32x4*)(H + (size_t)a * 54 * 1024 + (f0 >> 5) * 1024 + ((f0 >> 4) & 1) * 512) = (u32x4){s0[0], s1[0], s0[1], s1[1]};
      }
    }
  } else if (TYPE == 1) {
    char* PQ = ws + OFF_PQ + ((size_t)(mt * 8 + wm * 4 + (g & 1)) * 20 + j * 10) * 1024 + ((g >> 1) * 16 + c) * 16;
#pragma unroll
    for (int a = 0; a < 4; a += 2) {
      u32x2 w[5][2];
      float mn = 1.0f;
#pragma unroll
      for (int b = 0; b < 5; ++b)
#pragma unroll
        for (int h = 0; h < 2; ++h) { w[b][h][0] = phi2a(lo2(acc[a + h][b]), mn); w[b][h][1] = phi2a(hi2(acc[a + h][b]), mn); }
      if (phi_need_eps(mn)) {
#pragma unroll
        for (int b = 0; b < 5; ++b)
#pragma unroll
          for (int h = 0; h < 2; ++h) { w[b][h][0] = phi2b(w[b][h][0]); w[b][h][1] = phi2b(w[b][h][1]); }
      }
#pragma unroll
      for (int b = 0; b < 5; ++b) {
        const auto s0 = __builtin_amdgcn_permlane16_swap(w[b][0][0], w[b][1][0], false, false);
        const auto s1 = __builtin_amdgcn_permlane16_swap(w[b][0][1], w[b][1][1], false, false);
        const int f0 = wn * 80 + b * 16;
        *(u32x4*)(PQ + (size_t)a * 20 * 1024 + (f0 >> 5) * 1024 + ((f0 >> 4) & 1) * 512) = (u32x4){s0[0], s1[0], s0[1], s1[1]};
      }
    }
  } else {
    static_assert(K1_NS == 4 && 332 * 272 <= 3 * K1_STAGE, "T must not overlap the slot of the last stage");
    char* tw = smem + c * 272 + (wm * 64 + 4 * g) * 2;
    if (tid < 16) *(u32x4*)(smem + 320 * 272 + tid * 16) = (u32x4){0x3F803F80u, 0x3F803F80u, 0x3F803F80u, 0x3F803F80u};
    auto kvepi = [&](auto NK_) {
      constexpr int NK = decltype(NK_)::value;
      u32x2 w[4][NK];
      float mn = 1.0f;
#pragma unroll
      for (int a = 0; a < 4; ++a)
#pragma unroll
        for (int b = 0; b < NK; ++b) w[a][b] = (u32x2){phi2a(lo2(acc[a][b]), mn), phi2a(hi2(acc[a][b]), mn)};
#pragma unroll
      for (int a = 0; a < 4; ++a)
#pragma unroll
        for (int b = NK; b < 5; ++b)
          *(u32x2*)(tw + kvnf(wn, b) * 16 * 272 + a * 32) = (u32x2){cvt2(lo2(acc[a][b])), cvt2(hi2(acc[a][b]))};
      if (phi_need_eps(mn)) {
#pragma unroll
        for (int a = 0; a < 4; ++a)
#pragma unroll
          for (int b = 0; b < NK; ++b) w[a][b] = (u32x2){phi2b(w[a][b][0]), phi2b(w[a][b][1])};
      }
#pragma unroll
      for (int a = 0; a < 4; ++a)
#pragma unroll
        for (int b = 0; b < NK; ++b) *(u32x2*)(tw + kvnf(wn, b) * 16 * 272 + a * 32) = w[a][b];
    };
    if (wn < 2) kvepi(std::integral_constant<int, 3>{}); else kvepi(std::integral_constant<int, 2>{});
    STAMP(1, 4);
    __syncthreads();
    STAMP(1, 5);
    const int r32 = lane & 31, hh = lane >> 5;
    f32x16 akv;
#pragma unroll
    for (int i = 0; i < 16; ++i) akv[i] = 0.f;
    const char* ap = smem + (20 * wave + r32) * 272 + 16 * hh;
    const char* bp = smem + (r32 == 20 ? 320 : 160 + 20 * wave + r32) * 272 + 16 * hh;
#pragma unroll
    for (int s = 0; s < 8; ++s)
      akv = __builtin_amdgcn_mfma_f32_32x32x16_bf16(*(const s16x8*)(ap + 32 * s), *(const s16x8*)(bp + 32 * s), akv, 0, 0, 0);
    STAMP(1, 6);
    const int head = j * 8 + wave;
    float* P = (float*)(ws + OFF_KVP) + (size_t)(mt * 32 + head) * 504;
    if (r32 <= 20) {
#pragma unroll
      for (int q = 0; q < 3; ++q) {
        const int d0 = 8 * q + 4 * hh;
        if (d0 < 20) {
          *(f32x4*)(P + r32 * 24 + d0) = (f32x4){akv[4 * q], akv[4 * q + 1], akv[4 * q + 2], akv[4 * q + 3]};
        }
      }
    }
  }
}

__global__ __launch_bounds__(512, 2) void k1_gemm(const u16* __restrict__ wqkv, const u16* __restrict__ wg,
                                                   const u16* __restrict__ wu, const u16* __restrict__ wo,
                                                   const u16* __restrict__ wdown, char* __restrict__ ws) {
  extern __shared__ __attribute__((aligned(16))) char smem[];
  const int bid = blockIdx.x;
  if (bid >= 240) {
    retile_all((bid - 240) * 512 + threadIdx.x, wo, wdown, ws);
    return;
  }
  const int p = (bid & 7) * 30 + (bid >> 3);
  const int mh = p / 120, rem = p % 120, nt = rem >> 3, mt = mh * 8 + (rem & 7);
  if (nt < 4) k1_body<2>(wqkv, wqkv, ws, mt, nt, smem);
  else if (nt < 6) k1_body<1>(wqkv, wqkv, ws, mt, nt - 4, smem);
  else k1_body<0>(wg, wu, ws, mt, nt - 6, smem);
  STAMP(1, 3);
}

constexpr int K2_LDS = 163840;
#define K2DD 3
constexpr int K2_D = K2DD;
#define K2DW 3
constexpr int K2_DW = K2DW;

__global__ __launch_bounds__(512, 2) void k2_gemm(const u16* __restrict__ x, const char* __restrict__ ws, u16* __restrict__ out) {
  extern __shared__ __attribute__((aligned(16))) char smem[];
  const int tid = threadIdx.x, lane = tid & 63;
  const int wave = __builtin_amdgcn_readfirstlane(tid >> 6);
  const int wq = wave & 3;
  const int g = lane >> 4, c = lane & 15;
  const int bid = blockIdx.x;
  const int mt = (bid & 7) * 4 + (bid >> 6), nt = (bid >> 3) & 7;

  STAMP(2, 0);
  f32x4 acc[4][5];
#pragma unroll
  for (int a = 0; a < 4; ++a)
#pragma unroll
    for (int b = 0; b < 5; ++b) acc[a][b] = (f32x4){0.f, 0.f, 0.f, 0.f};
  const int b0 = wave < 4 ? 0 : 3, nb = wave < 4 ? 3 : 2;
  const int tok = mt * 64 + wq * 16 + c;

  if (wave < 4) {
    const char* hsrc = ws + OFF_H + ((size_t)(mt * 4) * 54 + wq) * 1024 + lane * 16;
    const char* wsrc = ws + OFF_WC + ((size_t)nt * 54 + wq) * 5120 + lane * 16;
    s16x8 xr[K2_D][4], wr[K2_D][5];
    auto ld = [&](int slot, int i) {
#pragma unroll
      for (int b = 0; b < 5; ++b) wr[slot][b] = *(const s16x8*)(wsrc + (size_t)(20 * i + b) * 1024);
#pragma unroll
      for (int a = 0; a < 4; ++a) xr[slot][a] = *(const s16x8*)(hsrc + (size_t)(a * 54 + 4 * i) * 1024);
    };
    auto mma1 = [&](int slot) {
#pragma unroll
      for (int a = 0; a < 4; ++a)
#pragma unroll
        for (int b = 0; b < 5; ++b) acc[a][b] = __builtin_amdgcn_mfma_f32_16x16x32_bf16(wr[slot][b], xr[slot][a], acc[a][b], 0, 0, 0);
    };
#pragma unroll
    for (int i = 0; i < K2_D; ++i) ld(i, i);
    SCHED;
#pragma unroll
    for (int i = 0; i < 13; ++i) {
      mma1(i % K2_D);
      if (i + K2_D < 13) ld(i % K2_D, i + K2_D);
      else if (i + K2_D == 13) { if (wave < 2) ld(i % K2_D, 13); }
      SCHED;
    }
    if (wave < 2) mma1(13 % K2_D);
    STAMP(2, 1);
  } else {
    __builtin_amdgcn_s_setprio(3);
    char* reg = smem + wq * 36864;
    if (bid < 32) {
      const int t = tid - 256, u = t >> 2, q = t & 3, uu = u < 63 ? u : 62, e = uu / 3, g3 = uu - e * 3;
      const float* src = (const float*)(ws + OFF_KVP) + ((size_t)(4 * q) * 32 + bid) * 504 + e * 24 + 8 * g3;
      f32x4 lo[4], hi[4];
#pragma unroll
      for (int i = 0; i < 4; ++i) { lo[i] = *(const f32x4*)(src + i * 32 * 504); hi[i] = *(const f32x4*)(src + i * 32 * 504 + (g3 < 2 ? 4 : 0)); }
      f32x4 s0 = (lo[0] + lo[1]) + (lo[2] + lo[3]), s1 = (hi[0] + hi[1]) + (hi[2] + hi[3]);
#pragma unroll
      for (int o = 1; o <= 2; o <<= 1)
#pragma unroll
        for (int r = 0; r < 4; ++r) { s0[r] += __shfl_xor(s0[r], o); s1[r] += __shfl_xor(s1[r], o); }
      u32x4 w;
      w[0] = pack2(s0[0], s0[1]); w[1] = pack2(s0[2], s0[3]);
      w[2] = g3 < 2 ? pack2(s1[0], s1[1]) : 0u; w[3] = g3 < 2 ? pack2(s1[2], s1[3]) : 0u;
#pragma unroll
      for (int r = 0; r < 4; ++r) w[r] = w[r] == KV_EMPTY ? 0x7FC07FC0u : w[r];
      const int slot = e < 16 ? g3 * 16 + e : 48 + (e - 16) * 3 + g3;
      if (q == 0 && u < 63)
        asm volatile("global_store_dwordx4 %0, %1, off sc1\n\ts_nop 1" :: "v"(ws + OFF_KVF + ((size_t)bid * 64 + slot) * 16), "v"(w) : "memory");
    }
    {
      const char* pqg = ws + OFF_PQ + ((size_t)(mt * 4) * 32 + wq * 8) * 640 + lane * 16;
#pragma unroll
      for (int tg = 0; tg < 4; ++tg)
#pragma unroll
        for (int i = 0; i < 5; ++i)
          __builtin_amdgcn_global_load_lds((const unsigned*)(pqg + tg * 20480 + i * 1024), (unsigned*)(reg + tg * 5120 + i * 1024), 16, 0, 0);
    }
    const char* wosrc = ws + OFF_WO + (size_t)(nt * 4 + wq) * 25600 + lane * 16;
    s16x8 w2[K2_DW][5];
    auto ldW = [&](int s) {
#pragma unroll
      for (int b = 0; b < 5; ++b) w2[s % K2_DW][b] = *(const s16x8*)(wosrc + (s * 5 + b) * 1024);
    };
#pragma unroll
    for (int s = 0; s < K2_DW; ++s) ldW(s);
    SCHED;
    const char* kvl = reg + 20480 + lane * 16;
    const char* kg = ws + OFF_KVF + (size_t)(wq * 8) * 1024;
    const unsigned kvm0 = __builtin_amdgcn_readfirstlane((unsigned)(size_t)(__attribute__((address_space(3))) char*)(reg + 20480));
    auto kvload = [&](int i0_, int i1_) {
#pragma unroll
      for (int i = 0; i < 8; ++i)
        if (i >= i0_ && i < i1_)
          asm volatile("s_mov_b32 m0, %0\n\ts_nop 0\n\tglobal_load_lds_dwordx4 %1, %2 sc1" :: "s"(kvm0 + i * 1024), "v"(lane * 16), "s"(kg + i * 1024) : "memory");
    };
    auto kvload_l2 = [&](int i0_, int i1_) {
#pragma unroll
      for (int i = 0; i < 8; ++i)
        if (i >= i0_ && i < i1_)
          asm volatile("s_mov_b32 m0, %0\n\ts_nop 0\n\tglobal_load_lds_dwordx4 %1, %2" :: "s"(kvm0 + i * 1024), "v"(lane * 16), "s"(kg + i * 1024) : "memory");
    };
    auto kvwait = [&](int i0_, int i1_) {
      while (true) {
        asm volatile("s_waitcnt vmcnt(0)" ::: "memory");
        bool bad = false;
#pragma unroll
        for (int i = 0; i < 8; ++i)
          if (i >= i0_ && i < i1_) {
            const u32x4 v = *(const __attribute__((address_space(3))) u32x4*)(kvl + i * 1024);
            bad = bad || v[0] == KV_EMPTY || v[1] == KV_EMPTY || v[2] == KV_EMPTY || v[3] == KV_EMPTY;
            if ((i & 3) == 3) __builtin_amdgcn_sched_barrier(0);
          }
        if (__builtin_amdgcn_ballot_w64(bad) == 0ull) break;
        __builtin_amdgcn_s_sleep(2);
        kvload(i0_, i1_);
      }
    };
    kvload(0, 2);
    kvwait(0, 2);
    __builtin_amdgcn_s_waitcnt(0x0F70);
    kvload_l2(2, 8);
    SCHED;
    STAMPW(4);
    const char* pql = reg + (g < 3 ? g : 0) * 256 + c * 16;
    const char* kv0 = reg + 20480 + (g < 3 ? lane : 63) * 16;
    const char* kv1 = reg + 20480 + ((g < 3 && (c & 3) < 2) ? ((c & 3) == 0 ? 48 + (c >> 2) * 3 + g : 60 + g) : 63) * 16;
    const f32x4 z4 = (f32x4){0.f, 0.f, 0.f, 0.f};
    unsigned ow[4][20];
    auto mma2 = [&](int s) {
#pragma unroll
      for (int a = 0; a < 4; ++a) {
        const u32x4 o4 = (u32x4){ow[a][4 * s], ow[a][4 * s + 1], ow[a][4 * s + 2], ow[a][4 * s + 3]};
        const s16x8 of = __builtin_bit_cast(s16x8, o4);
#pragma unroll
        for (int b = 0; b < 5; ++b) acc[a][b] = __builtin_amdgcn_mfma_f32_16x16x32_bf16(w2[s % K2_DW][b], of, acc[a][b], 0, 0, 0);
      }
      if (s + K2_DW < 5) ldW(s + K2_DW);
    };
#pragma unroll
    for (int m = 0; m < 4; ++m) {
      f32x4 ov[2][4]; float oe[2][4];
#pragma unroll
      for (int h2 = 0; h2 < 2; ++h2) {
        const int hh = 2 * m + h2;
        const s16x8 k0 = *(const s16x8*)(kv0 + hh * 1024), k1 = *(const s16x8*)(kv1 + hh * 1024);
#pragma unroll
        for (int tg = 0; tg < 4; ++tg) {
          u32x4 pq;
          if ((hh & 1) == 0) pq = *(const u32x4*)(pql + tg * 5120 + (5 * hh / 2) * 256);
          else {
            const u32x2 p0 = *(const u32x2*)(pql + tg * 5120 + ((5 * hh - 1) / 2) * 256 + 8), p1 = *(const u32x2*)(pql + tg * 5120 + ((5 * hh - 1) / 2) * 256 + 256);
            pq = (u32x4){p0[0], p0[1], p1[0], p1[1]};
          }
          u32x4 pk4;
          pk4[0] = g < 3 ? pq[0] : 0u; pk4[1] = g < 3 ? pq[1] : 0u;
          pk4[2] = g < 2 ? pq[2] : 0u; pk4[3] = g < 2 ? pq[3] : 0u;
          const s16x8 pf = __builtin_bit_cast(s16x8, pk4);
          const f32x4 d1 = __builtin_amdgcn_mfma_f32_16x16x32_bf16(k1, pf, z4, 0, 0, 0);
          const f32x4 d0 = __builtin_amdgcn_mfma_f32_16x16x32_bf16(k0, pf, z4, 0, 0, 0);
          const float rden = frcp(bfr(bfr(d1[1]) + 1e-6f));
          const f32x2 lo = bfr2(lo2(d0)) * rden, hi = bfr2(hi2(d0)) * rden;
          ov[h2][tg] = (f32x4){lo[0], lo[1], hi[0], hi[1]};
          oe[h2][tg] = bfr(d1[0]) * rden;
        }
      }
#pragma unroll
      for (int tg = 0; tg < 4; ++tg) {
        ow[tg][5 * m + 0] = cvt2(lo2(ov[0][tg]));
        ow[tg][5 * m + 1] = cvt2(hi2(ov[0][tg]));
        ow[tg][5 * m + 2] = cvt2((f32x2){oe[0][tg], ov[1][tg][0]});
        ow[tg][5 * m + 3] = cvt2((f32x2){ov[1][tg][1], ov[1][tg][2]});
        ow[tg][5 * m + 4] = cvt2((f32x2){ov[1][tg][3], oe[1][tg]});
      }
      if (m == 0) kvwait(2, 8);
      mma2(m);
      if (m == 3) mma2(4);
      SCHED;
      if (m < 2) STAMPW(5 + m);
    }
  }
  u32x2 xw[3];
#pragma unroll
  for (int i = 0; i < 3; ++i) xw[i] = *(const u32x2*)(x + (size_t)tok * D_ + nt * 80 + (i < nb ? b0 + i : b0) * 16 + 4 * g);
  __syncthreads();

  f32x4* red = (f32x4*)smem;
#pragma unroll
  for (int a = 0; a < 4; ++a)
#pragma unroll
    for (int b = 0; b < 5; ++b) red[(wave * 20 + a * 5 + b) * 64 + lane] = acc[a][b];
  __syncthreads();
#pragma unroll
  for (int i = 0; i < 3; ++i) {
    if (i < nb) {
      const int f = wq * 5 + b0 + i;
      f32x4 y1 = red[(0 * 20 + f) * 64 + lane], y2 = red[(4 * 20 + f) * 64 + lane];
#pragma unroll
      for (int sw = 1; sw < 4; ++sw) { y1 += red[(sw * 20 + f) * 64 + lane]; y2 += red[((4 + sw) * 20 + f) * 64 + lane]; }
      const int n = nt * 80 + (b0 + i) * 16 + 4 * g;
      u32x2 w;
      {
        const f32x2 y = bfr2(lo2(y1)), gs = bfr2(y * sigm2(y)), sv = bfr2(lo2(y2));
        w[0] = cvt2(unp2(xw[i][0]) + bfr2(sv * gs));
      }
      {
        const f32x2 y = bfr2(hi2(y1)), gs = bfr2(y * sigm2(y)), sv = bfr2(hi2(y2));
        w[1] = cvt2(unp2(xw[i][1]) + bfr2(sv * gs));
      }
      *(u32x2*)(out + (size_t)tok * D_ + n) = w;
    }
  }
  STAMP(2, 3);
}

extern "C" void kernel_launch(void* const* d_in, const int* in_sizes, int n_in,
                              void* d_out, int out_size, void* d_ws, size_t ws_size,
                              hipStream_t stream) {
  const u16* x = (const u16*)d_in[0];
  const u16* wqkv = (const u16*)d_in[1];
  const u16* wo = (const u16*)d_in[2];
  const u16* wg = (const u16*)d_in[4];
  const u16* wu = (const u16*)d_in[5];
  const u16* wd = (const u16*)d_in[6];
  char* ws = (char*)d_ws;
  u16* out = (u16*)d_out;

  hipFuncSetAttribute((const void*)k1_gemm, hipFuncAttributeMaxDynamicSharedMemorySize, K1_NS * K1_STAGE);
  hipFuncSetAttribute((const void*)k2_gemm, hipFuncAttributeMaxDynamicSharedMemorySize, K2_LDS);

  k0_prep<<<512, 256, 0, stream>>>(x, ws, wqkv, wg, wu);
  k1_gemm<<<256, 512, K1_NS * K1_STAGE, stream>>>(wqkv, wg, wu, wo, wd, ws);
  k2_gemm<<<256, 512, K2_LDS, stream>>>(x, ws, out);
}
```
